```python
import torch

def _softmax(x, axis=-1):
    return torch.softmax(x, dim=axis)
def _gelu(x):
    return torch.nn.functional.gelu(x, approximate="tanh")
def _silu(x):
    return torch.nn.functional.silu(x)
def _sigmoid(x):
    return torch.sigmoid(x)
def _layernorm(x, axis=-1, eps=1e-5):
    m = x.mean(dim=axis, keepdim=True)
    v = x.var(dim=axis, keepdim=True, unbiased=False)
    return (x - m) / torch.sqrt(v + eps)
def _scatter_add(tgt, idx, src, axis=0):
    return tgt.clone().index_add_(axis, idx, src)
def _segment_sum(src, seg, n):
    out = torch.zeros((n,) + src.shape[1:], dtype=src.dtype)
    return out.index_add_(0, seg, src)
def _pad(x, pw):
    flat = tuple(v for pair in reversed(pw) for v in pair)
    return torch.nn.functional.pad(x, flat)

kernel_name = 'sequential_preLN__mha_block__moe_arch_mod__prompt'

def setup_inputs():

    _w = (lambda a: (lambda t: t.to(torch.bfloat16) if t.is_floating_point() else t)(torch.as_tensor(a)))
    def _A_setup():
        B, S, D, H = 1, 2048, 512, 32
        return {
            "x": _w((lambda *s: torch.randn(*s, dtype=torch.bfloat16) * 0.05)(B, S, D)),
            "wqkv": _w((lambda *s: torch.randn(*s, dtype=torch.bfloat16) * 0.05)(D, 3 * D)),
            "wo": _w((lambda *s: torch.randn(*s, dtype=torch.bfloat16) * 0.05)(D, D)),
        }
    _A_in = _A_setup()

    def _B_setup():
        B, S, D, F, C = 1, 2048, 512, 1376, 8
        return {
            "x": _w((lambda *s: torch.randn(*s, dtype=torch.bfloat16) * 0.05)(B, S, D)),
            "w_r": _w((lambda *s: torch.randn(*s, dtype=torch.bfloat16) * 0.05)(D)),
            "w_gate": _w((lambda *s: torch.randn(*s, dtype=torch.bfloat16) * 0.05)(D, F)),
            "w_up": _w((lambda *s: torch.randn(*s, dtype=torch.bfloat16) * 0.05)(D, F)),
            "w_down": _w((lambda *s: torch.randn(*s, dtype=torch.bfloat16) * 0.05)(F, D)),
        }
    _B_in = _B_setup()

    return {
        "A_x": _A_in["x"],
        "A_wqkv": _A_in["wqkv"],
        "A_wo": _A_in["wo"],
        "B_x": _B_in["x"],
        "B_w_r": _B_in["w_r"],
        "B_w_gate": _B_in["w_gate"],
        "B_w_up": _B_in["w_up"],
        "B_w_down": _B_in["w_down"],
    }

def reference(A_x, A_wqkv, A_wo, B_x, B_w_r, B_w_gate, B_w_up, B_w_down):
    def _A_ref(x, wqkv, wo):
        B, S, D = x.shape
        H = 32
        Dh = D // H
        qkv = (lambda x, *d: x.permute(*d))((x @ wqkv).reshape(B, S, 3, H, Dh), 2, 0, 3, 1, 4)
        q, k, v = qkv[0], qkv[1], qkv[2]
        scores = torch.einsum("bhqd,bhkd->bhqk", q, k) * (Dh ** -0.5)
        p = _softmax(scores, axis=-1)
        o = torch.einsum("bhqk,bhkd->bhqd", p, v)
        return (lambda x, *d: x.permute(*d))(o, 0, 2, 1, 3).reshape(B, S, D) @ wo

    def _B_ref(x, w_r, w_gate, w_up, w_down):
        B, S, D = x.shape
        C = 8
        r = x @ w_r
        idx = torch.argsort(-r, axis=-1)[:, :C]
        idx_e = (lambda x, n, axis: torch.repeat_interleave(x, n, dim=axis))(idx.reshape(B, C, 1), D, axis=2)
        x_sel = (lambda x, i, axis: torch.take_along_dim(x, i, dim=axis))(x, idx_e, axis=1)
        g = _silu(x_sel @ w_gate)
        u = x_sel @ w_up
        y = (g * u) @ w_down
        r_sel = _sigmoid((lambda x, i, axis: torch.take_along_dim(x, i, dim=axis))(r, idx, axis=1))
        y = y * r_sel.reshape(B, C, 1)
        b_off = (torch.arange(B) * S).reshape(B, 1)
        flat_idx = (idx + b_off).reshape(B * C)
        zero = (x * 0.0).reshape(B * S, D)
        upd = _scatter_add(zero, flat_idx, y.reshape(B * C, D), axis=0)
        return x + upd.reshape(B, S, D)

    x = A_x
    h = x + _A_ref(_layernorm(x, axis=-1), A_wqkv, A_wo)
    h2 = h + _B_ref(_layernorm(h, axis=-1), B_w_r, B_w_gate, B_w_up, B_w_down)
    return h2
```

```cpp
#include <hip/hip_runtime.h>
#include <hip/hip_bf16.h>

typedef unsigned short u16;
typedef __bf16 bf16x2_t __attribute__((ext_vector_type(2)));
typedef __bf16 bf16x8_t __attribute__((ext_vector_type(8)));
typedef __bf16 bf16x16_t __attribute__((ext_vector_type(16)));
typedef short s16x4 __attribute__((ext_vector_type(4)));
typedef float f32x2 __attribute__((ext_vector_type(2)));
typedef float f32x4 __attribute__((ext_vector_type(4)));
typedef float f32x8 __attribute__((ext_vector_type(8)));
typedef float f32x16 __attribute__((ext_vector_type(16)));
typedef unsigned u32x2 __attribute__((ext_vector_type(2)));
typedef unsigned u32x4 __attribute__((ext_vector_type(4)));
#define LAS __attribute__((address_space(3)))
#define GAS __attribute__((address_space(1)))

constexpr int S_ = 2048, D_ = 512, F_ = 1376;
constexpr unsigned SENT = 0xFFFFFFFFu;
constexpr float LN_EPS = 1e-5f;
constexpr float QSCALE = 0.25f * 1.4426950408889634f;

constexpr size_t WS_QF = 0;
constexpr size_t WS_KF = 2u << 20;
constexpr size_t WS_VF = 4u << 20;
constexpr size_t WS_OF = 6u << 20;
constexpr size_t WS_HN = 8u << 20;
constexpr size_t WS_XS = 10u << 20;
constexpr size_t WS_ACT = WS_XS + (512u << 10);
constexpr size_t WS_KEY = WS_ACT + (32u << 10);

__device__ __forceinline__ f32x16 mfma32(u32x4 a, u32x4 b, f32x16 c) {
  return __builtin_amdgcn_mfma_f32_32x32x16_bf16(__builtin_bit_cast(bf16x8_t, a), __builtin_bit_cast(bf16x8_t, b), c, 0, 0, 0);
}
__device__ __forceinline__ f32x4 mfma16(u32x4 a, u32x4 b, f32x4 c) {
  return __builtin_amdgcn_mfma_f32_16x16x32_bf16(__builtin_bit_cast(bf16x8_t, a), __builtin_bit_cast(bf16x8_t, b), c, 0, 0, 0);
}
__device__ __forceinline__ f32x16 smfmac32(u32x4 a, f32x8 b, f32x16 c, int idx) {
  return __builtin_amdgcn_smfmac_f32_32x32x32_bf16(__builtin_bit_cast(bf16x8_t, a), __builtin_bit_cast(bf16x16_t, b), c, idx, 0, 0);
}
__device__ __forceinline__ float dot2bf(unsigned a, unsigned b, float c) {
  return __builtin_amdgcn_fdot2_f32_bf16(__builtin_bit_cast(bf16x2_t, a), __builtin_bit_cast(bf16x2_t, b), c, false);
}
__device__ __forceinline__ unsigned pkbf(float lo, float hi) {
  bf16x2_t v; v[0] = (__bf16)lo; v[1] = (__bf16)hi; return __builtin_bit_cast(unsigned, v);
}
__device__ __forceinline__ float bflo(unsigned u) { return __uint_as_float(u << 16); }
__device__ __forceinline__ float bfhi(unsigned u) { return __uint_as_float(u & 0xFFFF0000u); }
__device__ __forceinline__ float rbf(float f) { return (float)(__bf16)f; }
__device__ __forceinline__ float swap32_sum(float v) {
  auto r = __builtin_amdgcn_permlane32_swap(__float_as_uint(v), __float_as_uint(v), false, false);
  return __uint_as_float(r[0]) + __uint_as_float(r[1]);
}
__device__ __forceinline__ float swap32_max(float v) {
  auto r = __builtin_amdgcn_permlane32_swap(__float_as_uint(v), __float_as_uint(v), false, false);
  return __builtin_fmaxf(__uint_as_float(r[0]), __uint_as_float(r[1]));
}
__device__ __forceinline__ void glds16(const void* sbase, unsigned voff, unsigned lds_base) {
  unsigned sv;
  asm volatile("s_mov_b32 %0, m0\n\ts_mov_b32 m0, %3\n\ts_nop 0\n\tglobal_load_lds_dwordx4 %1, %2\n\ts_mov_b32 m0, %0"
               : "=&s"(sv) : "v"(voff), "s"(sbase), "s"(lds_base) : "memory");
}
#define WAIT_BAR(N) asm volatile("s_waitcnt vmcnt(" #N ") lgkmcnt(0)\n\ts_barrier" ::: "memory")
#define WAIT_VM(N) asm volatile("s_waitcnt vmcnt(" #N ")" ::: "memory")
__device__ __forceinline__ u32x4 ldsr128(const LAS char* p) { return *(const LAS u32x4*)p; }
__device__ __forceinline__ u32x2 ldstr(const LAS char* p) {
  return __builtin_bit_cast(u32x2, __builtin_amdgcn_ds_read_tr16_b64_v4i16((LAS s16x4*)p));
}
__device__ __forceinline__ int swapb23(int v) { return (v & ~12) | ((v & 4) << 1) | ((v & 8) >> 1); }
__device__ __forceinline__ unsigned nosent(unsigned v) { return v == SENT ? 0x7FC00000u : v; }

constexpr int K1_SLOT = 16384 + 12288;
constexpr int K1_LDS = 5 * K1_SLOT;

template <int NV>
__device__ __forceinline__ void k1_body(const u16* __restrict__ x, const u16* __restrict__ w, char* __restrict__ ws, LAS char* lds,
                                        int rb, int cb, int rot) {
  const int tid = threadIdx.x, lane = tid & 63, t = lane & 31, h = lane >> 5;
  const int wid = __builtin_amdgcn_readfirstlane(tid >> 6);
  const unsigned lds0 = (unsigned)(uintptr_t)lds;
  unsigned xoff[4], woff[3];
#pragma unroll
  for (int p = 0; p < 4; ++p) {
    const int row = p * 8 + (lane >> 3), slot = lane & 7;
    xoff[p] = (unsigned)((rb * 128 + wid * 32 + row) * 1024 + ((slot ^ ((row >> 1) & 7)) * 16));
  }
#pragma unroll
  for (int p = 0; p < 3; ++p) {
    const int idx = (wid * 3 + p) * 64 + lane, k = idx / 12, c = idx - k * 12;
    woff[p] = (unsigned)(k * 3072 + cb * 192 + c * 16);
  }
  auto stage = [&](int c, int slot) {
    const int kc = (c + rot) & 7;
    const char* xs = (const char*)x + kc * 128;
    const char* wsrc = (const char*)w + kc * (64 * 3072);
    const unsigned base = lds0 + slot * K1_SLOT;
#pragma unroll
    for (int p = 0; p < 4; ++p) glds16(xs, xoff[p], base + wid * 4096 + p * 1024);
#pragma unroll
    for (int p = 0; p < 3; ++p) glds16(wsrc, woff[p], base + 16384 + (wid * 3 + p) * 1024);
  };
#pragma unroll
  for (int c = 0; c < 5; ++c) stage(c, c);

  int xr[4];
#pragma unroll
  for (int u = 0; u < 4; ++u) xr[u] = wid * 4096 + t * 128 + (((2 * u + h) ^ ((t >> 1) & 7)) * 16);
  const int g = lane >> 4, q = (lane >> 2) & 3, p4 = lane & 3;
  const int wrd = 16384 + (8 * h + q) * 192 + 32 * (g & 1) + 8 * p4;

  f32x16 acc[3];
#pragma unroll
  for (int j = 0; j < 3; ++j) acc[j] = f32x16{};
  float s1 = 0.f, s2 = 0.f, cs[3] = {0.f, 0.f, 0.f};
  const unsigned ONES = 0x3F803F80u;

#pragma unroll
  for (int c = 0; c < 8; ++c) {
    if (c == 0) WAIT_BAR(28);
    else if (c <= 4) WAIT_BAR(21);
    else if (c == 5) WAIT_BAR(14);
    else if (c == 6) WAIT_BAR(7);
    else WAIT_BAR(0);
    if (c >= 1 && c + 4 < 8) stage(c + 4, (c + 4) % 5);
    const LAS char* sb = lds + (c % 5) * K1_SLOT;
#pragma unroll
    for (int u = 0; u < 4; ++u) {
      const u32x4 xf = ldsr128(sb + xr[u]);
      u32x4 wf[3];
#pragma unroll
      for (int j = 0; j < 3; ++j) {
        const u32x2 lo = ldstr(sb + wrd + u * 3072 + j * 64), hi = ldstr(sb + wrd + u * 3072 + 768 + j * 64);
        wf[j] = u32x4{lo[0], lo[1], hi[0], hi[1]};
      }
#pragma unroll
      for (int j = 0; j < 3; ++j) {
        if (j >= 3 - NV) acc[j] = mfma32(xf, wf[j], acc[j]);
        else acc[j] = mfma32(wf[j], xf, acc[j]);
      }
#pragma unroll
      for (int e = 0; e < 4; ++e) { s1 = dot2bf(xf[e], ONES, s1); s2 = dot2bf(xf[e], xf[e], s2); }
#pragma unroll
      for (int j = 0; j < 3; ++j)
#pragma unroll
        for (int e = 0; e < 4; ++e) cs[j] = dot2bf(wf[j][e], ONES, cs[j]);
    }
  }
  s1 = swap32_sum(s1); s2 = swap32_sum(s2);
  const float mean = s1 * (1.f / D_);
  const float var = __builtin_fmaxf(s2 * (1.f / D_) - mean * mean, 0.f);
  const float rstd = __builtin_amdgcn_rsqf(var + LN_EPS);
#pragma unroll
  for (int j = 0; j < 3; ++j) cs[j] = swap32_sum(cs[j]);
  LAS float* scr = (LAS float*)(lds + wid * 4096);
  if (h == 0) {
    scr[t] = rstd; scr[32 + t] = mean * rstd;
#pragma unroll
    for (int j = 0; j < 3; ++j) scr[64 + 32 * j + t] = cs[j];
  }
  const int tile = rb * 4 + wid;
#pragma unroll
  for (int j = 0; j < 3; ++j) {
    const int gt = cb * 3 + j;
    float v[16];
    if (j >= 3 - NV) {
#pragma unroll
      for (int gq = 0; gq < 4; ++gq) {
        const f32x4 a = *(const LAS f32x4*)(scr + 8 * gq + 4 * h), b = *(const LAS f32x4*)(scr + 32 + 8 * gq + 4 * h);
#pragma unroll
        for (int e = 0; e < 4; ++e) v[4 * gq + e] = acc[j][4 * gq + e] * a[e] - b[e] * cs[j];
      }
      const int head = (gt - 32) * 2 + (t >> 4);
      char* dst = ws + WS_VF + (size_t)(head * 64 + tile) * 1024 + h * 256 + swapb23(t & 15) * 16;
#pragma unroll
      for (int s = 0; s < 2; ++s)
        *(u32x4*)(dst + s * 512) = u32x4{pkbf(v[8 * s], v[8 * s + 1]), pkbf(v[8 * s + 2], v[8 * s + 3]), pkbf(v[8 * s + 4], v[8 * s + 5]), pkbf(v[8 * s + 6], v[8 * s + 7])};
    } else {
      const bool isq = gt < 16;
      const float sc = isq ? QSCALE : 1.f;
      const float a = rstd * sc, b = mean * rstd * sc;
#pragma unroll
      for (int gq = 0; gq < 4; ++gq) {
        const f32x4 c4 = *(const LAS f32x4*)(scr + 64 + 32 * j + 8 * gq + 4 * h);
#pragma unroll
        for (int e = 0; e < 4; ++e) v[4 * gq + e] = acc[j][4 * gq + e] * a - b * c4[e];
      }
      const int head = (gt & 15) * 2;
      char* dst = ws + (isq ? WS_QF : WS_KF) + (size_t)head * 65536 + tile * 1024 + (isq ? t : swapb23(t)) * 32 + h * 16;
#pragma unroll
      for (int s = 0; s < 2; ++s)
        *(u32x4*)(dst + s * 65536) = u32x4{pkbf(v[8 * s], v[8 * s + 1]), pkbf(v[8 * s + 2], v[8 * s + 3]), pkbf(v[8 * s + 4], v[8 * s + 5]), pkbf(v[8 * s + 6], v[8 * s + 7])};
    }
  }
}

__global__ __launch_bounds__(256, 1) void k1_qkv(const u16* __restrict__ x, const u16* __restrict__ w, char* __restrict__ ws) {
  extern __shared__ __attribute__((aligned(16))) char smem[];
  LAS char* lds = (LAS char*)smem;
  const int b = blockIdx.x, g = b & 7, i = b >> 3;
  const int rb = 4 * (g & 3) + (i & 3), cb = 8 * (g >> 2) + (i >> 2);
  const int rot = ((i >> 2) + 2 * (i & 3)) & 7;
  if (cb < 10) k1_body<0>(x, w, ws, lds, rb, cb, rot);
  else if (cb == 10) k1_body<1>(x, w, ws, lds, rb, cb, rot);
  else k1_body<3>(x, w, ws, lds, rb, cb, rot);
}

constexpr int K2_K = 0, K2_V = 65536, K2_ONE = 131072, K2_Q = K2_ONE + 4096, K2_LDS = K2_Q + 8192;

__device__ __forceinline__ float rowmax16(const f32x16& s) {
  float a = __builtin_fmaxf(__builtin_fmaxf(s[0], s[1]), s[2]), b = __builtin_fmaxf(__builtin_fmaxf(s[3], s[4]), s[5]);
  a = __builtin_fmaxf(__builtin_fmaxf(a, s[6]), s[7]); b = __builtin_fmaxf(__builtin_fmaxf(b, s[8]), s[9]);
  a = __builtin_fmaxf(__builtin_fmaxf(a, s[10]), s[11]); b = __builtin_fmaxf(__builtin_fmaxf(b, s[12]), s[13]);
  a = __builtin_fmaxf(__builtin_fmaxf(a, s[14]), s[15]);
  return swap32_max(__builtin_fmaxf(a, b));
}
__device__ __forceinline__ f32x8 lo8(const f32x16& s) { return __builtin_shufflevector(s, s, 0, 1, 2, 3, 4, 5, 6, 7); }
__device__ __forceinline__ f32x8 hi8(const f32x16& s) { return __builtin_shufflevector(s, s, 8, 9, 10, 11, 12, 13, 14, 15); }

__global__ __launch_bounds__(512, 2) void k2_attn(char* __restrict__ ws) {
  extern __shared__ __attribute__((aligned(16))) char smem[];
  LAS char* lds = (LAS char*)smem;
  const unsigned lds0 = (unsigned)(uintptr_t)lds;
  const int tid = threadIdx.x, lane = tid & 63, t = lane & 31, h = lane >> 5;
  const int wid = __builtin_amdgcn_readfirstlane(tid >> 6);
  const int b = blockIdx.x, head = (b & 7) * 4 + ((b >> 3) & 3), qb = b >> 5;
  glds16(ws + WS_QF + (size_t)head * 65536 + (qb * 8 + wid) * 1024, lane * 16, lds0 + K2_Q + wid * 1024);
  {
    const char* src = ws + ((wid & 1) ? WS_VF : WS_KF) + (size_t)head * 65536;
    const unsigned dst = lds0 + ((wid & 1) ? K2_V : K2_K) + (wid >> 1) * 1024;
#pragma unroll
    for (int i = 0; i < 16; ++i) {
      const int gtile = (4 * i + (wid >> 1) + 8 * qb) & 63;
      glds16(src + gtile * 1024, lane * 16, dst + i * 4096);
    }
  }
  if (tid < 128) *(u32x4*)(ws + WS_XS + (size_t)b * 2048 + tid * 16) = u32x4{SENT, SENT, SENT, SENT};
  else if (b < 11 && tid < 256) { const int e = b * 128 + tid - 128; if (e < 1376) *(u32x4*)(ws + WS_ACT + e * 16) = u32x4{SENT, SENT, SENT, SENT}; }
  if (tid < 8) *(LAS u32x4*)(lds + K2_ONE + tid * 512) = u32x4{0x3F803F80u, 0x3F803F80u, 0x3F803F80u, 0x3F803F80u};

  const LAS char* kp = lds + K2_K + t * 32 + h * 16;
  const bool onel = (lane & 16) != 0;
  const LAS char* vp = lds + (onel ? K2_ONE : K2_V + h * 256 + (lane & 15) * 16);
  const int vstep = onel ? 0 : 4096;
  const int IDX = 0xDDDDDDDD;

  asm volatile("s_waitcnt vmcnt(15) lgkmcnt(0)\n\ts_barrier" ::: "memory");
  const u32x4 qf = ldsr128(lds + K2_Q + wid * 1024 + t * 32 + h * 16);
  f32x16 negm;
  {
    const f32x16 s0 = mfma32(ldsr128(kp), qf, f32x16{});
    const float m = -rowmax16(s0);
#pragma unroll
    for (int r = 0; r < 16; ++r) negm[r] = m;
  }
  f32x16 o = f32x16{};
  auto group = [&](const LAS char* kq, const LAS char* vq) {
#pragma unroll
    for (int u = 0; u < 4; ++u) {
      f32x16 s = mfma32(ldsr128(kq + u * 1024), qf, negm);
      const u32x4 v0 = ldsr128(vq + u * 1024), v1 = ldsr128(vq + u * 1024 + 512);
#pragma unroll
      for (int r = 0; r < 16; ++r) s[r] = __builtin_amdgcn_exp2f(s[r]);
      o = smfmac32(v0, lo8(s), o, IDX);
      o = smfmac32(v1, hi8(s), o, IDX);
    }
  };
  group(kp, vp);
  WAIT_BAR(12);
  {
    const LAS char* kq = kp + 4096; const LAS char* vq = vp + vstep;
#pragma unroll 1
    for (int i = 1; i < 4; ++i) { group(kq, vq); kq += 4096; vq += vstep; }
    WAIT_BAR(0);
#pragma unroll 1
    for (int i = 4; i < 16; ++i) { group(kq, vq); kq += 4096; vq += vstep; }
  }
  float inv = __builtin_amdgcn_rcpf(o[8]);
  bool bad = !(o[8] < 1.8446744e19f);
#pragma unroll
  for (int r = 0; r < 8; ++r) bad |= !(__builtin_fabsf(o[r]) < 3.0e38f);
  if (__builtin_expect(__any(bad), 0)) {
    float m = -INFINITY;
    o = f32x16{};
    const LAS char* kq = kp; const LAS char* vq = vp;
    const int vs1 = onel ? 0 : 1024;
#pragma unroll 1
    for (int i = 0; i < 64; ++i) {
      f32x16 s = mfma32(ldsr128(kq), qf, f32x16{});
      const u32x4 v0 = ldsr128(vq), v1 = ldsr128(vq + 512);
      const float mn = __builtin_fmaxf(m, rowmax16(s));
      const float al = __builtin_amdgcn_exp2f(m - mn);
      m = mn;
#pragma unroll
      for (int r = 0; r < 16; ++r) { o[r] *= al; s[r] = __builtin_amdgcn_exp2f(s[r] - mn); }
      o = smfmac32(v0, lo8(s), o, IDX);
      o = smfmac32(v1, hi8(s), o, IDX);
      kq += 1024; vq += vs1;
    }
    inv = __builtin_amdgcn_rcpf(o[8]);
  }
  *(u32x4*)(ws + WS_OF + (size_t)head * 65536 + (qb * 8 + wid) * 1024 + t * 32 + h * 16) =
      u32x4{pkbf(o[0] * inv, o[1] * inv), pkbf(o[2] * inv, o[3] * inv), pkbf(o[4] * inv, o[5] * inv), pkbf(o[6] * inv, o[7] * inv)};
}

constexpr int K3_O = 0, K3_W = 65536, K3_LDS = 131072;
constexpr unsigned SPIN_CAP = 1u << 16;

__global__ __launch_bounds__(256, 1) void k3_oproj(const u16* __restrict__ x, const u16* __restrict__ wo, const u16* __restrict__ wr,
                                                    char* __restrict__ ws, u16* __restrict__ out) {
  extern __shared__ __attribute__((aligned(16))) char smem[];
  LAS char* lds = (LAS char*)smem;
  const unsigned lds0 = (unsigned)(uintptr_t)lds;
  const int tid = threadIdx.x, lane = tid & 63, t = lane & 31, h = lane >> 5;
  const int wid = __builtin_amdgcn_readfirstlane(tid >> 6);
  const int b = blockIdx.x, i = b >> 3, pm = (b & 7) * 4 + (i >> 3), cb = i & 7;
  const int mh = wid & 1, a = wid >> 1;
  const int rot = (cb + 2 * (i >> 3)) & 7;
  {
    const int krow = lane >> 3, slot = lane & 7;
    const unsigned woff = (unsigned)((wid * 16 + krow) * 1024 + cb * 128 + ((slot ^ (((krow >> 1) & 1) << 1)) * 16));
#pragma unroll
    for (int c = 0; c < 8; ++c) {
      const int kc = (c + rot) & 7;
      const char* osrc = ws + WS_OF + (size_t)(kc * 4 + wid) * 65536 + pm * 2048;
      glds16(osrc, lane * 16, lds0 + K3_O + c * 8192 + wid * 2048);
      glds16(osrc + 1024, lane * 16, lds0 + K3_O + c * 8192 + wid * 2048 + 1024);
      const char* wsrc = (const char*)wo + kc * 65536;
      glds16(wsrc, woff, lds0 + K3_W + c * 8192 + wid * 2048);
      glds16(wsrc + 8192, woff, lds0 + K3_W + c * 8192 + wid * 2048 + 1024);
    }
  }
  const int row = pm * 64 + mh * 32 + t, col = cb * 64 + a * 32 + h * 16;
  const u32x4 x0 = *(const u32x4*)(x + row * D_ + col), x1 = *(const u32x4*)(x + row * D_ + col + 8);
  const u32x4 w0 = *(const u32x4*)(wr + col), w1 = *(const u32x4*)(wr + col + 8);

  const int g = lane >> 4, q = (lane >> 2) & 3, p4 = lane & 3;
  const int wbyte = (32 * a + 16 * (p4 & 1) + 4 * (2 * (g & 1) + (p4 >> 1))) * 2;
  const int wrd = K3_W + (8 * h + q) * 128 + (wbyte ^ (((q >> 1) & 1) << 5));
  const int ord = K3_O + mh * 1024 + t * 32 + h * 16;
  f32x16 acc = f32x16{};
#pragma unroll
  for (int c = 0; c < 8; ++c) {
    if (c == 0) WAIT_BAR(28); else if (c == 1) WAIT_BAR(24); else if (c == 2) WAIT_BAR(20); else if (c == 3) WAIT_BAR(16);
    else if (c == 4) WAIT_BAR(12); else if (c == 5) WAIT_BAR(8); else if (c == 6) WAIT_BAR(4); else WAIT_BAR(0);
#pragma unroll
    for (int u = 0; u < 4; ++u) {
      const u32x4 of = ldsr128(lds + ord + c * 8192 + u * 2048);
      const u32x2 lo = ldstr(lds + wrd + c * 8192 + u * 2048), hi = ldstr(lds + wrd + c * 8192 + u * 2048 + 512);
      acc = mfma32(u32x4{lo[0], lo[1], hi[0], hi[1]}, of, acc);
    }
  }
  float hv[16];
  float s1 = 0.f, s2 = 0.f, sd = 0.f, sw = 0.f;
#pragma unroll
  for (int e = 0; e < 8; ++e) {
    const unsigned xe = e < 4 ? x0[e] : x1[e - 4], we = e < 4 ? w0[e] : w1[e - 4];
    hv[2 * e] = rbf(bflo(xe) + rbf(acc[2 * e])); hv[2 * e + 1] = rbf(bfhi(xe) + rbf(acc[2 * e + 1]));
    s1 += hv[2 * e] + hv[2 * e + 1];
    s2 = fmaf(hv[2 * e], hv[2 * e], s2); s2 = fmaf(hv[2 * e + 1], hv[2 * e + 1], s2);
    sd = fmaf(hv[2 * e], bflo(we), sd); sd = fmaf(hv[2 * e + 1], bfhi(we), sd);
    sw += bflo(we) + bfhi(we);
  }
  s1 = swap32_sum(s1); s2 = swap32_sum(s2); sd = swap32_sum(sd); sw = swap32_sum(sw);
  const __amdgpu_buffer_rsrc_t xs = __builtin_amdgcn_make_buffer_rsrc(ws + WS_XS + (size_t)pm * 16384, 0, 16384, 0x00020000);
  if (h == 0) {
    const u32x4 pv = u32x4{nosent(__float_as_uint(s1)), nosent(__float_as_uint(s2)), nosent(__float_as_uint(sd)), nosent(__float_as_uint(sw))};
    __builtin_amdgcn_raw_buffer_store_b128(pv, xs, (cb * 2 + a) * 1024 + (mh * 32 + t) * 16, 0, 16);
  }
  {
    u32x4 pv[8];
    for (unsigned spins = 0;; ++spins) {
      bool ok = true;
      asm volatile("" ::: "memory");
#pragma unroll
      for (int e = 0; e < 8; ++e) pv[e] = __builtin_amdgcn_raw_buffer_load_b128(xs, (h * 8 + e) * 1024 + (mh * 32 + t) * 16, 0, 16);
#pragma unroll
      for (int e = 0; e < 8; ++e) ok &= (pv[e][0] != SENT) & (pv[e][1] != SENT) & (pv[e][2] != SENT) & (pv[e][3] != SENT);
      if (__all(ok) || spins > SPIN_CAP) break;
      __builtin_amdgcn_s_sleep(1);
    }
    s1 = s2 = sd = sw = 0.f;
#pragma unroll
    for (int e = 0; e < 8; ++e) { s1 += __uint_as_float(pv[e][0]); s2 += __uint_as_float(pv[e][1]); sd += __uint_as_float(pv[e][2]); sw += __uint_as_float(pv[e][3]); }
    s1 = swap32_sum(s1); s2 = swap32_sum(s2); sd = swap32_sum(sd); sw = swap32_sum(sw);
  }
  const float mean = s1 * (1.f / D_);
  const float var = __builtin_fmaxf(s2 * (1.f / D_) - mean * mean, 0.f);
  const float rstd = __builtin_amdgcn_rsqf(var + LN_EPS);
  const float mr = mean * rstd;
  unsigned hp[8], op[8];
#pragma unroll
  for (int e = 0; e < 8; ++e) {
    hp[e] = pkbf(hv[2 * e] * rstd - mr, hv[2 * e + 1] * rstd - mr);
    op[e] = pkbf(hv[2 * e] + bflo(hp[e]), hv[2 * e + 1] + bfhi(hp[e]));
  }
  u16* hn = (u16*)(ws + WS_HN);
  *(u32x4*)(hn + row * D_ + col) = u32x4{hp[0], hp[1], hp[2], hp[3]};
  *(u32x4*)(hn + row * D_ + col + 8) = u32x4{hp[4], hp[5], hp[6], hp[7]};
  *(u32x4*)(out + row * D_ + col) = u32x4{op[0], op[1], op[2], op[3]};
  *(u32x4*)(out + row * D_ + col + 8) = u32x4{op[4], op[5], op[6], op[7]};
  if (cb == 0 && a == 0 && h == 0) {
    const float r = rstd * sd - mr * sw;
    const unsigned u = __float_as_uint(r);
    const unsigned ordv = (u & 0x80000000u) ? ~u : (u | 0x80000000u);
    ((unsigned*)(ws + WS_KEY))[row] = (ordv & 0xFFFFF800u) | (unsigned)(2047 - row);
  }
}

constexpr int K4_G = 0, K4_U = 16384, K4_D = 32768, K4_CAND = K4_D + 44032, K4_MISC = K4_CAND + 8208, K4_RED = K4_MISC + 256,
              K4_LDS = K4_RED + 8192;

__global__ __launch_bounds__(256, 1) void k4_ffn(const u16* __restrict__ wg, const u16* __restrict__ wu, const u16* __restrict__ wd,
                                                  char* __restrict__ ws, u16* __restrict__ out) {
  extern __shared__ __attribute__((aligned(16))) char smem[];
  LAS char* lds = (LAS char*)smem;
  const unsigned lds0 = (unsigned)(uintptr_t)lds;
  const int tid = threadIdx.x, lane = tid & 63;
  const int wid = __builtin_amdgcn_readfirstlane(tid >> 6);
  const int nb = blockIdx.x;
  const u32x4 ka = *(const u32x4*)(ws + WS_KEY + tid * 32), kb = *(const u32x4*)(ws + WS_KEY + tid * 32 + 16);
  {
    const unsigned goff = (unsigned)((wid * 128 + (lane >> 1)) * (F_ * 2) + nb * 32 + (lane & 1) * 16);
#pragma unroll
    for (int p = 0; p < 4; ++p) {
      glds16((const char*)wg + p * 32 * F_ * 2, goff, lds0 + K4_G + wid * 4096 + p * 1024);
      glds16((const char*)wu + p * 32 * F_ * 2, goff, lds0 + K4_U + wid * 4096 + p * 1024);
    }
    if (nb < 32) {
      const unsigned doff = (unsigned)((wid * 32 + (lane >> 1)) * 1024 + nb * 32 + (lane & 1) * 16);
#pragma unroll
      for (int p = 0; p < 11; ++p)
        if (p * 4 + wid < 43) glds16((const char*)wd + p * 131072, doff, lds0 + K4_D + (p * 4 + wid) * 1024);
    }
  }
  LAS unsigned* cand = (LAS unsigned*)(lds + K4_CAND);
  LAS unsigned* misc = (LAS unsigned*)(lds + K4_MISC);
  *(LAS u32x4*)(cand + tid * 8) = u32x4{0, 0, 0, 0}; *(LAS u32x4*)(cand + tid * 8 + 4) = u32x4{0, 0, 0, 0};
  if (tid == 0) { *(LAS u32x4*)(cand + 2048) = u32x4{0, 0, 0, 0}; misc[8] = 0; }
  unsigned k[8] = {ka[0], ka[1], ka[2], ka[3], kb[0], kb[1], kb[2], kb[3]};
  unsigned m = 0;
#pragma unroll
  for (int e = 0; e < 8; ++e) m = max(m, k[e]);
#pragma unroll
  for (int d = 1; d < 32; d <<= 1) m = max(m, (unsigned)__shfl_xor((int)m, d));
  if ((lane & 31) == 0) misc[wid * 2 + (lane >> 5)] = m;
  asm volatile("s_waitcnt lgkmcnt(0)\n\ts_barrier" ::: "memory");
  unsigned T;
  {
    const u32x4 g0 = *(LAS u32x4*)misc, g1 = *(LAS u32x4*)(misc + 4);
    T = min(min(min(g0[0], g0[1]), min(g0[2], g0[3])), min(min(g1[0], g1[1]), min(g1[2], g1[3])));
  }
  unsigned cnt = 0;
#pragma unroll
  for (int e = 0; e < 8; ++e) cnt += k[e] >= T;
  if (cnt) {
    unsigned pos = __hip_atomic_fetch_add(misc + 8, cnt, __ATOMIC_RELAXED, __HIP_MEMORY_SCOPE_WORKGROUP);
#pragma unroll
    for (int e = 0; e < 8; ++e) if (k[e] >= T) cand[pos++] = k[e];
  }
  asm volatile("s_waitcnt lgkmcnt(0)\n\ts_barrier" ::: "memory");
  const int n = __builtin_amdgcn_readfirstlane(misc[8]);
  LAS unsigned* sel = misc + 16 + wid * 8;
  for (int i0 = 0; i0 < n; i0 += 64) {
    const unsigned mine = cand[min(i0 + lane, 2048)];
    unsigned rank = 0;
    for (int j = 0; j < n; j += 4) {
      const u32x4 c4 = *(LAS u32x4*)(cand + j);
      rank += (c4[0] > mine) + (c4[1] > mine) + (c4[2] > mine) + (c4[3] > mine);
    }
    if (i0 + lane < n && rank < 8) sel[rank] = mine;
  }
  const int tok = lane & 15, g = lane >> 4;
  const unsigned key = sel[tok & 7];
  const int trow = 2047 - (int)(key & 2047u);
  u32x4 xb[4];
  {
    const u16* hn = (const u16*)(ws + WS_HN) + trow * D_ + wid * 128 + g * 8;
#pragma unroll
    for (int u = 0; u < 4; ++u) xb[u] = *(const u32x4*)(hn + u * 32);
  }
  u32x2 o0 = u32x2{0, 0};
  if (nb < 32 && wid == 0) o0 = *(const u32x2*)(out + trow * D_ + nb * 16 + g * 4);
  WAIT_VM(0);
  const int q = (lane >> 2) & 3, p4 = lane & 3;
  const int trd = (8 * g + q) * 32 + 8 * p4;
  f32x4 ag = f32x4{}, au = f32x4{};
#pragma unroll
  for (int u = 0; u < 4; ++u) {
    const LAS char* pg = lds + K4_G + wid * 4096 + u * 1024 + trd;
    const LAS char* pu = lds + K4_U + wid * 4096 + u * 1024 + trd;
    const u32x2 g0 = ldstr(pg), g1 = ldstr(pg + 128), u0 = ldstr(pu), u1 = ldstr(pu + 128);
    ag = mfma16(u32x4{g0[0], g0[1], g1[0], g1[1]}, xb[u], ag);
    au = mfma16(u32x4{u0[0], u0[1], u1[0], u1[1]}, xb[u], au);
  }
  LAS f32x4* red = (LAS f32x4*)(lds + K4_RED);
  red[(wid * 2) * 64 + lane] = ag; red[(wid * 2 + 1) * 64 + lane] = au;
  asm volatile("s_waitcnt lgkmcnt(0)\n\ts_barrier" ::: "memory");
  const __amdgpu_buffer_rsrc_t act = __builtin_amdgcn_make_buffer_rsrc(ws + WS_ACT, 0, 22016, 0x00020000);
  if (wid == 0) {
#pragma unroll
    for (int w2 = 1; w2 < 4; ++w2) { ag += red[(w2 * 2) * 64 + lane]; au += red[(w2 * 2 + 1) * 64 + lane]; }
    float av[4];
#pragma unroll
    for (int r = 0; r < 4; ++r) {
      const float gg = rbf(ag[r]);
      const float sl = rbf(gg * __builtin_amdgcn_rcpf(1.f + __expf(-gg)));
      av[r] = sl * rbf(au[r]);
    }
    if (tok < 8) {
      const u32x2 pv = u32x2{nosent(pkbf(av[0], av[1])), nosent(pkbf(av[2], av[3]))};
      __builtin_amdgcn_raw_buffer_store_b64(pv, act, (nb >> 1) * 512 + tok * 64 + (nb & 1) * 32 + g * 8, 0, 16);
    }
  }
  if (nb >= 32) return;
  u32x4 ab[11];
  for (unsigned spins = 0;; ++spins) {
    bool ok = true;
    asm volatile("" ::: "memory");
#pragma unroll
    for (int p = 0; p < 11; ++p) {
      ab[p] = __builtin_amdgcn_raw_buffer_load_b128(act, (p * 4 + wid) * 512 + (tok & 7) * 64 + g * 16, 0, 16);
    }
#pragma unroll
    for (int p = 0; p < 11; ++p) ok &= (ab[p][0] != SENT) & (ab[p][1] != SENT) & (ab[p][2] != SENT) & (ab[p][3] != SENT);
    if (__all(ok) || spins > SPIN_CAP) break;
    __builtin_amdgcn_s_sleep(1);
  }
  f32x4 ay = f32x4{};
#pragma unroll
  for (int p = 0; p < 11; ++p) {
    if (p * 4 + wid < 43) {
      const LAS char* pd = lds + K4_D + (p * 4 + wid) * 1024 + trd;
      const u32x2 d0 = ldstr(pd), d1 = ldstr(pd + 128);
      ay = mfma16(u32x4{d0[0], d0[1], d1[0], d1[1]}, ab[p], ay);
    }
  }
  asm volatile("s_waitcnt lgkmcnt(0)\n\ts_barrier" ::: "memory");
  red[wid * 64 + lane] = ay;
  asm volatile("s_waitcnt lgkmcnt(0)\n\ts_barrier" ::: "memory");
  if (wid == 0 && tok < 8) {
#pragma unroll
    for (int w2 = 1; w2 < 4; ++w2) ay += red[w2 * 64 + lane];
    const unsigned ov = key & 0xFFFFF800u;
    const float r = rbf(__uint_as_float((ov & 0x80000000u) ? (ov & 0x7FFFFFFFu) : ~(ov | 0x7FFu)));
    const float sg = rbf(__builtin_amdgcn_rcpf(1.f + __expf(-r)));
    const float y0 = rbf(rbf(ay[0]) * sg), y1 = rbf(rbf(ay[1]) * sg), y2 = rbf(rbf(ay[2]) * sg), y3 = rbf(rbf(ay[3]) * sg);
    *(u32x2*)(out + trow * D_ + nb * 16 + g * 4) = u32x2{pkbf(bflo(o0[0]) + y0, bfhi(o0[0]) + y1), pkbf(bflo(o0[1]) + y2, bfhi(o0[1]) + y3)};
  }
}

extern "C" void kernel_launch(void* const* d_in, const int* in_sizes, int n_in, void* d_out, int out_size, void* d_ws, size_t ws_size,
                              hipStream_t stream) {
  const u16* x = (const u16*)d_in[0];
  const u16* wqkv = (const u16*)d_in[1];
  const u16* wo = (const u16*)d_in[2];
  const u16* wr = (const u16*)d_in[4];
  const u16* wg = (const u16*)d_in[5];
  const u16* wu = (const u16*)d_in[6];
  const u16* wd = (const u16*)d_in[7];
  char* ws = (char*)d_ws;
  u16* out = (u16*)d_out;
  (void)hipFuncSetAttribute((const void*)k1_qkv, hipFuncAttributeMaxDynamicSharedMemorySize, K1_LDS);
  (void)hipFuncSetAttribute((const void*)k2_attn, hipFuncAttributeMaxDynamicSharedMemorySize, K2_LDS);
  (void)hipFuncSetAttribute((const void*)k3_oproj, hipFuncAttributeMaxDynamicSharedMemorySize, K3_LDS);
  (void)hipFuncSetAttribute((const void*)k4_ffn, hipFuncAttributeMaxDynamicSharedMemorySize, K4_LDS);
  k1_qkv<<<256, 256, K1_LDS, stream>>>(x, wqkv, ws);
  k2_attn<<<256, 512, K2_LDS, stream>>>(ws);
  k3_oproj<<<256, 256, K3_LDS, stream>>>(x, wo, wr, ws, out);
  k4_ffn<<<86, 256, K4_LDS, stream>>>(wg, wu, wd, ws, out);
}
```

```cpp
#include <hip/hip_runtime.h>
#include <hip/hip_bf16.h>
#define STAMP_DECL
#define STAMP(k, s)
#define STAMP_FLUSH(k)

typedef unsigned short u16;
typedef __bf16 bf16x2_t __attribute__((ext_vector_type(2)));
typedef __bf16 bf16x8_t __attribute__((ext_vector_type(8)));
typedef __bf16 bf16x16_t __attribute__((ext_vector_type(16)));
typedef short s16x4 __attribute__((ext_vector_type(4)));
typedef float f32x4 __attribute__((ext_vector_type(4)));
typedef float f32x8 __attribute__((ext_vector_type(8)));
typedef float f32x16 __attribute__((ext_vector_type(16)));
typedef unsigned u32x2 __attribute__((ext_vector_type(2)));
typedef unsigned u32x4 __attribute__((ext_vector_type(4)));
#define LAS __attribute__((address_space(3)))

constexpr int D_ = 512, F_ = 1376;
constexpr unsigned SENT = 0xFFFFFFFFu;
constexpr float LN_EPS = 1e-5f;
constexpr float QSCALE = 0.25f * 1.4426950408889634f;
constexpr unsigned SPIN_CAP = 1u << 16;
constexpr int SC1 = 16;

constexpr size_t WS_QF = 0;
constexpr size_t WS_KF = 2u << 20;
constexpr size_t WS_VF = 4u << 20;
constexpr size_t WS_OF = 6u << 20;
constexpr size_t WS_HN = 8u << 20;
constexpr size_t WS_XS = 10u << 20;
constexpr size_t WS_ACT = WS_XS + (256u << 10);
constexpr size_t WS_KEY = WS_ACT + 22016;

__device__ __forceinline__ f32x16 mfma32(u32x4 a, u32x4 b, f32x16 c) {
  return __builtin_amdgcn_mfma_f32_32x32x16_bf16(__builtin_bit_cast(bf16x8_t, a), __builtin_bit_cast(bf16x8_t, b), c, 0, 0, 0);
}
__device__ __forceinline__ f32x4 mfma16(u32x4 a, u32x4 b, f32x4 c) {
  return __builtin_amdgcn_mfma_f32_16x16x32_bf16(__builtin_bit_cast(bf16x8_t, a), __builtin_bit_cast(bf16x8_t, b), c, 0, 0, 0);
}
__device__ __forceinline__ f32x16 smfmac32(u32x4 a, f32x8 b, f32x16 c, int idx) {
  return __builtin_amdgcn_smfmac_f32_32x32x32_bf16(__builtin_bit_cast(bf16x8_t, a), __builtin_bit_cast(bf16x16_t, b), c, idx, 0, 0);
}
__device__ __forceinline__ float dot2bf(unsigned a, unsigned b, float c) {
  return __builtin_amdgcn_fdot2_f32_bf16(__builtin_bit_cast(bf16x2_t, a), __builtin_bit_cast(bf16x2_t, b), c, false);
}
__device__ __forceinline__ float swap32_sum(float v) {
  auto r = __builtin_amdgcn_permlane32_swap(__float_as_uint(v), __float_as_uint(v), false, false);
  return __uint_as_float(r[0]) + __uint_as_float(r[1]);
}
__device__ __forceinline__ unsigned pkbf(float lo, float hi) {
  bf16x2_t v; v[0] = (__bf16)lo; v[1] = (__bf16)hi; return __builtin_bit_cast(unsigned, v);
}
__device__ __forceinline__ float bflo(unsigned u) { return __uint_as_float(u << 16); }
__device__ __forceinline__ float bfhi(unsigned u) { return __uint_as_float(u & 0xFFFF0000u); }
__device__ __forceinline__ float rbf(float f) { return (float)(__bf16)f; }
__device__ __forceinline__ float swap32_max(float v) {
  auto r = __builtin_amdgcn_permlane32_swap(__float_as_uint(v), __float_as_uint(v), false, false);
  return __builtin_fmaxf(__uint_as_float(r[0]), __uint_as_float(r[1]));
}
__device__ __forceinline__ void glds16(const void* sbase, unsigned voff, unsigned lds_base) {
  unsigned sv;
  asm volatile("s_mov_b32 %0, m0\n\ts_mov_b32 m0, %3\n\ts_nop 0\n\tglobal_load_lds_dwordx4 %1, %2\n\ts_mov_b32 m0, %0"
               : "=&s"(sv) : "v"(voff), "s"(sbase), "s"(lds_base) : "memory");
}
#define WAIT_BAR(N) asm volatile("s_waitcnt vmcnt(" #N ") lgkmcnt(0)\n\ts_barrier" ::: "memory")
#define VM_BAR(N) asm volatile("s_waitcnt vmcnt(" #N ")\n\ts_barrier" ::: "memory")
#define LDS_BAR() do { asm volatile("" ::: "memory"); __builtin_amdgcn_s_waitcnt(0xC07F); __builtin_amdgcn_s_barrier(); asm volatile("" ::: "memory"); } while (0)
#define WAIT_VM(N) asm volatile("s_waitcnt vmcnt(" #N ")" ::: "memory")
__device__ __forceinline__ u32x4 ldsr128(const LAS char* p) { return *(const LAS u32x4*)p; }
__device__ __forceinline__ u32x2 ldstr(const LAS char* p) {
  return __builtin_bit_cast(u32x2, __builtin_amdgcn_ds_read_tr16_b64_v4i16((LAS s16x4*)p));
}
__device__ __forceinline__ u32x4 ldstr2(const LAS char* p, int off2) { const u32x2 a = ldstr(p), b = ldstr(p + off2); return u32x4{a[0], a[1], b[0], b[1]}; }
__device__ __forceinline__ int swapb23(int v) { return (v & ~12) | ((v & 4) << 1) | ((v & 8) >> 1); }
__device__ __forceinline__ unsigned nosent(unsigned v) { return min(v, 0xFFFFFFFEu); }
__device__ __forceinline__ u32x4 nosent4(u32x4 v) { return u32x4{nosent(v[0]), nosent(v[1]), nosent(v[2]), nosent(v[3])}; }
__device__ __forceinline__ unsigned umax4(u32x4 v) { return max(max(v[0], v[1]), max(v[2], v[3])); }
__device__ __forceinline__ __amdgpu_buffer_rsrc_t mkrsrc(const void* p, unsigned bytes) {
  return __builtin_amdgcn_make_buffer_rsrc((void*)p, 0, bytes, 0x00020000);
}
template <int CTRL> __device__ __forceinline__ float dppf(float v) {
  return __uint_as_float(__builtin_amdgcn_update_dpp(0, __float_as_uint(v), CTRL, 0xF, 0xF, true));
}
template <int CTRL> __device__ __forceinline__ unsigned dppu(unsigned v) { return __builtin_amdgcn_update_dpp(0, v, CTRL, 0xF, 0xF, true); }
__device__ __forceinline__ unsigned rdl(unsigned v, int l) { return (unsigned)__builtin_amdgcn_readlane((int)v, l); }
__device__ __forceinline__ float rdlf(float v, int l) { return __uint_as_float(rdl(__float_as_uint(v), l)); }
__device__ __forceinline__ float sum8(float v) { v += dppf<0xB1>(v); v += dppf<0x4E>(v); v += dppf<0x141>(v); return v; }

#define K1ROT 3
#define K1DIST 2
constexpr int K1_SLOT = 16384 + 12288;
constexpr int K1_CS = 4 * K1_SLOT, K1_CSP = K1_CS + 512, K1_LDS = K1_CSP + 8 * 384;

template <int NV>
__device__ __forceinline__ void k1_body(char* __restrict__ ws, LAS char* lds, int rb, int cb) {
  const int tid = threadIdx.x, lane = tid & 63, t = lane & 31, h = lane >> 5;
  const int wid = __builtin_amdgcn_readfirstlane(tid >> 6);
  STAMP_DECL STAMP(0, 0);
  int xr[4];
#pragma unroll
  for (int u = 0; u < 4; ++u) xr[u] = wid * 4096 + t * 128 + (((2 * u + h) ^ ((t >> 1) & 7)) * 16);
  const int g = lane >> 4, q = (lane >> 2) & 3, p4 = lane & 3;
  const int wrd = 16384 + (8 * h + q) * 192 + 32 * (g & 1) + 8 * p4;
  f32x16 acc[3];
#pragma unroll
  for (int j = 0; j < 3; ++j) acc[j] = f32x16{};
  float s1a = 0.f, s1b = 0.f, s2a = 0.f, s2b = 0.f;
  u32x4 xf = u32x4{0, 0, 0, 0}, wf[3];
#pragma unroll
  for (int j = 0; j < 3; ++j) wf[j] = u32x4{0, 0, 0, 0};
  auto mm = [&]() {
#pragma unroll
    for (int j = 0; j < 3; ++j) {
      if (j >= 3 - NV) acc[j] = mfma32(xf, wf[j], acc[j]);
      else acc[j] = mfma32(wf[j], xf, acc[j]);
    }
  };
#pragma unroll 1
  for (int c = 0; c < 8; ++c) {
    LDS_BAR();
    if (c == 0) STAMP(0, 1);
    const LAS char* sb = lds + (c & 3) * K1_SLOT;
    u32x4 xn[4];
#pragma unroll
    for (int u = 0; u < 4; ++u) {
      xn[u] = ldsr128(sb + xr[u]);
      u32x4 wn[3];
#pragma unroll
      for (int j = 0; j < 3; ++j) wn[j] = ldstr2(sb + wrd + u * 3072 + j * 64, 768);
      mm();
#pragma unroll
      for (int z = 0; z < 3; ++z) { __builtin_amdgcn_sched_group_barrier(0x8, 1, 0); __builtin_amdgcn_sched_group_barrier(0x102, 3, 0); }
      __builtin_amdgcn_sched_barrier(0);
      xf = xn[u];
#pragma unroll
      for (int j = 0; j < 3; ++j) wf[j] = wn[j];
    }
#pragma unroll
    for (int u = 0; u < 4; ++u)
#pragma unroll
      for (int e = 0; e < 4; e += 2) {
        s1a = dot2bf(xn[u][e], 0x3F803F80u, s1a); s2a = dot2bf(xn[u][e], xn[u][e], s2a);
        s1b = dot2bf(xn[u][e + 1], 0x3F803F80u, s1b); s2b = dot2bf(xn[u][e + 1], xn[u][e + 1], s2b);
      }
    asm volatile("" : "+v"(s1a), "+v"(s1b), "+v"(s2a), "+v"(s2b));
    __builtin_amdgcn_sched_barrier(0);
  }
  __builtin_amdgcn_s_barrier();
  mm();
  STAMP(0, 2);
  LAS float* scr = (LAS float*)(lds + wid * 4096);
  const LAS float* csum = (const LAS float*)(lds + K1_CS);
  const float s2 = swap32_sum(s2a + s2b);
  const float mean = swap32_sum(s1a + s1b) * (1.f / D_);
  const float var = __builtin_fmaxf(s2 * (1.f / D_) - mean * mean, 0.f);
  const float rstd = __builtin_amdgcn_rsqf(var + LN_EPS);
  if (NV > 0 && h == 0) { scr[160 + t] = rstd; scr[192 + t] = mean * rstd; }
  const int tile = rb * 4 + wid;
  asm volatile("" ::: "memory"); __builtin_amdgcn_s_barrier(); asm volatile("" ::: "memory");
#pragma unroll
  for (int j = 0; j < 3; ++j) {
    const int gt = cb * 3 + j;
    float v[16];
    if (j >= 3 - NV) {
      const float cs = csum[32 * j + t];
#pragma unroll
      for (int gq = 0; gq < 4; ++gq) {
        const f32x4 a = *(const LAS f32x4*)(scr + 160 + 8 * gq + 4 * h), b = *(const LAS f32x4*)(scr + 192 + 8 * gq + 4 * h);
#pragma unroll
        for (int e = 0; e < 4; ++e) v[4 * gq + e] = acc[j][4 * gq + e] * a[e] - b[e] * cs;
      }
      const int head = (gt - 32) * 2 + (t >> 4);
      char* dst = ws + WS_VF + (size_t)(head * 64 + tile) * 1024 + h * 256 + swapb23(t & 15) * 16;
#pragma unroll
      for (int s = 0; s < 2; ++s)
        *(u32x4*)(dst + s * 512) = u32x4{pkbf(v[8 * s], v[8 * s + 1]), pkbf(v[8 * s + 2], v[8 * s + 3]), pkbf(v[8 * s + 4], v[8 * s + 5]), pkbf(v[8 * s + 6], v[8 * s + 7])};
    } else {
      const bool isq = gt < 16;
      const float sc = isq ? QSCALE : 1.f;
      const float a = rstd * sc, b = mean * rstd * sc;
#pragma unroll
      for (int gq = 0; gq < 4; ++gq) {
        const f32x4 c4 = *(const LAS f32x4*)(csum + 32 * j + 8 * gq + 4 * h);
#pragma unroll
        for (int e = 0; e < 4; ++e) v[4 * gq + e] = acc[j][4 * gq + e] * a - b * c4[e];
      }
      const int head = (gt & 15) * 2;
      char* dst = ws + (isq ? WS_QF : WS_KF) + (size_t)head * 65536 + tile * 1024 + (isq ? t : swapb23(t)) * 32 + h * 16;
#pragma unroll
      for (int s = 0; s < 2; ++s)
        *(u32x4*)(dst + s * 65536) = u32x4{pkbf(v[8 * s], v[8 * s + 1]), pkbf(v[8 * s + 2], v[8 * s + 3]), pkbf(v[8 * s + 4], v[8 * s + 5]), pkbf(v[8 * s + 6], v[8 * s + 7])};
    }
  }
  STAMP(0, 3);
}

__global__ __launch_bounds__(512, 2) void k1_qkv(const u16* __restrict__ x, const u16* __restrict__ w, char* __restrict__ ws) {
  extern __shared__ __attribute__((aligned(16))) char smem[];
  LAS char* lds = (LAS char*)smem;
  const int b = blockIdx.x, g = b & 7, i = b >> 3;
  const int rb = 4 * (g & 3) + (i & 3), cb = 8 * (g >> 2) + (i >> 2);
  const int wid = __builtin_amdgcn_readfirstlane(threadIdx.x >> 6);
  if (wid >= 4) {
    const int rot = K1ROT == 1 ? ((i >> 2) + 2 * (i & 3)) & 7 : K1ROT == 3 ? (((i >> 2) + (i & 3)) & 1) * 4 : K1ROT == 4 ? (((i >> 2) + (i & 3)) & 3) * 2 : 0;
    const int lane = threadIdx.x & 63, lw = wid - 4;
    const unsigned lds0 = (unsigned)(uintptr_t)lds;
    unsigned xoff[2], woff[3];
#pragma unroll
    for (int p = 0; p < 2; ++p) {
      const int row = p * 8 + (lane >> 3), slot = lane & 7;
      xoff[p] = (unsigned)((rb * 128 + lw * 32 + row) * 1024 + ((slot ^ ((row >> 1) & 7)) * 16));
    }
#pragma unroll
    for (int p = 0; p < 3; ++p) {
      const int idx = (lw * 3 + p) * 64 + lane, k = idx / 12, c = idx - k * 12;
      woff[p] = (unsigned)(k * 3072 + cb * 192 + c * 16);
    }
    auto stage_x = [&](int c) {
      const char* xs = (const char*)x + ((c + rot) & 7) * 128;
      const unsigned base = lds0 + (c & 3) * K1_SLOT;
#pragma unroll
      for (int p = 0; p < 4; ++p) glds16(xs + (p >> 1) * 16384, xoff[p & 1], base + lw * 4096 + p * 1024);
    };
    auto stage_w = [&](int c) {
      const char* wsrc = (const char*)w + ((c + rot) & 7) * (64 * 3072);
      const unsigned base = lds0 + (c & 3) * K1_SLOT;
#pragma unroll
      for (int p = 0; p < 3; ++p) glds16(wsrc, woff[p], base + 16384 + (lw * 3 + p) * 1024);
    };
    const int wrd = 16384 + lw * 3072 + (8 * (lane >> 5) + ((lane >> 2) & 3)) * 192 + 32 * ((lane >> 4) & 1) + 8 * (lane & 3);
    float cs[3] = {0.f, 0.f, 0.f};
    stage_x(0); stage_w(0); stage_x(1); stage_w(1);
#pragma unroll 1
    for (int c = 0; c < 8; ++c) {
      if (c < 7) WAIT_BAR(7); else WAIT_BAR(0);
      const LAS char* sb = lds + (c & 3) * K1_SLOT + wrd;
      u32x4 wf[3];
#pragma unroll
      for (int j = 0; j < 3; ++j) wf[j] = ldstr2(sb + j * 64, 768);
      if (c < 6) { stage_x(c + 2); stage_w(c + 2); }
#pragma unroll
      for (int e = 0; e < 4; ++e)
#pragma unroll
        for (int j = 0; j < 3; ++j) cs[j] = dot2bf(wf[j][e], 0x3F803F80u, cs[j]);
    }
#pragma unroll
    for (int j = 0; j < 3; ++j) *(LAS float*)(lds + K1_CSP + (lw * 2 + (lane >> 5)) * 384 + (32 * j + (lane & 31)) * 4) = cs[j];
    LDS_BAR();
    if (lw == 0 && lane < 24) {
      const LAS char* pp = lds + K1_CSP + lane * 16;
      f32x4 sacc = *(const LAS f32x4*)pp;
#pragma unroll
      for (int e = 1; e < 8; ++e) sacc += *(const LAS f32x4*)(pp + e * 384);
      *(LAS f32x4*)(lds + K1_CS + lane * 16) = sacc;
    }
    LDS_BAR();
    const int lt = threadIdx.x - 256;
    const u32x4 s4 = u32x4{SENT, SENT, SENT, SENT};
    char* po = ws + WS_OF + (size_t)b * 8192 + lt * 16;
    *(u32x4*)po = s4; *(u32x4*)(po + 4096) = s4;
    const int e = b * 8 + lt;
    if (lt < 8 && e < (22016 + 8192) / 16) *(u32x4*)(ws + WS_ACT + e * 16) = s4;
    return;
  }
  if (cb < 10) k1_body<0>(ws, lds, rb, cb);
  else if (cb == 10) k1_body<1>(ws, lds, rb, cb);
  else k1_body<3>(ws, lds, rb, cb);
}

constexpr int L_K = 0, L_V = 65536, L_ONE = 131072, L_Q = L_ONE + 4096, K2_LDS = 163840;
constexpr int L_WO0 = L_K, L_WO1 = L_V;
constexpr int L_F1 = L_K + 32768, L_F2 = L_V + 32768;
constexpr int L_SEL = L_K + 49152;
constexpr int NGU = 86, DN0 = 128;

__device__ __forceinline__ float rowmax16(const f32x16& s) {
  float a = __builtin_fmaxf(__builtin_fmaxf(s[0], s[1]), s[2]), b = __builtin_fmaxf(__builtin_fmaxf(s[3], s[4]), s[5]);
  a = __builtin_fmaxf(__builtin_fmaxf(a, s[6]), s[7]); b = __builtin_fmaxf(__builtin_fmaxf(b, s[8]), s[9]);
  a = __builtin_fmaxf(__builtin_fmaxf(a, s[10]), s[11]); b = __builtin_fmaxf(__builtin_fmaxf(b, s[12]), s[13]);
  a = __builtin_fmaxf(__builtin_fmaxf(a, s[14]), s[15]);
  return swap32_max(__builtin_fmaxf(a, b));
}
__device__ __forceinline__ f32x8 lo8(const f32x16& s) { return __builtin_shufflevector(s, s, 0, 1, 2, 3, 4, 5, 6, 7); }
__device__ __forceinline__ f32x8 hi8(const f32x16& s) { return __builtin_shufflevector(s, s, 8, 9, 10, 11, 12, 13, 14, 15); }
__device__ __forceinline__ int ffn_slot(int p) { return p < 28 ? L_Q + p * 1024 : p < 36 ? L_F1 + (p - 28) * 1024 : L_F2 + (p - 36) * 1024; }
#define BAR() do { asm volatile("" ::: "memory"); __builtin_amdgcn_s_barrier(); asm volatile("" ::: "memory"); } while (0)
#define LD128(r, off) __builtin_amdgcn_raw_buffer_load_b128(r, off, 0, SC1)
#define ST128(v, r, off) __builtin_amdgcn_raw_buffer_store_b128(v, r, off, 0, SC1)

#define E1(X, i) "v_exp_f32 v[" #X "+" #i "], v[" #X "+" #i "]\n\t"
#define SMF(A, B, off) "v_smfmac_f32_32x32x32_bf16 v[0:15], v[" #A ":" #A "+3], v[" #B "+" #off ":" #B "+" #off "+7], v88\n\t"
#define QK(Y) "v_mfma_f32_32x32x16_bf16 v[" #Y ":" #Y "+15], v[68:71], v[64:67], v[16:31]\n\t"
#define RDV(D, off) "ds_read_b128 v[" #D ":" #D "+3], v90 offset:" #off "\n\t"
#define RDK(off) "ds_read_b128 v[68:71], v89 offset:" #off "\n\t"
#define STEP(X, Y, VA, VPB, VA2, VB2, KOFF, VOFF, VOFF2, EXTRA) \
  E1(X, 0) E1(X, 1) SMF(VPB, Y, 8) RDV(VA2, VOFF) \
  E1(X, 2) E1(X, 3) E1(X, 4) E1(X, 5) \
  "s_waitcnt lgkmcnt(1)\n\t" QK(Y) RDV(VB2, VOFF2) RDK(KOFF) \
  E1(X, 6) E1(X, 7) E1(X, 8) E1(X, 9) \
  SMF(VA, X, 0) EXTRA \
  E1(X, 10) E1(X, 11) E1(X, 12) E1(X, 13) E1(X, 14) E1(X, 15)

__global__ __launch_bounds__(576, 3) void k2_fused(const u16* __restrict__ x, const u16* __restrict__ wo, const u16* __restrict__ wr,
                                                   const u16* __restrict__ wg, const u16* __restrict__ wu, const u16* __restrict__ wd,
                                                   char* __restrict__ ws, u16* __restrict__ out) {
  extern __shared__ __attribute__((aligned(16))) char smem[];
  LAS char* lds = (LAS char*)smem;
  const unsigned lds0 = (unsigned)(uintptr_t)lds;
  const int tid = threadIdx.x, lane = tid & 63, t = lane & 31, h = lane >> 5;
  const int wid = __builtin_amdgcn_readfirstlane(tid >> 6);
  const int b = blockIdx.x, head = (b & 7) * 4 + ((b >> 3) & 3), qb = b >> 5;
  const int pm = qb * 4 + (head >> 3), cb = head & 7;
  const unsigned l16 = lane * 16;
  const bool isgu = b < NGU, isdn = b >= DN0 && b < DN0 + 32;
  const __amdgpu_buffer_rsrc_t r_hn = mkrsrc(ws + WS_HN, 2u << 20), r_xs = mkrsrc(ws + WS_XS, 256u << 10), r_key = mkrsrc(ws + WS_KEY, 8192);
  STAMP_DECL STAMP(1, 0);

  if (wid == 8) {
    __builtin_amdgcn_s_setprio(3);
    const char* ksrc = ws + WS_KF + (size_t)head * 65536;
    auto kv = [&](int t0, int t1) {
#pragma unroll 1
      for (int tl = t0; tl < t1; ++tl) {
        glds16(ksrc + tl * 1024, l16, lds0 + L_K + tl * 1024);
        glds16(ksrc + (WS_VF - WS_KF) + tl * 1024, l16, lds0 + L_V + tl * 1024);
      }
    };
    const unsigned foff = isgu ? (unsigned)((lane >> 1) * (F_ * 2) + b * 32 + (lane & 1) * 16) : (unsigned)((lane >> 1) * 1024 + (b - DN0) * 32 + (lane & 1) * 16);
    auto ffn = [&](int p0, int p1) {
      if (isgu) {
#pragma unroll 1
        for (int p = p0; p < min(p1, 32); ++p) glds16((const char*)(p < 16 ? wg : wu) + (p & 15) * (32 * F_ * 2), foff, lds0 + ffn_slot(p));
      } else if (isdn) {
#pragma unroll 1
        for (int p = p0; p < min(p1, 43); ++p) glds16((const char*)wd + p * 32768, foff, lds0 + ffn_slot(p));
      }
    };
    BAR();
    kv(12, 24); kv(24, 40);
    VM_BAR(32);
    WAIT_VM(8);
    kv(40, 64);
    WAIT_VM(40);
    {
      const u32x4 s4 = u32x4{SENT, SENT, SENT, SENT};
#pragma unroll
      for (int e = 0; e < 8; ++e) ST128(s4, r_hn, (pm * 64 + e * 8 + (lane >> 3)) * 1024 + cb * 128 + (lane & 7) * 16);
      ST128(s4, r_xs, (pm * 8 + cb) * 1024 + l16);
    }
    VM_BAR(0);
    ffn(0, 28);
    float sw;
    {
      const u32x4 w4 = *(const u32x4*)(wr + lane * 8);
      sw = ((bflo(w4[0]) + bfhi(w4[0])) + (bflo(w4[1]) + bfhi(w4[1]))) + ((bflo(w4[2]) + bfhi(w4[2])) + (bflo(w4[3]) + bfhi(w4[3])));
      sw = sum8(sw); sw += dppf<0x140>(sw);
      sw = (rdlf(sw, 0) + rdlf(sw, 16)) + (rdlf(sw, 32) + rdlf(sw, 48));
    }
    VM_BAR(0);
    const unsigned wooff = (unsigned)((lane >> 3) * 1024 + cb * 128 + (((lane & 7) ^ (((lane >> 4) & 1) << 2)) * 16));
#pragma unroll 1
    for (int p = 0; p < 32; ++p) glds16((const char*)wo + p * 8192, wooff, lds0 + L_WO0 + p * 1024);
    WAIT_VM(8);
#pragma unroll 1
    for (int p = 0; p < 32; ++p) glds16((const char*)wo + (32 + p) * 8192, wooff, lds0 + L_WO1 + p * 1024);
    ffn(28, 44);
    VM_BAR(0);
    if (cb != 0) return;
    BAR(); BAR();
    u32x4 pv[8];
    for (unsigned spins = 0;; ++spins) {
      asm volatile("" ::: "memory");
      unsigned mx = 0;
#pragma unroll
      for (int e = 0; e < 8; ++e) pv[e] = LD128(r_xs, (pm * 8 + e) * 1024 + l16);
#pragma unroll
      for (int e = 0; e < 8; ++e) mx = max(mx, umax4(pv[e]));
      if (__all(mx != SENT) || spins > SPIN_CAP) break;
      __builtin_amdgcn_s_sleep(2);
    }
    float s1, s2, sd;
#define SUM8(c) (((__uint_as_float(pv[0][c]) + __uint_as_float(pv[1][c])) + (__uint_as_float(pv[2][c]) + __uint_as_float(pv[3][c]))) + \
                 ((__uint_as_float(pv[4][c]) + __uint_as_float(pv[5][c])) + (__uint_as_float(pv[6][c]) + __uint_as_float(pv[7][c]))))
    s1 = SUM8(0); s2 = SUM8(1); sd = SUM8(2);
    const float mean = s1 * (1.f / D_);
    const float rstd = __builtin_amdgcn_rsqf(__builtin_fmaxf(s2 * (1.f / D_) - mean * mean, 0.f) + LN_EPS);
    const unsigned u = __float_as_uint(rstd * (sd - mean * sw));
    const unsigned ordv = (u & 0x80000000u) ? ~u : (u | 0x80000000u);
    __builtin_amdgcn_raw_buffer_store_b32(max(min(ordv & 0xFFFFF800u, 0xFFFFF000u), 0x800u) | (unsigned)(2047 - (pm * 64 + lane)), r_key, (pm * 64 + lane) * 4, 0, SC1);
    return;
  }

  {
    const char* kvsrc = ws + ((wid & 1) ? WS_VF : WS_KF) + (size_t)head * 65536 + (wid >> 1) * 1024;
    const unsigned kvdst = lds0 + ((wid & 1) ? L_V : L_K) + (wid >> 1) * 1024;
    glds16(ws + WS_QF + (size_t)head * 65536 + (qb * 8 + wid) * 1024, l16, lds0 + L_Q + wid * 1024);
#pragma unroll
    for (int i = 0; i < 3; ++i) glds16(kvsrc + i * 4096, l16, kvdst + i * 4096);
  }
  if (tid < 16) { const unsigned v = (tid & 1) ? 0u : 0x3F803F80u; *(LAS u32x4*)(lds + L_ONE + (tid >> 1) * 512 + (tid & 1) * 16) = u32x4{v, v, v, v}; }
  const bool onel = (lane & 16) != 0;
  unsigned kaddr = lds0 + L_K + t * 32 + h * 16;
  unsigned vaddr = lds0 + (onel ? L_ONE + ((lane & 11) ? 16 : 0) : L_V + h * 256 + (lane & 15) * 16);
  const int vstep = onel ? 0 : 4096;
  const int IDX = 0xDDDDDDDD;
  const int row = pm * 64 + wid * 8 + (lane >> 3), col = cb * 64 + (lane & 7) * 8;
  f32x16 negm, sx, sy = f32x16{}, o = f32x16{};
  u32x4 vpb = u32x4{0, 0, 0, 0};
  asm volatile("" : "+{v[0:15]}"(o), "+{v[48:63]}"(sy), "+{v[84:87]}"(vpb));

  WAIT_BAR(0);
  STAMP(1, 1);
  const u32x4 xres = *(const u32x4*)(x + row * D_ + col), wrv = *(const u32x4*)(wr + col);
  const u32x4 qf = ldsr128(lds + L_Q + wid * 1024 + t * 32 + h * 16);
  u32x4 kn, va, vb;
  {
    const u32x4 k0 = ldsr128((const LAS char*)(uintptr_t)kaddr);
    kn = ldsr128((const LAS char*)(uintptr_t)kaddr + 1024);
    va = ldsr128((const LAS char*)(uintptr_t)vaddr); vb = ldsr128((const LAS char*)(uintptr_t)vaddr + 512);
    const float m = -rowmax16(mfma32(k0, qf, f32x16{}));
#pragma unroll
    for (int r = 0; r < 16; ++r) negm[r] = m;
    sx = mfma32(k0, qf, negm);
  }
  STAMP(1, 2);
  {
    unsigned cnt;
    asm volatile(
        "s_mov_b32 %[cnt], 0\n\t"
        "s_nop 9\n\t"
        "1:\n\t"
        "s_bitcmp1_b32 0x424, %[cnt]\n\t"
        "s_cbranch_scc0 2f\n\t"
        "s_barrier\n\t"
        "2:\n\t"
        STEP(32, 48, 72, 84, 80, 84, 2048, 1024, 1536, "")
        STEP(48, 32, 80, 76, 72, 76, 3072, 2048, 2560, "")
        STEP(32, 48, 72, 84, 80, 84, 4096, 3072, 3584, "v_add_u32 v90, v90, v91\n\t")
        STEP(48, 32, 80, 76, 72, 76, 5120, 0, 512, "v_add_u32 v89, 0x1000, v89\n\ts_add_u32 %[cnt], %[cnt], 1\n\ts_cmp_lg_u32 %[cnt], 16\n\t")
        "s_cbranch_scc1 1b\n\t"
        "s_nop 1\n\t"
        SMF(84, 48, 8)
        "s_waitcnt lgkmcnt(0)\n\t"
        "s_nop 11\n\t"
        : "+{v[0:15]}"(o), "+{v[32:47]}"(sx), "+{v[48:63]}"(sy), "+{v[68:71]}"(kn), "+{v[72:75]}"(va), "+{v[76:79]}"(vb),
          "+{v[84:87]}"(vpb), "+{v89}"(kaddr), "+{v90}"(vaddr), [cnt] "=&s"(cnt)
        : "{v[16:31]}"(negm), "{v[64:67]}"(qf), "{v88}"(IDX), "{v91}"(vstep)
        : "v80", "v81", "v82", "v83", "scc", "memory");
  }
  STAMP(1, 3);
  {
    float am = __builtin_fmaxf(__builtin_fmaxf(__builtin_fabsf(o[0]), __builtin_fabsf(o[1])), __builtin_fabsf(o[2]));
    am = __builtin_fmaxf(__builtin_fmaxf(am, __builtin_fabsf(o[3])), __builtin_fabsf(o[4]));
    am = __builtin_fmaxf(__builtin_fmaxf(am, __builtin_fabsf(o[5])), __builtin_fabsf(o[6]));
    am = __builtin_fmaxf(am, __builtin_fabsf(o[7]));
    const float chk = ((o[0] + o[1]) + (o[2] + o[3])) + ((o[4] + o[5]) + (o[6] + o[7]));
    const bool bad = !(o[8] < 1.8446744e19f) || !(am < 3.0e38f) || (chk != chk);
    if (__builtin_expect(__any(bad), 0)) {
      float m = -INFINITY;
      o = f32x16{};
      const char* kq = ws + WS_KF + (size_t)head * 65536 + t * 32 + h * 16;
      const char* vq = ws + WS_VF + (size_t)head * 65536 + h * 256 + (lane & 15) * 16;
      const u32x4 ones = u32x4{0x3F803F80u, 0x3F803F80u, 0x3F803F80u, 0x3F803F80u};
#pragma unroll 1
      for (int i = 0; i < 64; ++i) {
        f32x16 s = mfma32(*(const u32x4*)(kq + i * 1024), qf, f32x16{});
        u32x4 v0 = *(const u32x4*)(vq + i * 1024), v1 = *(const u32x4*)(vq + i * 1024 + 512);
        if (onel) { v0 = ones; v1 = ones; }
        const float mn = __builtin_fmaxf(m, rowmax16(s));
        const float al = __builtin_amdgcn_exp2f(m - mn);
        m = mn;
#pragma unroll
        for (int r = 0; r < 16; ++r) { o[r] *= al; s[r] = __builtin_amdgcn_exp2f(s[r] - mn); }
        o = smfmac32(v0, lo8(s), o, IDX);
        o = smfmac32(v1, hi8(s), o, IDX);
      }
    }
  }
  const __amdgpu_buffer_rsrc_t r_o = mkrsrc(ws + WS_OF, 2u << 20);
  {
    const float inv = __builtin_amdgcn_rcpf(o[8]);
    ST128(nosent4(u32x4{pkbf(o[0] * inv, o[1] * inv), pkbf(o[2] * inv, o[3] * inv), pkbf(o[4] * inv, o[5] * inv), pkbf(o[6] * inv, o[7] * inv)}),
          r_o, head * 65536 + (qb * 8 + wid) * 1024 + t * 32 + h * 16);
  }
  BAR();
  STAMP(1, 4);

  const int mh = wid & 1, kq = wid >> 1;
  const int g = lane >> 4, q = (lane >> 2) & 3, p4 = lane & 3;
  LAS unsigned* cand = (LAS unsigned*)(lds + L_SEL);
  {
    u32x4 of[8], wf0[8], wf1[8];
    const int flip = ((q >> 1) & 1) << 6;
    const LAS char* wb = lds + (kq < 2 ? L_WO0 + kq * 16384 : L_WO1 + (kq - 2) * 16384) + (8 * h + q) * 128 + 32 * (g & 1) + 8 * p4;
#pragma unroll
    for (int e = 0; e < 8; ++e) { wf0[e] = ldstr2(wb + flip + e * 2048, 512); wf1[e] = ldstr2(wb + (64 ^ flip) + e * 2048, 512); }
    const int ooff = kq * 8 * 65536 + pm * 2048 + mh * 1024 + t * 32 + h * 16;
#pragma unroll
    for (int e = 0; e < 8; ++e) of[e] = __builtin_amdgcn_raw_buffer_load_b128(r_o, ooff, e * 65536, SC1);
    if (tid < 80) cand[tid] = 0;
    LDS_BAR();
    for (unsigned spins = 0; spins < SPIN_CAP; ++spins) {
      bool all = true;
#pragma unroll
      for (int e = 0; e < 8; ++e) {
        if (__builtin_expect(__any(umax4(of[e]) == SENT), 0)) {
          asm volatile("" ::: "memory");
          of[e] = __builtin_amdgcn_raw_buffer_load_b128(r_o, ooff, e * 65536, SC1);
          all = false;
        }
      }
      if (all) break;
    }
    STAMP(1, 5);
    f32x16 acc0 = f32x16{}, acc1 = f32x16{};
#pragma unroll
    for (int e = 0; e < 8; ++e) { acc0 = mfma32(wf0[e], of[e], acc0); acc1 = mfma32(wf1[e], of[e], acc1); }
    LAS char* rb = lds + ((kq & 1) ? L_WO1 : L_WO0) + (kq >> 1) * 16384 + (mh * 32 + t) * 256;
    const int swz = (t & 7) | ((t & 1) << 3);
#pragma unroll
    for (int gq = 0; gq < 4; ++gq) {
      *(LAS f32x4*)(rb + (((h * 8 + gq) ^ swz) * 16)) = f32x4{acc0[4 * gq], acc0[4 * gq + 1], acc0[4 * gq + 2], acc0[4 * gq + 3]};
      *(LAS f32x4*)(rb + (((h * 8 + 4 + gq) ^ swz) * 16)) = f32x4{acc1[4 * gq], acc1[4 * gq + 1], acc1[4 * gq + 2], acc1[4 * gq + 3]};
    }
  }
  LDS_BAR();
  STAMP(1, 6);
  float hv[8];
  {
    const int lr = wid * 8 + (lane >> 3);
    const int sl = ((lane & 7) ^ ((lr & 7) | ((lr & 1) << 3))) * 16;
    const LAS char* r0 = lds + lr * 256 + sl;
    const LAS char* r1 = lds + lr * 256 + (sl ^ 128);
    f32x4 a0 = f32x4{bflo(xres[0]), bfhi(xres[0]), bflo(xres[1]), bfhi(xres[1])}, a1 = f32x4{bflo(xres[2]), bfhi(xres[2]), bflo(xres[3]), bfhi(xres[3])};
    a0 += (*(const LAS f32x4*)(r0 + L_WO0) + *(const LAS f32x4*)(r0 + L_WO1)) + (*(const LAS f32x4*)(r0 + L_WO0 + 16384) + *(const LAS f32x4*)(r0 + L_WO1 + 16384));
    a1 += (*(const LAS f32x4*)(r1 + L_WO0) + *(const LAS f32x4*)(r1 + L_WO1)) + (*(const LAS f32x4*)(r1 + L_WO0 + 16384) + *(const LAS f32x4*)(r1 + L_WO1 + 16384));
    const u32x4 hb = u32x4{pkbf(a0[0], a0[1]), pkbf(a0[2], a0[3]), pkbf(a1[0], a1[1]), pkbf(a1[2], a1[3])};
    float s1 = 0.f, s2 = 0.f, sd = 0.f;
#pragma unroll
    for (int e = 0; e < 4; ++e) { s1 = dot2bf(hb[e], 0x3F803F80u, s1); s2 = dot2bf(hb[e], hb[e], s2); sd = dot2bf(hb[e], wrv[e], sd); }
    s1 = sum8(s1); s2 = sum8(s2); sd = sum8(sd);
    if ((lane & 7) == 0)
      ST128(nosent4(u32x4{__float_as_uint(s1), __float_as_uint(s2), __float_as_uint(sd), 0u}), r_xs, (pm * 8 + cb) * 1024 + lr * 16);
#pragma unroll
    for (int e = 0; e < 4; ++e) { hv[2 * e] = bflo(hb[e]); hv[2 * e + 1] = bfhi(hb[e]); }
  }
  STAMP(1, 7);
  const __amdgpu_buffer_rsrc_t r_out = mkrsrc(out, 2u << 20);
  u32x4 k4;
  {
    u32x2 pv;
    for (unsigned spins = 0;; ++spins) {
      asm volatile("" ::: "memory");
      pv = __builtin_amdgcn_raw_buffer_load_b64(r_xs, (pm * 8 + (lane & 7)) * 1024 + (wid * 8 + (lane >> 3)) * 16, 0, SC1);
      if (__all(max(pv[0], pv[1]) != SENT) || spins > SPIN_CAP) break;
      __builtin_amdgcn_s_sleep(1);
    }
    STAMP(1, 8);
    const float mean = sum8(__uint_as_float(pv[0])) * (1.f / D_);
    const float var = __builtin_fmaxf(sum8(__uint_as_float(pv[1])) * (1.f / D_) - mean * mean, 0.f);
    const float rstd = __builtin_amdgcn_rsqf(var + LN_EPS);
    const float mr = mean * rstd;
    u32x4 hp, op;
#pragma unroll
    for (int e = 0; e < 4; ++e) {
      hp[e] = pkbf(hv[2 * e] * rstd - mr, hv[2 * e + 1] * rstd - mr);
      op[e] = pkbf(hv[2 * e] + bflo(hp[e]), hv[2 * e + 1] + bfhi(hp[e]));
    }
    ST128(op, r_out, row * 1024 + col * 2);
    if (isgu || isdn) k4 = LD128(r_key, tid * 16);
    WAIT_VM(0);
    ST128(nosent4(hp), r_hn, row * 1024 + col * 2);
  }
  STAMP(1, 9);
  if (!isgu && !isdn) { STAMP_FLUSH(1); return; }

  for (unsigned spins = 0; spins < SPIN_CAP; ++spins) {
    if (__all(umax4(k4) != SENT)) break;
    asm volatile("" ::: "memory");
    k4 = LD128(r_key, tid * 16);
  }
  STAMP(1, 10);
  {
    unsigned m = umax4(k4);
    m = max(m, dppu<0xB1>(m)); m = max(m, dppu<0x4E>(m)); m = max(m, dppu<0x141>(m)); m = max(m, dppu<0x140>(m));
    m = max(max(rdl(m, 0), rdl(m, 16)), max(rdl(m, 32), rdl(m, 48)));
    if (lane == 0) cand[64 + wid] = m;
  }
  LDS_BAR();
  {
    const u32x4 g0 = *(LAS u32x4*)(cand + 64), g1 = *(LAS u32x4*)(cand + 68);
    const unsigned T = min(min(min(g0[0], g0[1]), min(g0[2], g0[3])), min(min(g1[0], g1[1]), min(g1[2], g1[3])));
    unsigned base = 0;
#pragma unroll
    for (int e = 0; e < 4; ++e) {
      const bool p = k4[e] >= T;
      const unsigned long long mk = __ballot(p);
      const unsigned pos = __builtin_amdgcn_mbcnt_hi((unsigned)(mk >> 32), __builtin_amdgcn_mbcnt_lo((unsigned)mk, base));
      if (p) cand[pos < 8 ? wid * 8 + pos : 72] = k4[e];
      base += __builtin_popcountll(mk);
    }
  }
  LDS_BAR();
  LAS unsigned* sel = cand + 80 + wid * 8;
  {
    const unsigned c = cand[lane];
    const unsigned ovf = __builtin_amdgcn_readfirstlane(cand[72]);
    if (__builtin_expect(ovf != 0, 0)) {
      LAS unsigned* all = cand + 256;
      *(LAS u32x4*)(all + tid * 4) = k4;
      LDS_BAR();
      unsigned rk[4] = {0, 0, 0, 0};
#pragma unroll 1
      for (int j = 0; j < 2048; ++j) {
        const unsigned v = all[j];
#pragma unroll
        for (int e = 0; e < 4; ++e) rk[e] += v > k4[e];
      }
#pragma unroll
      for (int e = 0; e < 4; ++e) if (rk[e] < 8) cand[160 + rk[e]] = k4[e];
      LDS_BAR();
      sel = cand + 160;
    } else {
      unsigned long long act = __ballot(c != 0);
      unsigned long long ge;
      for (;;) {
        const int pl = __builtin_ctzll(act);
        const unsigned pv = rdl(c, pl);
        ge = __ballot(c >= pv);
        const int n = __builtin_popcountll(ge);
        if (n == 8) break;
        act &= n > 8 ? (ge & ~(1ull << pl)) : ~ge;
        if (act == 0) break;
      }
      const unsigned pos = __builtin_amdgcn_mbcnt_hi((unsigned)(ge >> 32), __builtin_amdgcn_mbcnt_lo((unsigned)ge, 0));
      if (((ge >> lane) & 1) && pos < 8) sel[pos] = c;
    }
  }
  const int tok = lane & 15;
  const unsigned key = sel[tok & 7];
  const int trow = 2047 - (int)(key & 2047u);
  STAMP(1, 11);
  const int trd = (8 * g + q) * 32 + 8 * p4;
  LAS f32x4* red = (LAS f32x4*)(lds + L_WO0);
  const __amdgpu_buffer_rsrc_t r_act = mkrsrc(ws + WS_ACT, 22016);
  if (isgu) {
    u32x4 xb[2], wg2[2], wu2[2];
#pragma unroll
    for (int u = 0; u < 2; ++u) xb[u] = LD128(r_hn, trow * 1024 + wid * 128 + u * 64 + g * 16);
#pragma unroll
    for (int u = 0; u < 2; ++u) { wg2[u] = ldstr2(lds + ffn_slot(wid * 2 + u) + trd, 128); wu2[u] = ldstr2(lds + ffn_slot(16 + wid * 2 + u) + trd, 128); }
    for (unsigned spins = 0; spins < SPIN_CAP; ++spins) {
      if (__all(max(umax4(xb[0]), umax4(xb[1])) != SENT)) break;
      asm volatile("" ::: "memory");
#pragma unroll
      for (int u = 0; u < 2; ++u) xb[u] = LD128(r_hn, trow * 1024 + wid * 128 + u * 64 + g * 16);
    }
    STAMP(1, 12);
    f32x4 ag = mfma16(wg2[0], xb[0], f32x4{}), au = mfma16(wu2[0], xb[0], f32x4{});
    ag = mfma16(wg2[1], xb[1], ag); au = mfma16(wu2[1], xb[1], au);
    red[wid * 64 + lane] = tok < 8 ? ag : au;
    LDS_BAR();
    if (wid == 0) {
      const f32x4 s = ((red[lane] + red[64 + lane]) + (red[128 + lane] + red[192 + lane])) + ((red[256 + lane] + red[320 + lane]) + (red[384 + lane] + red[448 + lane]));
      float av[4];
#pragma unroll
      for (int r = 0; r < 4; ++r) {
        const float gg = rbf(s[r]);
        const float uu = dppf<0x128>(gg);
        const float sl = rbf(gg * __builtin_amdgcn_rcpf(1.f + __expf(-gg)));
        av[r] = sl * uu;
      }
      if (tok < 8)
        __builtin_amdgcn_raw_buffer_store_b64(u32x2{nosent(pkbf(av[0], av[1])), nosent(pkbf(av[2], av[3]))}, r_act, b * 256 + tok * 32 + g * 8, 0, SC1);
    }
    STAMP(1, 13);
    STAMP_FLUSH(1);
    return;
  }
  const int nb = b - DN0;
  u32x2 o0 = u32x2{0, 0};
  float sg = 0.f;
  if (wid == 0) {
    for (unsigned spins = 0;; ++spins) {
      asm volatile("" ::: "memory");
      const u32x2 hc = __builtin_amdgcn_raw_buffer_load_b64(r_hn, trow * 1024 + nb * 32 + g * 8, 0, SC1);
      if (__all(max(hc[0], hc[1]) != SENT) || spins > SPIN_CAP) break;
    }
    o0 = __builtin_amdgcn_raw_buffer_load_b64(r_out, trow * 1024 + nb * 32 + g * 8, 0, SC1);
    const unsigned ov = key & 0xFFFFF800u;
    const float r = rbf(__uint_as_float((ov & 0x80000000u) ? (ov & 0x7FFFFFFFu) : ~(ov | 0x7FFu)));
    sg = rbf(__builtin_amdgcn_rcpf(1.f + __expf(-r)));
  }
  u32x4 ab[6], wd6[6];
#pragma unroll
  for (int p = 0; p < 6; ++p) wd6[p] = ldstr2(lds + ffn_slot(min(p * 8 + wid, 42)) + trd, 128);
  const int aoff = wid * 512 + (g >> 1) * 256 + (tok & 7) * 32 + (g & 1) * 16;
#pragma unroll
  for (int p = 0; p < 6; ++p) ab[p] = __builtin_amdgcn_raw_buffer_load_b128(r_act, aoff, p * 4096, SC1);
  for (unsigned spins = 0; spins < SPIN_CAP; ++spins) {
    bool all = true;
#pragma unroll
    for (int p = 0; p < 6; ++p) {
      if (__any(umax4(ab[p]) == SENT)) {
        asm volatile("" ::: "memory");
        ab[p] = __builtin_amdgcn_raw_buffer_load_b128(r_act, aoff, p * 4096, SC1);
        all = false;
      }
    }
    if (all) break;
  }
  STAMP(1, 12);
  f32x4 ay = f32x4{};
#pragma unroll
  for (int p = 0; p < 6; ++p) ay = mfma16(wd6[p], ab[p], ay);
  red[wid * 64 + lane] = ay;
  LDS_BAR();
  if (wid == 0 && tok < 8) {
    ay = ((ay + red[64 + lane]) + (red[128 + lane] + red[192 + lane])) + ((red[256 + lane] + red[320 + lane]) + (red[384 + lane] + red[448 + lane]));
    const float y0 = rbf(rbf(ay[0]) * sg), y1 = rbf(rbf(ay[1]) * sg), y2 = rbf(rbf(ay[2]) * sg), y3 = rbf(rbf(ay[3]) * sg);
    __builtin_amdgcn_raw_buffer_store_b64(u32x2{pkbf(bflo(o0[0]) + y0, bfhi(o0[0]) + y1), pkbf(bflo(o0[1]) + y2, bfhi(o0[1]) + y3)}, r_out,
                                          trow * 1024 + nb * 32 + g * 8, 0, SC1);
  }
  STAMP(1, 13);
}

extern "C" void kernel_launch(void* const* d_in, const int* in_sizes, int n_in, void* d_out, int out_size, void* d_ws, size_t ws_size,
                              hipStream_t stream) {
  const u16* x = (const u16*)d_in[0];
  const u16* wqkv = (const u16*)d_in[1];
  const u16* wo = (const u16*)d_in[2];
  const u16* wr = (const u16*)d_in[4];
  const u16* wg = (const u16*)d_in[5];
  const u16* wu = (const u16*)d_in[6];
  const u16* wd = (const u16*)d_in[7];
  char* ws = (char*)d_ws;
  u16* out = (u16*)d_out;
  (void)hipFuncSetAttribute((const void*)k1_qkv, hipFuncAttributeMaxDynamicSharedMemorySize, K1_LDS);
  (void)hipFuncSetAttribute((const void*)k2_fused, hipFuncAttributeMaxDynamicSharedMemorySize, K2_LDS);
  k1_qkv<<<256, 512, K1_LDS, stream>>>(x, wqkv, ws);
  k2_fused<<<256, 576, K2_LDS, stream>>>(x, wo, wr, wg, wu, wd, ws, out);
}
```

```cpp
#include <hip/hip_runtime.h>
#include <hip/hip_bf16.h>
#define STAMP_DECL
#define STAMP(s)
#define STAMP_FLUSH()
#define STAMP_ARG
#define STAMP_PASS

typedef unsigned short u16;
typedef __bf16 bf16x2_t __attribute__((ext_vector_type(2)));
typedef __bf16 bf16x8_t __attribute__((ext_vector_type(8)));
typedef __bf16 bf16x16_t __attribute__((ext_vector_type(16)));
typedef short s16x4 __attribute__((ext_vector_type(4)));
typedef float f32x4 __attribute__((ext_vector_type(4)));
typedef float f32x8 __attribute__((ext_vector_type(8)));
typedef float f32x16 __attribute__((ext_vector_type(16)));
typedef unsigned u32x2 __attribute__((ext_vector_type(2)));
typedef unsigned u32x4 __attribute__((ext_vector_type(4)));
#define LAS __attribute__((address_space(3)))

constexpr int D_ = 512, F_ = 1376;
constexpr unsigned SENT = 0xFFFFFFFFu;
constexpr float LN_EPS = 1e-5f;
constexpr float QSCALE = 0.25f * 1.4426950408889634f;
constexpr unsigned SPIN_CAP = 1u << 16;
constexpr int SC1 = 16;
constexpr unsigned ONE2 = 0x3F803F80u;

constexpr unsigned WS_KV = 0;
constexpr unsigned WS_QF = 4u << 20;
constexpr unsigned WS_OF = 6u << 20;
constexpr unsigned WS_HN = 8u << 20;
constexpr unsigned WS_XS = 10u << 20;
constexpr unsigned WS_ACT = WS_XS + (256u << 10);
constexpr unsigned WS_KEY = WS_ACT + 22016;
constexpr unsigned WS_FLAG = WS_XS + (320u << 10);
constexpr unsigned WS_FLAG2 = WS_FLAG + 32768;
constexpr unsigned WS_EPOCH = WS_FLAG2 + 32768;
constexpr unsigned WS_BYTES = WS_EPOCH + 128;

__device__ __forceinline__ f32x16 mfma32(u32x4 a, u32x4 b, f32x16 c) {
  return __builtin_amdgcn_mfma_f32_32x32x16_bf16(__builtin_bit_cast(bf16x8_t, a), __builtin_bit_cast(bf16x8_t, b), c, 0, 0, 0);
}
__device__ __forceinline__ f32x4 mfma16(u32x4 a, u32x4 b, f32x4 c) {
  return __builtin_amdgcn_mfma_f32_16x16x32_bf16(__builtin_bit_cast(bf16x8_t, a), __builtin_bit_cast(bf16x8_t, b), c, 0, 0, 0);
}
__device__ __forceinline__ f32x16 smfmac32(u32x4 a, f32x8 b, f32x16 c, int idx) {
  return __builtin_amdgcn_smfmac_f32_32x32x32_bf16(__builtin_bit_cast(bf16x8_t, a), __builtin_bit_cast(bf16x16_t, b), c, idx, 0, 0);
}
__device__ __forceinline__ float dot2bf(unsigned a, unsigned b, float c) {
  return __builtin_amdgcn_fdot2_f32_bf16(__builtin_bit_cast(bf16x2_t, a), __builtin_bit_cast(bf16x2_t, b), c, false);
}
__device__ __forceinline__ float swap32_sum(float v) {
  auto r = __builtin_amdgcn_permlane32_swap(__float_as_uint(v), __float_as_uint(v), false, false);
  return __uint_as_float(r[0]) + __uint_as_float(r[1]);
}
__device__ __forceinline__ float swap32_max(float v) {
  auto r = __builtin_amdgcn_permlane32_swap(__float_as_uint(v), __float_as_uint(v), false, false);
  return __builtin_fmaxf(__uint_as_float(r[0]), __uint_as_float(r[1]));
}
__device__ __forceinline__ unsigned pkbf(float lo, float hi) {
  bf16x2_t v; v[0] = (__bf16)lo; v[1] = (__bf16)hi; return __builtin_bit_cast(unsigned, v);
}
__device__ __forceinline__ float bflo(unsigned u) { return __uint_as_float(u << 16); }
__device__ __forceinline__ float bfhi(unsigned u) { return __uint_as_float(u & 0xFFFF0000u); }
__device__ __forceinline__ float rbf(float f) { return (float)(__bf16)f; }
__device__ __forceinline__ void glds16(const void* sbase, unsigned voff, unsigned lds_base) {
  unsigned sv;
  asm volatile("s_mov_b32 %0, m0\n\ts_mov_b32 m0, %3\n\ts_nop 0\n\tglobal_load_lds_dwordx4 %1, %2\n\ts_mov_b32 m0, %0"
               : "=&s"(sv) : "v"(voff), "s"(sbase), "s"(lds_base) : "memory");
}
__device__ __forceinline__ void glds16c(const void* sbase, unsigned voff, unsigned lds_base) {
  unsigned sv;
  asm volatile("s_mov_b32 %0, m0\n\ts_mov_b32 m0, %3\n\ts_nop 0\n\tglobal_load_lds_dwordx4 %1, %2 sc1\n\ts_mov_b32 m0, %0"
               : "=&s"(sv) : "v"(voff), "s"(sbase), "s"(lds_base) : "memory");
}
__device__ __forceinline__ void glds8c(const void* sbase, unsigned voff, unsigned lds_base) {
  unsigned sv;
  asm volatile("s_mov_b32 %0, m0\n\ts_mov_b32 m0, %3\n\ts_nop 0\n\t"
               "global_load_lds_dwordx4 %1, %2 offset:-4096 sc1\n\tglobal_load_lds_dwordx4 %1, %2 offset:-3072 sc1\n\t"
               "global_load_lds_dwordx4 %1, %2 offset:-2048 sc1\n\tglobal_load_lds_dwordx4 %1, %2 offset:-1024 sc1\n\t"
               "global_load_lds_dwordx4 %1, %2 sc1\n\tglobal_load_lds_dwordx4 %1, %2 offset:1024 sc1\n\t"
               "global_load_lds_dwordx4 %1, %2 offset:2048 sc1\n\tglobal_load_lds_dwordx4 %1, %2 offset:3072 sc1\n\t"
               "s_mov_b32 m0, %0"
               : "=&s"(sv) : "v"(voff), "s"((const char*)sbase + 4096), "s"(lds_base + 4096) : "memory");
}
__device__ __forceinline__ void glds4(const void* sbase, const unsigned (&voff)[4], unsigned lds_base) {
  unsigned sv;
  asm volatile("s_mov_b32 %0, m0\n\ts_mov_b32 m0, %6\n\ts_nop 0\n\t"
               "global_load_lds_dwordx4 %1, %5\n\tglobal_load_lds_dwordx4 %2, %5 offset:1024\n\t"
               "global_load_lds_dwordx4 %3, %5 offset:2048\n\tglobal_load_lds_dwordx4 %4, %5 offset:3072\n\t"
               "s_mov_b32 m0, %0"
               : "=&s"(sv) : "v"(voff[0]), "v"(voff[1]), "v"(voff[2]), "v"(voff[3]), "s"(sbase), "s"(lds_base) : "memory");
}
#define FENCE() asm volatile("" ::: "memory")
#define WAIT_BAR(N) asm volatile("s_waitcnt vmcnt(" #N ") lgkmcnt(0)\n\ts_barrier" ::: "memory")
#define VM_BAR(N) asm volatile("s_waitcnt vmcnt(" #N ")\n\ts_barrier" ::: "memory")
#define LDS_BAR() do { FENCE(); __builtin_amdgcn_s_waitcnt(0xC07F); __builtin_amdgcn_s_barrier(); FENCE(); } while (0)
#define BAR() do { FENCE(); __builtin_amdgcn_s_barrier(); FENCE(); } while (0)
#define WAIT_VM(N) asm volatile("s_waitcnt vmcnt(" #N ")" ::: "memory")
#define LD128(off, soff) __builtin_amdgcn_raw_buffer_load_b128(r_ws, off, soff, SC1)
#define LD64(off, soff) __builtin_amdgcn_raw_buffer_load_b64(r_ws, off, soff, SC1)
#define ST128(v, off, soff) __builtin_amdgcn_raw_buffer_store_b128(v, r_ws, (off) + (soff), 0, SC1)
#define ST64(v, off, soff) __builtin_amdgcn_raw_buffer_store_b64(v, r_ws, off, soff, SC1)
__device__ __forceinline__ u32x4 ldsr128(const LAS char* p) { return *(const LAS u32x4*)p; }
__device__ __forceinline__ u32x2 ldstr(const LAS char* p) {
  return __builtin_bit_cast(u32x2, __builtin_amdgcn_ds_read_tr16_b64_v4i16((LAS s16x4*)p));
}
__device__ __forceinline__ u32x4 ldstr2(const LAS char* p, int off2) { const u32x2 a = ldstr(p), b = ldstr(p + off2); return u32x4{a[0], a[1], b[0], b[1]}; }
__device__ __forceinline__ int swapb23(int v) { return (v & ~12) | ((v & 4) << 1) | ((v & 8) >> 1); }
__device__ __forceinline__ unsigned nosent(unsigned v) { return min(v, 0xFFFFFFFEu); }
__device__ __forceinline__ u32x4 nosent4(u32x4 v) { return u32x4{nosent(v[0]), nosent(v[1]), nosent(v[2]), nosent(v[3])}; }
__device__ __forceinline__ unsigned umax4(u32x4 v) { return max(max(v[0], v[1]), max(v[2], v[3])); }
__device__ __forceinline__ __amdgpu_buffer_rsrc_t mkrsrc(const void* p, unsigned bytes) {
  return __builtin_amdgcn_make_buffer_rsrc((void*)p, 0, bytes, 0x00020000);
}
template <int CTRL> __device__ __forceinline__ float dppf(float v) {
  return __uint_as_float(__builtin_amdgcn_update_dpp(0, __float_as_uint(v), CTRL, 0xF, 0xF, true));
}
template <int CTRL> __device__ __forceinline__ unsigned dppu(unsigned v) { return __builtin_amdgcn_update_dpp(0, v, CTRL, 0xF, 0xF, true); }
__device__ __forceinline__ unsigned rdl(unsigned v, int l) { return (unsigned)__builtin_amdgcn_readlane((int)v, l); }
__device__ __forceinline__ float rdlf(float v, int l) { return __uint_as_float(rdl(__float_as_uint(v), l)); }
__device__ __forceinline__ float sum8(float v) { v += dppf<0xB1>(v); v += dppf<0x4E>(v); v += dppf<0x141>(v); return v; }
__device__ __forceinline__ u32x4 pack8(const float* v) { return u32x4{pkbf(v[0], v[1]), pkbf(v[2], v[3]), pkbf(v[4], v[5]), pkbf(v[6], v[7])}; }

constexpr int K1_SLOT = 16384 + 12288, K1_CS = 4 * K1_SLOT, K1_CSP = K1_CS + 512, K1_TAG = K1_CSP + 8 * 384;
constexpr int L_Q = 0, L_KV = 24576, L_ONE = L_KV + 131072, LDS_BYTES = 163840;
constexpr int L_F = 0, L_WO = 45056, L_SEL = 114688;
constexpr int NGU = 86, DN0 = 128, NDN = 32;

template <int NV>
__device__ __forceinline__ void qkv_body(const __amdgpu_buffer_rsrc_t r_ws, LAS char* lds, int rb, int cb, int wid STAMP_ARG) {
  const int lane = threadIdx.x & 63, t = lane & 31, h = lane >> 5;
  int xr[4];
#pragma unroll
  for (int u = 0; u < 4; ++u) xr[u] = wid * 4096 + t * 128 + (((2 * u + h) ^ ((t >> 1) & 7)) * 16);
  const int wrd = 16384 + (8 * h + ((lane >> 2) & 3)) * 192 + 32 * ((lane >> 4) & 1) + 8 * (lane & 3);
  f32x16 acc[3];
#pragma unroll
  for (int j = 0; j < 3; ++j) acc[j] = f32x16{};
  float s1a = 0.f, s1b = 0.f, s2a = 0.f, s2b = 0.f;
  u32x4 xf = u32x4{0, 0, 0, 0}, wf[3];
#pragma unroll
  for (int j = 0; j < 3; ++j) wf[j] = u32x4{0, 0, 0, 0};
  auto mm = [&]() {
#pragma unroll
    for (int j = 0; j < 3; ++j) {
      if (j >= 3 - NV) acc[j] = mfma32(xf, wf[j], acc[j]);
      else acc[j] = mfma32(wf[j], xf, acc[j]);
    }
  };
  const int tile = rb * 4 + wid;
  const int voffV = tile * 2048 + 1024 + (t >> 4) * 131072 + h * 256 + swapb23(t & 15) * 16;
  const int voffK = tile * 2048 + swapb23(t) * 32 + h * 16;
  const int voffQ = tile * 1024 + t * 32 + h * 16;
#pragma unroll 1
  for (int c = 0; c < 8; ++c) {
    LDS_BAR();
    if (c == 0) STAMP(1);
    const LAS char* sb = lds + (c & 3) * K1_SLOT;
    u32x4 xn[4];
#pragma unroll
    for (int u = 0; u < 4; ++u) {
      xn[u] = ldsr128(sb + xr[u]);
      u32x4 wn[3];
#pragma unroll
      for (int j = 0; j < 3; ++j) wn[j] = ldstr2(sb + wrd + u * 3072 + j * 64, 768);
      mm();
#pragma unroll
      for (int z = 0; z < 3; ++z) { __builtin_amdgcn_sched_group_barrier(0x8, 1, 0); __builtin_amdgcn_sched_group_barrier(0x102, 3, 0); }
      __builtin_amdgcn_sched_barrier(0);
      xf = xn[u];
#pragma unroll
      for (int j = 0; j < 3; ++j) wf[j] = wn[j];
    }
#pragma unroll
    for (int u = 0; u < 4; ++u)
#pragma unroll
      for (int e = 0; e < 4; e += 2) {
        s1a = dot2bf(xn[u][e], ONE2, s1a); s2a = dot2bf(xn[u][e], xn[u][e], s2a);
        s1b = dot2bf(xn[u][e + 1], ONE2, s1b); s2b = dot2bf(xn[u][e + 1], xn[u][e + 1], s2b);
      }
    asm volatile("" : "+v"(s1a), "+v"(s1b), "+v"(s2a), "+v"(s2b));
    __builtin_amdgcn_sched_barrier(0);
  }
  __builtin_amdgcn_s_barrier();
  mm();
  STAMP(2);
  LAS float* scr = (LAS float*)(lds + wid * 4096);
  const LAS float* csum = (const LAS float*)(lds + K1_CS);
  const float s2 = swap32_sum(s2a + s2b);
  const float mean = swap32_sum(s1a + s1b) * (1.f / D_);
  const float var = __builtin_fmaxf(s2 * (1.f / D_) - mean * mean, 0.f);
  const float rstd = __builtin_amdgcn_rsqf(var + LN_EPS);
  if (NV > 0 && h == 0) { scr[t] = rstd; scr[32 + t] = mean * rstd; }
  BAR();
#pragma unroll
  for (int j = 0; j < 3; ++j) {
    const int gt = cb * 3 + j;
    float v[16];
    if (j >= 3 - NV) {
      const float cs = csum[32 * j + t];
#pragma unroll
      for (int gq = 0; gq < 4; ++gq) {
        const f32x4 a = *(const LAS f32x4*)(scr + 8 * gq + 4 * h), b = *(const LAS f32x4*)(scr + 32 + 8 * gq + 4 * h);
#pragma unroll
        for (int e = 0; e < 4; ++e) v[4 * gq + e] = acc[j][4 * gq + e] * a[e] - b[e] * cs;
      }
      const int so = WS_KV + (gt - 32) * 262144;
      ST128(pack8(v), voffV, so); ST128(pack8(v + 8), voffV + 512, so);
    } else {
      const bool isq = gt < 16;
      const float sc = isq ? QSCALE : 1.f;
      const float a = rstd * sc, b = mean * rstd * sc;
#pragma unroll
      for (int gq = 0; gq < 4; ++gq) {
        const f32x4 c4 = *(const LAS f32x4*)(csum + 32 * j + 8 * gq + 4 * h);
#pragma unroll
        for (int e = 0; e < 4; ++e) v[4 * gq + e] = acc[j][4 * gq + e] * a - b * c4[e];
      }
      const int vo = isq ? voffQ : voffK;
      const int so = isq ? WS_QF + gt * 131072 : WS_KV + (gt - 16) * 262144;
      ST128(pack8(v), vo, so); ST128(pack8(v + 8), vo, so + (isq ? 65536 : 131072));
    }
  }
  STAMP(3);
}

__device__ __forceinline__ float rowmax16(const f32x16& s) {
  float a = __builtin_fmaxf(__builtin_fmaxf(s[0], s[1]), s[2]), b = __builtin_fmaxf(__builtin_fmaxf(s[3], s[4]), s[5]);
  a = __builtin_fmaxf(__builtin_fmaxf(a, s[6]), s[7]); b = __builtin_fmaxf(__builtin_fmaxf(b, s[8]), s[9]);
  a = __builtin_fmaxf(__builtin_fmaxf(a, s[10]), s[11]); b = __builtin_fmaxf(__builtin_fmaxf(b, s[12]), s[13]);
  a = __builtin_fmaxf(__builtin_fmaxf(a, s[14]), s[15]);
  return swap32_max(__builtin_fmaxf(a, b));
}
__device__ __forceinline__ f32x8 lo8(const f32x16& s) { return __builtin_shufflevector(s, s, 0, 1, 2, 3, 4, 5, 6, 7); }
__device__ __forceinline__ f32x8 hi8(const f32x16& s) { return __builtin_shufflevector(s, s, 8, 9, 10, 11, 12, 13, 14, 15); }

#define E1(X, i) "v_exp_f32 v[" #X "+" #i "], v[" #X "+" #i "]\n\t"
#define SMF(A, B, off) "v_smfmac_f32_32x32x32_bf16 v[0:15], v[" #A ":" #A "+3], v[" #B "+" #off ":" #B "+" #off "+7], v88\n\t"
#define QK(Y) "v_mfma_f32_32x32x16_bf16 v[" #Y ":" #Y "+15], v[68:71], v[64:67], v[16:31]\n\t"
#define RDV(D, off) "ds_read_b128 v[" #D ":" #D "+3], v90 offset:" #off "\n\t"
#define RDK(off) "ds_read_b128 v[68:71], v89 offset:" #off "\n\t"
#define STEP(X, Y, VA, VPB, VA2, VB2, KOFF, VOFF, VOFF2, PA, PB, EXTRA) \
  PA E1(X, 0) E1(X, 1) SMF(VPB, Y, 8) RDV(VA2, VOFF) \
  PB E1(X, 2) E1(X, 3) E1(X, 4) E1(X, 5) \
  "s_waitcnt lgkmcnt(1)\n\t" QK(Y) RDV(VB2, VOFF2) RDK(KOFF) \
  E1(X, 6) E1(X, 7) E1(X, 8) E1(X, 9) \
  SMF(VA, X, 0) EXTRA \
  E1(X, 10) E1(X, 11) E1(X, 12) E1(X, 13) E1(X, 14) E1(X, 15)
#define ATTN_LOOP(L1, L2, PHI, PLO) \
        L1 ":\n\t" \
        "s_bitcmp1_b32 0x824, %[cnt]\n\t" \
        "s_cbranch_scc0 " L2 "f\n\t" \
        "s_barrier\n\t" \
        L2 ":\n\t" \
        STEP(32, 48, 72, 84, 80, 84, 4096, 2048, 2560, PHI, "", "") \
        STEP(48, 32, 80, 76, 72, 76, 6144, 4096, 4608, "", "", "") \
        STEP(32, 48, 72, 84, 80, 84, 8192, 6144, 6656, "", "", "v_add_u32 v90, v90, v91\n\t") \
        STEP(48, 32, 80, 76, 72, 76, 10240, 0, 512, "", PLO, "v_add_u32 v89, 0x2000, v89\n\ts_add_u32 %[cnt], %[cnt], 1\n\ts_cmp_lg_u32 %[cnt], 16\n\t") \
        "s_cbranch_scc1 " L1 "b\n\t"

__global__ __launch_bounds__(576, 3) void fused_block(const u16* __restrict__ x, const u16* __restrict__ wqkv, const u16* __restrict__ wo,
                                                      const u16* __restrict__ wr, const u16* __restrict__ wg, const u16* __restrict__ wu,
                                                      const u16* __restrict__ wd, char* __restrict__ ws, u16* __restrict__ out) {
  extern __shared__ __attribute__((aligned(16))) char smem[];
  LAS char* lds = (LAS char*)smem;
  const unsigned lds0 = (unsigned)(uintptr_t)lds;
  const int tid = threadIdx.x, lane = tid & 63, t = lane & 31, h = lane >> 5;
  const int wid = __builtin_amdgcn_readfirstlane(tid >> 6);
  const int b = blockIdx.x;
  const int rb1 = 4 * (b & 3) + ((b >> 3) & 3), cb1 = 8 * ((b >> 2) & 1) + (b >> 5);
  const int head = (b & 7) * 4 + ((b >> 3) & 3), qb = b >> 5;
  const int pm = head, cb = qb;
  const unsigned l16 = lane * 16;
  const bool isgu = b < NGU, isdn = b >= DN0 && b < DN0 + NDN;
  const __amdgpu_buffer_rsrc_t r_ws = mkrsrc(ws, WS_BYTES);
  STAMP_DECL STAMP(0);

  if (wid == 8) {
    __builtin_amdgcn_s_setprio(3);
    const u32x2 ep = LD64(0, WS_EPOCH);
    const u32x4 w4 = *(const u32x4*)(wr + lane * 8);
    if (lane < 16) { const unsigned v = (lane & 1) ? 0u : ONE2; *(LAS u32x4*)(lds + L_ONE + (lane >> 2) * 2048 + ((lane >> 1) & 1) * 512 + (lane & 1) * 16) = u32x4{v, v, v, v}; }
#pragma unroll 1
    for (int c = 0; c < 5; ++c) BAR();
    const unsigned long long e64 = (((unsigned long long)ep[1] << 32) | ep[0]) + 1ull;
    const u32x2 tag = u32x2{(unsigned)e64, (unsigned)(e64 >> 32)};
    float sw;
    sw = dot2bf(w4[0], ONE2, dot2bf(w4[1], ONE2, 0.f)) + dot2bf(w4[2], ONE2, dot2bf(w4[3], ONE2, 0.f));
    sw = sum8(sw); sw += dppf<0x140>(sw);
    sw = (rdlf(sw, 0) + rdlf(sw, 16)) + (rdlf(sw, 32) + rdlf(sw, 48));
    const int hp = head >> 1;
    const int fidx = lane < 16 ? lane * 16 + (16 + hp) / 3 : lane < 32 ? (lane - 16) * 16 + (32 + hp) / 3 : (qb * 2 + (lane & 1)) * 16 + hp / 3;
    const char* kvsrc = ws + WS_KV + (size_t)head * 131072;
    LDS_BAR();
#pragma unroll 1
    for (int c = 6; c < 10; ++c) BAR();
    BAR();
    if (lane == 0) ST64(tag, 0, WS_FLAG + (rb1 * 16 + cb1) * 128);
    STAMP(1);
    unsigned long long seen = 0;
    auto wait_flags = [&](unsigned long long need) {
#pragma unroll 1
      for (unsigned spins = 0; spins < SPIN_CAP && (seen & need) != need; ++spins) {
        FENCE();
        const u32x2 f = LD64(fidx * 128, WS_FLAG);
        seen = __ballot((f[0] == tag[0]) & (f[1] == tag[1]));
      }
      FENCE();
    };
    wait_flags(0x300070007ull);
    STAMP(5);
    glds8c(ws + WS_QF + head * 65536 + qb * 8192, l16, lds0 + L_Q);
#pragma unroll
    for (int p = 0; p < 3; ++p) glds8c(kvsrc + p * 8192, l16, lds0 + L_KV + p * 8192);
    VM_BAR(0);
    wait_flags(0xFFFFFFFFull);
#pragma unroll 1
    for (int p = 3; p < 10; ++p) glds8c(kvsrc + p * 8192, l16, lds0 + L_KV + p * 8192);
    VM_BAR(32);
    WAIT_VM(8);
#pragma unroll 1
    for (int p = 10; p < 16; ++p) glds8c(kvsrc + p * 8192, l16, lds0 + L_KV + p * 8192);
    VM_BAR(0);
    {
      const u32x4 s4 = u32x4{SENT, SENT, SENT, SENT};
#pragma unroll
      for (int e = 0; e < 8; ++e) ST128(s4, l16, WS_OF + head * 65536 + qb * 8192 + e * 1024);
#pragma unroll
      for (int e = 0; e < 8; ++e) ST128(s4, (lane >> 3) * 1024 + (lane & 7) * 16, WS_HN + (pm * 64 + e * 8) * 1024 + cb * 128);
      ST128(s4, l16, WS_XS + (pm * 8 + cb) * 1024);
      if (lane < 8 && b * 8 + lane < (22016 + 8192) / 16) ST128(s4, l16, WS_ACT + b * 128);
      WAIT_VM(0);
      if (lane == 0) ST64(tag, 0, WS_FLAG2 + b * 128);
    }
    unsigned foff[4];
    const unsigned fstr = isgu ? 32 * F_ * 2 : 32768;
#pragma unroll
    for (int j = 0; j < 4; ++j)
      foff[j] = (isgu ? (unsigned)((lane >> 1) * (F_ * 2) + b * 32) : (unsigned)((lane >> 1) * 1024 + (b - DN0) * 32)) + (lane & 1) * 16 + j * (fstr - 1024);
    auto ffn = [&](int q0, int q1) {
      if (isgu) {
#pragma unroll 1
        for (int p = q0; p < min(q1, 8); ++p) glds4((const char*)(p < 4 ? wg : wu) + (p & 3) * (128 * F_ * 2), foff, lds0 + L_F + p * 4096);
      } else if (isdn) {
#pragma unroll 1
        for (int p = q0; p < min(q1, 10); ++p) glds4((const char*)wd + p * 131072, foff, lds0 + L_F + p * 4096);
        if (q1 > 10) {
#pragma unroll 1
          for (int p = 40; p < 43; ++p) glds16((const char*)wd + p * 32768, foff[0], lds0 + L_F + p * 1024);
        }
      }
    };
    ffn(0, 6);
#pragma unroll 1
    for (unsigned spins = 0; spins < SPIN_CAP; ++spins) {
      FENCE();
      bool ok = true;
#pragma unroll
      for (int e = 0; e < 4; ++e) { const u32x2 f = LD64(lane * 128, WS_FLAG2 + e * 8192); ok &= (f[0] == tag[0]) & (f[1] == tag[1]); }
      if (__all(ok)) break;
      __builtin_amdgcn_s_sleep(16);
    }
    if (b == 0 && lane == 0) ST64(tag, 0, WS_EPOCH);
    unsigned wooff[4];
#pragma unroll
    for (int j = 0; j < 4; ++j) wooff[j] = (unsigned)((lane >> 3) * 1024 + cb * 128 + (((lane & 7) ^ (((lane >> 4) & 1) << 2)) * 16) + j * 7168);
    VM_BAR(0);
#pragma unroll 1
    for (int p = 0; p < 8; ++p) glds4((const char*)wo + p * 32768, wooff, lds0 + L_WO + p * 4096);
    WAIT_VM(8);
#pragma unroll 1
    for (int p = 8; p < 16; ++p) glds4((const char*)wo + p * 32768, wooff, lds0 + L_WO + p * 4096);
    ffn(6, 11);
    WAIT_VM(0);
    STAMP(2);
    BAR();
    if (cb != 0) { STAMP_FLUSH(); return; }
    BAR(); BAR();
    u32x4 pv[8];
    for (unsigned spins = 0;; ++spins) {
      FENCE();
      unsigned mx = 0;
#pragma unroll
      for (int e = 0; e < 8; ++e) pv[e] = LD128(l16, WS_XS + (pm * 8 + e) * 1024);
#pragma unroll
      for (int e = 0; e < 8; ++e) mx = max(mx, umax4(pv[e]));
      if (__all(mx != SENT) || spins > SPIN_CAP) break;
    }
    STAMP(3);
#define SUM8(c) (((__uint_as_float(pv[0][c]) + __uint_as_float(pv[1][c])) + (__uint_as_float(pv[2][c]) + __uint_as_float(pv[3][c]))) + \
                 ((__uint_as_float(pv[4][c]) + __uint_as_float(pv[5][c])) + (__uint_as_float(pv[6][c]) + __uint_as_float(pv[7][c]))))
    const float s1 = SUM8(0), s2 = SUM8(1), sd = SUM8(2);
    const float mean = s1 * (1.f / D_);
    const float rstd = __builtin_amdgcn_rsqf(__builtin_fmaxf(s2 * (1.f / D_) - mean * mean, 0.f) + LN_EPS);
    const unsigned u = __float_as_uint(rstd * (sd - mean * sw));
    const unsigned ordv = (u & 0x80000000u) ? ~u : (u | 0x80000000u);
    __builtin_amdgcn_raw_buffer_store_b32(max(min(ordv & 0xFFFFF800u, 0xFFFFF000u), 0x800u) | (unsigned)(2047 - (pm * 64 + lane)), r_ws, lane * 4, WS_KEY + pm * 256, SC1);
    STAMP(4);
    STAMP_FLUSH();
    return;
  }

  if (wid >= 4) {
    const int i = b >> 3, rot = (((i >> 2) + (i & 3)) & 1) * 4;
    const int lw = wid - 4;
    unsigned xoff[2], woff[3];
#pragma unroll
    for (int p = 0; p < 2; ++p) {
      const int row = p * 8 + (lane >> 3), slot = lane & 7;
      xoff[p] = (unsigned)((rb1 * 128 + lw * 32 + row) * 1024 + ((slot ^ ((row >> 1) & 7)) * 16));
    }
#pragma unroll
    for (int p = 0; p < 3; ++p) {
      const int idx = (lw * 3 + p) * 64 + lane, k = idx / 12, c = idx - k * 12;
      woff[p] = (unsigned)(k * 3072 + cb1 * 192 + c * 16);
    }
    auto stage = [&](int c) {
      const int kc = (c + rot) & 7;
      const char* xs = (const char*)x + kc * 128;
      const char* wsrc = (const char*)wqkv + kc * (64 * 3072);
      const unsigned base = lds0 + (c & 3) * K1_SLOT;
#pragma unroll
      for (int p = 0; p < 4; ++p) glds16(xs + (p >> 1) * 16384, xoff[p & 1], base + lw * 4096 + p * 1024);
#pragma unroll
      for (int p = 0; p < 3; ++p) glds16(wsrc, woff[p], base + 16384 + (lw * 3 + p) * 1024);
    };
    stage(0); stage(1);
    const int wrd = 16384 + lw * 3072 + (8 * h + ((lane >> 2) & 3)) * 192 + 32 * ((lane >> 4) & 1) + 8 * (lane & 3);
    float cs[3] = {0.f, 0.f, 0.f};
#pragma unroll 1
    for (int c = 0; c < 8; ++c) {
      if (c < 7) WAIT_BAR(7); else WAIT_BAR(0);
      const LAS char* sb = lds + (c & 3) * K1_SLOT + wrd;
      u32x4 wf[3];
#pragma unroll
      for (int j = 0; j < 3; ++j) wf[j] = ldstr2(sb + j * 64, 768);
      if (c < 6) stage(c + 2);
#pragma unroll
      for (int e = 0; e < 4; ++e)
#pragma unroll
        for (int j = 0; j < 3; ++j) cs[j] = dot2bf(wf[j][e], ONE2, cs[j]);
    }
#pragma unroll
    for (int j = 0; j < 3; ++j) *(LAS float*)(lds + K1_CSP + (lw * 2 + h) * 384 + (32 * j + t) * 4) = cs[j];
    LDS_BAR();
    if (lw == 0 && lane < 24) {
      const LAS char* pp = lds + K1_CSP + l16;
      f32x4 sacc = *(const LAS f32x4*)pp;
#pragma unroll
      for (int e = 1; e < 8; ++e) sacc += *(const LAS f32x4*)(pp + e * 384);
      *(LAS f32x4*)(lds + K1_CS + l16) = sacc;
    }
    LDS_BAR();
    BAR();
  } else {
    if (cb1 < 10) qkv_body<0>(r_ws, lds, rb1, cb1, wid STAMP_PASS);
    else if (cb1 == 10) qkv_body<1>(r_ws, lds, rb1, cb1, wid STAMP_PASS);
    else qkv_body<3>(r_ws, lds, rb1, cb1, wid STAMP_PASS);
    WAIT_BAR(0);
    STAMP(4);
  }

  const bool onel = (lane & 16) != 0;
  unsigned kaddr = lds0 + L_KV + t * 32 + h * 16;
  unsigned vaddr = lds0 + (onel ? L_ONE + ((lane & 11) ? 16 : 0) : L_KV + 1024 + h * 256 + (lane & 15) * 16);
  const int vstep = onel ? 0 : 8192;
  const int IDX = 0xDDDDDDDD;
  const int row = pm * 64 + wid * 8 + (lane >> 3), col = cb * 64 + (lane & 7) * 8;
  f32x16 negm, sx, sy = f32x16{}, o = f32x16{};
  u32x4 vpb = u32x4{0, 0, 0, 0};
  asm volatile("" : "+{v[0:15]}"(o), "+{v[48:63]}"(sy), "+{v[84:87]}"(vpb));

  BAR();
  STAMP(5);
  const u32x4 xres = *(const u32x4*)(x + row * D_ + col), wrv = *(const u32x4*)(wr + col);
  const u32x4 qf = ldsr128(lds + L_Q + wid * 1024 + t * 32 + h * 16);
  u32x4 kn, va, vb;
  {
    const u32x4 k0 = ldsr128((const LAS char*)(uintptr_t)kaddr);
    kn = ldsr128((const LAS char*)(uintptr_t)kaddr + 2048);
    va = ldsr128((const LAS char*)(uintptr_t)vaddr); vb = ldsr128((const LAS char*)(uintptr_t)vaddr + 512);
    const float m = -rowmax16(mfma32(k0, qf, f32x16{}));
#pragma unroll
    for (int r = 0; r < 16; ++r) negm[r] = m;
    sx = mfma32(k0, qf, negm);
  }
  STAMP(6);
  {
    unsigned cnt;
    asm volatile(
        "s_mov_b32 %[cnt], 0\n\t"
        "s_nop 7\n\t"
        "s_cmp_gt_u32 %[wid], 3\n\t"
        "s_cbranch_scc1 3f\n\t"
        "s_setprio 1\n\t"
        ATTN_LOOP("1", "2", "", "")
        "s_setprio 0\n\t"
        "s_branch 5f\n\t"
        ATTN_LOOP("3", "4", "s_setprio 2\n\t", "s_setprio 0\n\t")
        "5:\n\t"
        "s_nop 1\n\t"
        SMF(84, 48, 8)
        "s_waitcnt lgkmcnt(0)\n\t"
        "s_nop 11\n\t"
        : "+{v[0:15]}"(o), "+{v[32:47]}"(sx), "+{v[48:63]}"(sy), "+{v[68:71]}"(kn), "+{v[72:75]}"(va), "+{v[76:79]}"(vb),
          "+{v[84:87]}"(vpb), "+{v89}"(kaddr), "+{v90}"(vaddr), [cnt] "=&s"(cnt)
        : "{v[16:31]}"(negm), "{v[64:67]}"(qf), "{v88}"(IDX), "{v91}"(vstep), [wid] "s"(wid)
        : "v80", "v81", "v82", "v83", "scc", "memory");
  }
  STAMP(7);
  {
    const float chk = ((__builtin_fabsf(o[0]) + __builtin_fabsf(o[1])) + (__builtin_fabsf(o[2]) + __builtin_fabsf(o[3]))) +
                      ((__builtin_fabsf(o[4]) + __builtin_fabsf(o[5])) + (__builtin_fabsf(o[6]) + __builtin_fabsf(o[7])));
    const bool bad = !(o[8] < 1.8446744e19f) || !(chk < 3.0e38f);
    if (__builtin_expect(__any(bad), 0)) {
      float m = -INFINITY;
      o = f32x16{};
      const int kq = head * 131072 + t * 32 + h * 16, vq = head * 131072 + 1024 + h * 256 + (lane & 15) * 16;
      const u32x4 ones = u32x4{ONE2, ONE2, ONE2, ONE2};
#pragma unroll 1
      for (int i = 0; i < 64; ++i) {
        f32x16 s = mfma32(LD128(kq, WS_KV + i * 2048), qf, f32x16{});
        u32x4 v0 = LD128(vq, WS_KV + i * 2048), v1 = LD128(vq + 512, WS_KV + i * 2048);
        if (onel) { v0 = ones; v1 = ones; }
        const float mn = __builtin_fmaxf(m, rowmax16(s));
        const float al = __builtin_amdgcn_exp2f(m - mn);
        m = mn;
#pragma unroll
        for (int r = 0; r < 16; ++r) { o[r] *= al; s[r] = __builtin_amdgcn_exp2f(s[r] - mn); }
        o = smfmac32(v0, lo8(s), o, IDX);
        o = smfmac32(v1, hi8(s), o, IDX);
      }
    }
  }
  {
    const float inv = __builtin_amdgcn_rcpf(o[8]);
    ST128(nosent4(u32x4{pkbf(o[0] * inv, o[1] * inv), pkbf(o[2] * inv, o[3] * inv), pkbf(o[4] * inv, o[5] * inv), pkbf(o[6] * inv, o[7] * inv)}),
          t * 32 + h * 16, WS_OF + head * 65536 + (qb * 8 + wid) * 1024);
  }
  BAR();
  STAMP(8);

  const int mh = wid & 1, kq = wid >> 1;
  const int g = lane >> 4, q = (lane >> 2) & 3, p4 = lane & 3;
  LAS unsigned* cand = (LAS unsigned*)(lds + L_SEL);
  {
    u32x4 of[8], wf0[8], wf1[8];
    const int flip = ((q >> 1) & 1) << 6;
    const LAS char* wb = lds + L_WO + kq * 16384 + (8 * h + q) * 128 + 32 * (g & 1) + 8 * p4;
#pragma unroll
    for (int e = 0; e < 8; ++e) { wf0[e] = ldstr2(wb + flip + e * 2048, 512); wf1[e] = ldstr2(wb + (64 ^ flip) + e * 2048, 512); }
    const int ooff = t * 32 + h * 16, osoff = WS_OF + kq * 8 * 65536 + pm * 2048 + mh * 1024;
#pragma unroll
    for (int e = 0; e < 8; ++e) of[e] = LD128(ooff, osoff + e * 65536);
    if (tid < 80) cand[tid] = 0;
    LDS_BAR();
    for (unsigned spins = 0; spins < SPIN_CAP; ++spins) {
      bool miss[8], any = false;
#pragma unroll
      for (int e = 0; e < 8; ++e) { miss[e] = __any(umax4(of[e]) == SENT); any |= miss[e]; }
      if (!any) break;
      FENCE();
#pragma unroll
      for (int e = 0; e < 8; ++e) if (miss[e]) of[e] = LD128(ooff, osoff + e * 65536);
    }
    STAMP(9);
    f32x16 acc0 = f32x16{}, acc1 = f32x16{};
#pragma unroll
    for (int e = 0; e < 8; ++e) { acc0 = mfma32(wf0[e], of[e], acc0); acc1 = mfma32(wf1[e], of[e], acc1); }
    LAS char* rb = lds + L_WO + kq * 16384 + (mh * 32 + t) * 256;
    const int swz = (t & 7) | ((t & 1) << 3);
#pragma unroll
    for (int gq = 0; gq < 4; ++gq) {
      *(LAS f32x4*)(rb + (((h * 8 + gq) ^ swz) * 16)) = f32x4{acc0[4 * gq], acc0[4 * gq + 1], acc0[4 * gq + 2], acc0[4 * gq + 3]};
      *(LAS f32x4*)(rb + (((h * 8 + 4 + gq) ^ swz) * 16)) = f32x4{acc1[4 * gq], acc1[4 * gq + 1], acc1[4 * gq + 2], acc1[4 * gq + 3]};
    }
  }
  LDS_BAR();
  STAMP(10);
  float hv[8];
  {
    const int lr = wid * 8 + (lane >> 3);
    const int sl = ((lane & 7) ^ ((lr & 7) | ((lr & 1) << 3))) * 16;
    const LAS char* r0 = lds + L_WO + lr * 256 + sl;
    const LAS char* r1 = lds + L_WO + lr * 256 + (sl ^ 128);
    f32x4 a0 = f32x4{bflo(xres[0]), bfhi(xres[0]), bflo(xres[1]), bfhi(xres[1])}, a1 = f32x4{bflo(xres[2]), bfhi(xres[2]), bflo(xres[3]), bfhi(xres[3])};
    a0 += (*(const LAS f32x4*)r0 + *(const LAS f32x4*)(r0 + 16384)) + (*(const LAS f32x4*)(r0 + 32768) + *(const LAS f32x4*)(r0 + 49152));
    a1 += (*(const LAS f32x4*)r1 + *(const LAS f32x4*)(r1 + 16384)) + (*(const LAS f32x4*)(r1 + 32768) + *(const LAS f32x4*)(r1 + 49152));
    const u32x4 hb = u32x4{pkbf(a0[0], a0[1]), pkbf(a0[2], a0[3]), pkbf(a1[0], a1[1]), pkbf(a1[2], a1[3])};
    float s1 = 0.f, s2 = 0.f, sd = 0.f;
#pragma unroll
    for (int e = 0; e < 4; ++e) { s1 = dot2bf(hb[e], ONE2, s1); s2 = dot2bf(hb[e], hb[e], s2); sd = dot2bf(hb[e], wrv[e], sd); }
    s1 = sum8(s1); s2 = sum8(s2); sd = sum8(sd);
    if ((lane & 7) == 0)
      ST128(nosent4(u32x4{__float_as_uint(s1), __float_as_uint(s2), __float_as_uint(sd), 0u}), lane * 2, WS_XS + (pm * 8 + cb) * 1024 + wid * 128);
#pragma unroll
    for (int e = 0; e < 4; ++e) { hv[2 * e] = bflo(hb[e]); hv[2 * e + 1] = bfhi(hb[e]); }
  }
  STAMP(11);
  const __amdgpu_buffer_rsrc_t r_out = mkrsrc(out, 2u << 20);
  u32x4 k4;
  {
    u32x2 pv;
    for (unsigned spins = 0;; ++spins) {
      FENCE();
      pv = LD64((lane & 7) * 1024 + (lane >> 3) * 16, WS_XS + pm * 8192 + wid * 128);
      if (__all(max(pv[0], pv[1]) != SENT) || spins > SPIN_CAP) break;
      __builtin_amdgcn_s_sleep(2);
    }
    STAMP(12);
    const float mean = sum8(__uint_as_float(pv[0])) * (1.f / D_);
    const float var = __builtin_fmaxf(sum8(__uint_as_float(pv[1])) * (1.f / D_) - mean * mean, 0.f);
    const float rstd = __builtin_amdgcn_rsqf(var + LN_EPS);
    const float mr = mean * rstd;
    u32x4 hp, op;
#pragma unroll
    for (int e = 0; e < 4; ++e) {
      hp[e] = pkbf(hv[2 * e] * rstd - mr, hv[2 * e + 1] * rstd - mr);
      op[e] = pkbf(hv[2 * e] + bflo(hp[e]), hv[2 * e + 1] + bfhi(hp[e]));
    }
    __builtin_amdgcn_raw_buffer_store_b128(op, r_out, row * 1024 + col * 2, 0, SC1);
    if (isgu || isdn) k4 = LD128(tid * 16, WS_KEY);
    WAIT_VM(0);
    ST128(nosent4(hp), row * 1024 + col * 2, WS_HN);
  }
  STAMP(13);
  if (!isgu && !isdn) { STAMP_FLUSH(); return; }

  const int trd = (8 * g + q) * 32 + 8 * p4;
  u32x4 fw[6];
  {
#pragma unroll
    for (int p = 0; p < 6; ++p) {
      const int pc = isgu ? (p >> 1) * 16 + wid * 2 + (p & 1) : min(p * 8 + wid, 42);
      fw[p] = ldstr2(lds + L_F + min(pc, 42) * 1024 + trd, 128);
    }
  }
#pragma unroll 1
  for (unsigned spins = 0; spins < SPIN_CAP; ++spins) {
    if (__all(umax4(k4) != SENT)) break;
    FENCE();
    k4 = LD128(tid * 16, WS_KEY);
  }
  STAMP(14);
  {
    unsigned m = umax4(k4);
    m = max(m, dppu<0xB1>(m)); m = max(m, dppu<0x4E>(m)); m = max(m, dppu<0x141>(m)); m = max(m, dppu<0x140>(m));
    m = max(m, dppu<0x142>(m)); m = max(m, dppu<0x143>(m));
    if (lane == 63) cand[64 + wid] = m;
  }
  LDS_BAR();
  {
    const u32x4 g0 = *(LAS u32x4*)(cand + 64), g1 = *(LAS u32x4*)(cand + 68);
    const unsigned T = min(min(min(g0[0], g0[1]), min(g0[2], g0[3])), min(min(g1[0], g1[1]), min(g1[2], g1[3])));
    const unsigned c0 = k4[0] >= T, c1 = k4[1] >= T, c2 = k4[2] >= T, c3 = k4[3] >= T;
    const unsigned cnt = (c0 + c1) + (c2 + c3);
    if (cnt) {
      unsigned pos;
      asm volatile("ds_add_rtn_u32 %0, %1, %2\n\ts_waitcnt lgkmcnt(0)" : "=v"(pos) : "v"((unsigned)(lds0 + L_SEL + 72 * 4)), "v"(cnt) : "memory");
      cand[c0 && pos < 64 ? pos : 73] = k4[0]; pos += c0;
      cand[c1 && pos < 64 ? pos : 73] = k4[1]; pos += c1;
      cand[c2 && pos < 64 ? pos : 73] = k4[2]; pos += c2;
      cand[c3 && pos < 64 ? pos : 73] = k4[3];
    }
  }
  LDS_BAR();
  LAS unsigned* sel = cand + 80 + wid * 8;
  {
    const unsigned nv = cand[72];
    const unsigned c = cand[t];
    u32x4 o4[4];
#pragma unroll
    for (int e = 0; e < 4; ++e) o4[e] = *(LAS u32x4*)(cand + h * 16 + e * 4);
    const unsigned n = __builtin_amdgcn_readfirstlane(nv);
    if (__builtin_expect(n <= 32, 1)) {
      unsigned rk = 0;
#pragma unroll
      for (int e = 0; e < 4; ++e) rk += (o4[e][0] > c) + (o4[e][1] > c) + (o4[e][2] > c) + (o4[e][3] > c);
      auto r2 = __builtin_amdgcn_permlane32_swap(rk, rk, false, false);
      rk = r2[0] + r2[1];
      if (rk < 8) sel[rk] = c;
    } else if (n <= 64) {
      const unsigned c2 = cand[lane];
      unsigned rk = 0;
#pragma unroll 1
      for (int j = 0; j < 64; j += 4) { const u32x4 v = *(LAS u32x4*)(cand + j); rk += (v[0] > c2) + (v[1] > c2) + (v[2] > c2) + (v[3] > c2); }
      if (rk < 8) sel[rk] = c2;
    } else {
      LAS unsigned* all = cand + 256;
      *(LAS u32x4*)(all + tid * 4) = k4;
      LDS_BAR();
      unsigned rk[4] = {0, 0, 0, 0};
#pragma unroll 1
      for (int j = 0; j < 2048; ++j) {
        const unsigned v = all[j];
#pragma unroll
        for (int e = 0; e < 4; ++e) rk[e] += v > k4[e];
      }
#pragma unroll
      for (int e = 0; e < 4; ++e) if (rk[e] < 8) cand[160 + rk[e]] = k4[e];
      LDS_BAR();
      sel = cand + 160;
    }
  }
  const int tok = lane & 15;
  const unsigned key = sel[tok & 7];
  const int trow = 2047 - (int)(key & 2047u);
  STAMP(15);
  LAS f32x4* red = (LAS f32x4*)(lds + L_WO);
  if (isgu) {
    u32x4 xb[2];
#pragma unroll 1
    for (unsigned spins = 0; spins < SPIN_CAP; ++spins) {
      FENCE();
#pragma unroll
      for (int u = 0; u < 2; ++u) xb[u] = LD128(trow * 1024 + u * 64 + g * 16, WS_HN + wid * 128);
      if (__all(max(umax4(xb[0]), umax4(xb[1])) != SENT)) break;
    }
    STAMP(16);
    f32x4 ag = mfma16(fw[0], xb[0], f32x4{}), au = mfma16(fw[2], xb[0], f32x4{});
    ag = mfma16(fw[1], xb[1], ag); au = mfma16(fw[3], xb[1], au);
    red[wid * 64 + lane] = tok < 8 ? ag : au;
    LDS_BAR();
    if (wid == 0) {
      const f32x4 s = ((red[lane] + red[64 + lane]) + (red[128 + lane] + red[192 + lane])) + ((red[256 + lane] + red[320 + lane]) + (red[384 + lane] + red[448 + lane]));
      float av[4];
#pragma unroll
      for (int r = 0; r < 4; ++r) {
        const float gg = rbf(s[r]);
        const float uu = dppf<0x128>(gg);
        const float sl = rbf(gg * __builtin_amdgcn_rcpf(1.f + __expf(-gg)));
        av[r] = sl * uu;
      }
      if (tok < 8) ST64((u32x2{nosent(pkbf(av[0], av[1])), nosent(pkbf(av[2], av[3]))}), tok * 32 + g * 8, WS_ACT + b * 256);
    }
    STAMP(17);
    STAMP_FLUSH();
    return;
  }
  const int nb = b - DN0;
  u32x2 o0 = u32x2{0, 0};
  float sg = 0.f;
  if (wid == 0) {
#pragma unroll 1
    for (unsigned spins = 0; spins < SPIN_CAP; ++spins) {
      FENCE();
      const u32x2 hc = LD64(trow * 1024 + g * 8, WS_HN + nb * 32);
      if (__all(max(hc[0], hc[1]) != SENT)) break;
    }
    FENCE();
    o0 = __builtin_amdgcn_raw_buffer_load_b64(r_out, trow * 1024 + g * 8, nb * 32, SC1);
    const unsigned ov = key & 0xFFFFF800u;
    const float r = rbf(__uint_as_float((ov & 0x80000000u) ? (ov & 0x7FFFFFFFu) : ~(ov | 0x7FFu)));
    sg = rbf(__builtin_amdgcn_rcpf(1.f + __expf(-r)));
  }
  const __amdgpu_buffer_rsrc_t r_act = mkrsrc(ws + WS_ACT, 22016);
  u32x4 ab[6];
  const int aoff = wid * 512 + (g >> 1) * 256 + (tok & 7) * 32 + (g & 1) * 16;
#pragma unroll
  for (int p = 0; p < 6; ++p) ab[p] = __builtin_amdgcn_raw_buffer_load_b128(r_act, aoff, p * 4096, SC1);
#pragma unroll 1
  for (unsigned spins = 0; spins < SPIN_CAP; ++spins) {
    bool miss[6], any = false;
#pragma unroll
    for (int p = 0; p < 6; ++p) { miss[p] = __any(umax4(ab[p]) == SENT); any |= miss[p]; }
    if (!any) break;
    FENCE();
#pragma unroll
    for (int p = 0; p < 6; ++p) if (miss[p]) ab[p] = __builtin_amdgcn_raw_buffer_load_b128(r_act, aoff, p * 4096, SC1);
  }
  STAMP(16);
  f32x4 ay = f32x4{};
#pragma unroll
  for (int p = 0; p < 6; ++p) ay = mfma16(fw[p], ab[p], ay);
  red[wid * 64 + lane] = ay;
  LDS_BAR();
  if (wid == 0 && tok < 8) {
    ay = ((ay + red[64 + lane]) + (red[128 + lane] + red[192 + lane])) + ((red[256 + lane] + red[320 + lane]) + (red[384 + lane] + red[448 + lane]));
    const float y0 = rbf(rbf(ay[0]) * sg), y1 = rbf(rbf(ay[1]) * sg), y2 = rbf(rbf(ay[2]) * sg), y3 = rbf(rbf(ay[3]) * sg);
    __builtin_amdgcn_raw_buffer_store_b64(u32x2{pkbf(bflo(o0[0]) + y0, bfhi(o0[0]) + y1), pkbf(bflo(o0[1]) + y2, bfhi(o0[1]) + y3)}, r_out,
                                          trow * 1024 + g * 8, nb * 32, SC1);
  }
  STAMP(17);
}

extern "C" void kernel_launch(void* const* d_in, const int* in_sizes, int n_in, void* d_out, int out_size, void* d_ws, size_t ws_size,
                              hipStream_t stream) {
  (void)hipFuncSetAttribute((const void*)fused_block, hipFuncAttributeMaxDynamicSharedMemorySize, LDS_BYTES);
  fused_block<<<256, 576, LDS_BYTES, stream>>>((const u16*)d_in[0], (const u16*)d_in[1], (const u16*)d_in[2], (const u16*)d_in[4],
                                               (const u16*)d_in[5], (const u16*)d_in[6], (const u16*)d_in[7], (char*)d_ws, (u16*)d_out);
}
```

```cpp
#include <hip/hip_runtime.h>
#include <hip/hip_bf16.h>
#define STAMP_DECL
#define STAMP(s)
#define STAMP_FLUSH()
#define STAMP_ARG
#define STAMP_PASS

#define PROG 0
#define CODEPF 1
#define CODE_KB 25
typedef unsigned short u16;
typedef __bf16 bf16x2_t __attribute__((ext_vector_type(2)));
typedef __bf16 bf16x8_t __attribute__((ext_vector_type(8)));
typedef __bf16 bf16x16_t __attribute__((ext_vector_type(16)));
typedef short s16x4 __attribute__((ext_vector_type(4)));
typedef float f32x4 __attribute__((ext_vector_type(4)));
typedef float f32x8 __attribute__((ext_vector_type(8)));
typedef float f32x16 __attribute__((ext_vector_type(16)));
typedef unsigned u32x2 __attribute__((ext_vector_type(2)));
typedef unsigned u32x4 __attribute__((ext_vector_type(4)));
#define LAS __attribute__((address_space(3)))

constexpr int D_ = 512, F_ = 1376;
constexpr unsigned SENT = 0xFFFFFFFFu;
constexpr float LN_EPS = 1e-5f;
constexpr float QSCALE = 0.25f * 1.4426950408889634f;
constexpr unsigned SPIN_CAP = 1u << 16;
constexpr int SC1 = 16;
constexpr unsigned ONE2 = 0x3F803F80u;

constexpr unsigned WS_KV = 0;
constexpr unsigned WS_QF = 4u << 20;
constexpr unsigned WS_OF = 6u << 20;
constexpr unsigned WS_HN = 8u << 20;
constexpr unsigned WS_XS = 10u << 20;
constexpr unsigned WS_ACT = WS_XS + (256u << 10);
constexpr unsigned WS_KEY = WS_ACT + 22016;
constexpr unsigned WS_FLAG = WS_XS + (320u << 10);
constexpr unsigned WS_FLAG2 = WS_FLAG + 32768;
constexpr unsigned WS_EPOCH = WS_FLAG2 + 32768;
constexpr unsigned WS_BYTES = WS_EPOCH + 128;

__device__ __forceinline__ f32x16 mfma32(u32x4 a, u32x4 b, f32x16 c) {
  return __builtin_amdgcn_mfma_f32_32x32x16_bf16(__builtin_bit_cast(bf16x8_t, a), __builtin_bit_cast(bf16x8_t, b), c, 0, 0, 0);
}
__device__ __forceinline__ f32x4 mfma16(u32x4 a, u32x4 b, f32x4 c) {
  return __builtin_amdgcn_mfma_f32_16x16x32_bf16(__builtin_bit_cast(bf16x8_t, a), __builtin_bit_cast(bf16x8_t, b), c, 0, 0, 0);
}
__device__ __forceinline__ f32x16 smfmac32(u32x4 a, f32x8 b, f32x16 c, int idx) {
  return __builtin_amdgcn_smfmac_f32_32x32x32_bf16(__builtin_bit_cast(bf16x8_t, a), __builtin_bit_cast(bf16x16_t, b), c, idx, 0, 0);
}
__device__ __forceinline__ float dot2bf(unsigned a, unsigned b, float c) {
  return __builtin_amdgcn_fdot2_f32_bf16(__builtin_bit_cast(bf16x2_t, a), __builtin_bit_cast(bf16x2_t, b), c, false);
}
__device__ __forceinline__ float swap32_sum(float v) {
  auto r = __builtin_amdgcn_permlane32_swap(__float_as_uint(v), __float_as_uint(v), false, false);
  return __uint_as_float(r[0]) + __uint_as_float(r[1]);
}
__device__ __forceinline__ float swap32_max(float v) {
  auto r = __builtin_amdgcn_permlane32_swap(__float_as_uint(v), __float_as_uint(v), false, false);
  return __builtin_fmaxf(__uint_as_float(r[0]), __uint_as_float(r[1]));
}
__device__ __forceinline__ unsigned pkbf(float lo, float hi) {
  bf16x2_t v; v[0] = (__bf16)lo; v[1] = (__bf16)hi; return __builtin_bit_cast(unsigned, v);
}
__device__ __forceinline__ float bflo(unsigned u) { return __uint_as_float(u << 16); }
__device__ __forceinline__ float bfhi(unsigned u) { return __uint_as_float(u & 0xFFFF0000u); }
__device__ __forceinline__ float rbf(float f) { return (float)(__bf16)f; }
__device__ __forceinline__ void glds16(const void* sbase, unsigned voff, unsigned lds_base) {
  unsigned sv;
  asm volatile("s_mov_b32 %0, m0\n\ts_mov_b32 m0, %3\n\ts_nop 0\n\tglobal_load_lds_dwordx4 %1, %2\n\ts_mov_b32 m0, %0"
               : "=&s"(sv) : "v"(voff), "s"(sbase), "s"(lds_base) : "memory");
}
__device__ __forceinline__ void glds16c(const void* sbase, unsigned voff, unsigned lds_base) {
  unsigned sv;
  asm volatile("s_mov_b32 %0, m0\n\ts_mov_b32 m0, %3\n\ts_nop 0\n\tglobal_load_lds_dwordx4 %1, %2 sc1\n\ts_mov_b32 m0, %0"
               : "=&s"(sv) : "v"(voff), "s"(sbase), "s"(lds_base) : "memory");
}
__device__ __forceinline__ void glds8c(const void* sbase, unsigned voff, unsigned lds_base) {
  unsigned sv;
  asm volatile("s_mov_b32 %0, m0\n\ts_mov_b32 m0, %3\n\ts_nop 0\n\t"
               "global_load_lds_dwordx4 %1, %2 offset:-4096 sc1\n\tglobal_load_lds_dwordx4 %1, %2 offset:-3072 sc1\n\t"
               "global_load_lds_dwordx4 %1, %2 offset:-2048 sc1\n\tglobal_load_lds_dwordx4 %1, %2 offset:-1024 sc1\n\t"
               "global_load_lds_dwordx4 %1, %2 sc1\n\tglobal_load_lds_dwordx4 %1, %2 offset:1024 sc1\n\t"
               "global_load_lds_dwordx4 %1, %2 offset:2048 sc1\n\tglobal_load_lds_dwordx4 %1, %2 offset:3072 sc1\n\t"
               "s_mov_b32 m0, %0"
               : "=&s"(sv) : "v"(voff), "s"((const char*)sbase + 4096), "s"(lds_base + 4096) : "memory");
}
__device__ __forceinline__ void glds4(const void* sbase, const unsigned (&voff)[4], unsigned lds_base) {
  unsigned sv;
  asm volatile("s_mov_b32 %0, m0\n\ts_mov_b32 m0, %6\n\ts_nop 0\n\t"
               "global_load_lds_dwordx4 %1, %5\n\tglobal_load_lds_dwordx4 %2, %5 offset:1024\n\t"
               "global_load_lds_dwordx4 %3, %5 offset:2048\n\tglobal_load_lds_dwordx4 %4, %5 offset:3072\n\t"
               "s_mov_b32 m0, %0"
               : "=&s"(sv) : "v"(voff[0]), "v"(voff[1]), "v"(voff[2]), "v"(voff[3]), "s"(sbase), "s"(lds_base) : "memory");
}
#define FENCE() asm volatile("" ::: "memory")
#define WAIT_BAR(N) asm volatile("s_waitcnt vmcnt(" #N ") lgkmcnt(0)\n\ts_barrier" ::: "memory")
#define VM_BAR(N) asm volatile("s_waitcnt vmcnt(" #N ")\n\ts_barrier" ::: "memory")
#define LDS_BAR() do { FENCE(); __builtin_amdgcn_s_waitcnt(0xC07F); __builtin_amdgcn_s_barrier(); FENCE(); } while (0)
#define BAR() do { FENCE(); __builtin_amdgcn_s_barrier(); FENCE(); } while (0)
#define WAIT_VM(N) asm volatile("s_waitcnt vmcnt(" #N ")" ::: "memory")
#define LD128(off, soff) __builtin_amdgcn_raw_buffer_load_b128(r_ws, off, soff, SC1)
#define LD64(off, soff) __builtin_amdgcn_raw_buffer_load_b64(r_ws, off, soff, SC1)
#define ST128(v, off, soff) __builtin_amdgcn_raw_buffer_store_b128(v, r_ws, (off) + (soff), 0, SC1)
#define ST64(v, off, soff) __builtin_amdgcn_raw_buffer_store_b64(v, r_ws, off, soff, SC1)
__device__ __forceinline__ u32x4 ldsr128(const LAS char* p) { return *(const LAS u32x4*)p; }
__device__ __forceinline__ u32x2 ldstr(const LAS char* p) {
  return __builtin_bit_cast(u32x2, __builtin_amdgcn_ds_read_tr16_b64_v4i16((LAS s16x4*)p));
}
__device__ __forceinline__ u32x4 ldstr2(const LAS char* p, int off2) { const u32x2 a = ldstr(p), b = ldstr(p + off2); return u32x4{a[0], a[1], b[0], b[1]}; }
__device__ __forceinline__ int swapb23(int v) { return (v & ~12) | ((v & 4) << 1) | ((v & 8) >> 1); }
__device__ __forceinline__ unsigned nosent(unsigned v) { return min(v, 0xFFFFFFFEu); }
__device__ __forceinline__ u32x4 nosent4(u32x4 v) { return u32x4{nosent(v[0]), nosent(v[1]), nosent(v[2]), nosent(v[3])}; }
__device__ __forceinline__ unsigned umax4(u32x4 v) { return max(max(v[0], v[1]), max(v[2], v[3])); }
__device__ __forceinline__ __amdgpu_buffer_rsrc_t mkrsrc(const void* p, unsigned bytes) {
  return __builtin_amdgcn_make_buffer_rsrc((void*)p, 0, bytes, 0x00020000);
}
template <int CTRL> __device__ __forceinline__ float dppf(float v) {
  return __uint_as_float(__builtin_amdgcn_update_dpp(0, __float_as_uint(v), CTRL, 0xF, 0xF, true));
}
template <int CTRL> __device__ __forceinline__ unsigned dppu(unsigned v) { return __builtin_amdgcn_update_dpp(0, v, CTRL, 0xF, 0xF, true); }
__device__ __forceinline__ unsigned rdl(unsigned v, int l) { return (unsigned)__builtin_amdgcn_readlane((int)v, l); }
__device__ __forceinline__ float rdlf(float v, int l) { return __uint_as_float(rdl(__float_as_uint(v), l)); }
__device__ __forceinline__ float sum8(float v) { v += dppf<0xB1>(v); v += dppf<0x4E>(v); v += dppf<0x141>(v); return v; }
__device__ __forceinline__ u32x4 pack8(const float* v) { return u32x4{pkbf(v[0], v[1]), pkbf(v[2], v[3]), pkbf(v[4], v[5]), pkbf(v[6], v[7])}; }

constexpr int K1_SLOT = 16384 + 12288, K1_CS = 4 * K1_SLOT, K1_CSP = K1_CS + 512, K1_TAG = K1_CSP + 8 * 384;
constexpr int L_Q = 0, L_KV = 24576, L_ONE = L_KV + 131072, LDS_BYTES = 163840;
constexpr int L_F = 0, L_WO = 45056, L_SEL = 114688, L_ST = L_SEL + 12288;
constexpr int NGU = 86, DN0 = 128, NDN = 32;

template <int NV>
__device__ __forceinline__ void qkv_body(const __amdgpu_buffer_rsrc_t r_ws, LAS char* lds, int rb, int cb, int wid STAMP_ARG) {
  const int lane = threadIdx.x & 63, t = lane & 31, h = lane >> 5;
  int xr[4];
#pragma unroll
  for (int u = 0; u < 4; ++u) xr[u] = wid * 4096 + t * 128 + (((2 * u + h) ^ ((t >> 1) & 7)) * 16);
  const int wrd = 16384 + (8 * h + ((lane >> 2) & 3)) * 192 + 32 * ((lane >> 4) & 1) + 8 * (lane & 3);
  f32x16 acc[3];
#pragma unroll
  for (int j = 0; j < 3; ++j) acc[j] = f32x16{};
  float s1a = 0.f, s1b = 0.f, s2a = 0.f, s2b = 0.f;
  u32x4 xf = u32x4{0, 0, 0, 0}, wf[3];
#pragma unroll
  for (int j = 0; j < 3; ++j) wf[j] = u32x4{0, 0, 0, 0};
  auto mm = [&]() {
#pragma unroll
    for (int j = 0; j < 3; ++j) {
      if (j >= 3 - NV) acc[j] = mfma32(xf, wf[j], acc[j]);
      else acc[j] = mfma32(wf[j], xf, acc[j]);
    }
  };
  const int tile = rb * 4 + wid;
  const int voffV = tile * 2048 + 1024 + (t >> 4) * 131072 + h * 256 + swapb23(t & 15) * 16;
  const int voffK = tile * 2048 + swapb23(t) * 32 + h * 16;
  const int voffQ = tile * 1024 + t * 32 + h * 16;
#pragma unroll 1
  for (int c = 0; c < 8; ++c) {
    LDS_BAR();
    if (c == 0) STAMP(1);
    const LAS char* sb = lds + (c & 3) * K1_SLOT;
    u32x4 xn[4];
#pragma unroll
    for (int u = 0; u < 4; ++u) {
      xn[u] = ldsr128(sb + xr[u]);
      u32x4 wn[3];
#pragma unroll
      for (int j = 0; j < 3; ++j) wn[j] = ldstr2(sb + wrd + u * 3072 + j * 64, 768);
      mm();
#pragma unroll
      for (int z = 0; z < 3; ++z) { __builtin_amdgcn_sched_group_barrier(0x8, 1, 0); __builtin_amdgcn_sched_group_barrier(0x102, 3, 0); }
      __builtin_amdgcn_sched_barrier(0);
      xf = xn[u];
#pragma unroll
      for (int j = 0; j < 3; ++j) wf[j] = wn[j];
    }
#pragma unroll
    for (int u = 0; u < 4; ++u)
#pragma unroll
      for (int e = 0; e < 4; e += 2) {
        s1a = dot2bf(xn[u][e], ONE2, s1a); s2a = dot2bf(xn[u][e], xn[u][e], s2a);
        s1b = dot2bf(xn[u][e + 1], ONE2, s1b); s2b = dot2bf(xn[u][e + 1], xn[u][e + 1], s2b);
      }
    asm volatile("" : "+v"(s1a), "+v"(s1b), "+v"(s2a), "+v"(s2b));
    __builtin_amdgcn_sched_barrier(0);
  }
  __builtin_amdgcn_s_barrier();
  mm();
  STAMP(2);
  LAS float* scr = (LAS float*)(lds + wid * 4096);
  const LAS float* csum = (const LAS float*)(lds + K1_CS);
  const float s2 = swap32_sum(s2a + s2b);
  const float mean = swap32_sum(s1a + s1b) * (1.f / D_);
  const float var = __builtin_fmaxf(s2 * (1.f / D_) - mean * mean, 0.f);
  const float rstd = __builtin_amdgcn_rsqf(var + LN_EPS);
  if (NV > 0 && h == 0) { scr[t] = rstd; scr[32 + t] = mean * rstd; }
  BAR();
#pragma unroll
  for (int j = 0; j < 3; ++j) {
    const int gt = cb * 3 + j;
    float v[16];
    if (j >= 3 - NV) {
      const float cs = csum[32 * j + t];
#pragma unroll
      for (int gq = 0; gq < 4; ++gq) {
        const f32x4 a = *(const LAS f32x4*)(scr + 8 * gq + 4 * h), b = *(const LAS f32x4*)(scr + 32 + 8 * gq + 4 * h);
#pragma unroll
        for (int e = 0; e < 4; ++e) v[4 * gq + e] = acc[j][4 * gq + e] * a[e] - b[e] * cs;
      }
      const int so = WS_KV + (gt - 32) * 262144;
      ST128(pack8(v), voffV, so); ST128(pack8(v + 8), voffV + 512, so);
    } else {
      const bool isq = gt < 16;
      const float sc = isq ? QSCALE : 1.f;
      const float a = rstd * sc, b = mean * rstd * sc;
#pragma unroll
      for (int gq = 0; gq < 4; ++gq) {
        const f32x4 c4 = *(const LAS f32x4*)(csum + 32 * j + 8 * gq + 4 * h);
#pragma unroll
        for (int e = 0; e < 4; ++e) v[4 * gq + e] = acc[j][4 * gq + e] * a - b * c4[e];
      }
      const int vo = isq ? voffQ : voffK;
      const int so = isq ? WS_QF + gt * 131072 : WS_KV + (gt - 16) * 262144;
      ST128(pack8(v), vo, so); ST128(pack8(v + 8), vo, so + (isq ? 65536 : 131072));
    }
  }
  STAMP(3);
}

__device__ __forceinline__ float rowmax16(const f32x16& s) {
  float a = __builtin_fmaxf(__builtin_fmaxf(s[0], s[1]), s[2]), b = __builtin_fmaxf(__builtin_fmaxf(s[3], s[4]), s[5]);
  a = __builtin_fmaxf(__builtin_fmaxf(a, s[6]), s[7]); b = __builtin_fmaxf(__builtin_fmaxf(b, s[8]), s[9]);
  a = __builtin_fmaxf(__builtin_fmaxf(a, s[10]), s[11]); b = __builtin_fmaxf(__builtin_fmaxf(b, s[12]), s[13]);
  a = __builtin_fmaxf(__builtin_fmaxf(a, s[14]), s[15]);
  return swap32_max(__builtin_fmaxf(a, b));
}
__device__ __forceinline__ f32x8 lo8(const f32x16& s) { return __builtin_shufflevector(s, s, 0, 1, 2, 3, 4, 5, 6, 7); }
__device__ __forceinline__ f32x8 hi8(const f32x16& s) { return __builtin_shufflevector(s, s, 8, 9, 10, 11, 12, 13, 14, 15); }

#define E1(X, i) "v_exp_f32 v[" #X "+" #i "], v[" #X "+" #i "]\n\t"
#define SMF(A, B, off) "v_smfmac_f32_32x32x32_bf16 v[0:15], v[" #A ":" #A "+3], v[" #B "+" #off ":" #B "+" #off "+7], v88\n\t"
#define QK(Y) "v_mfma_f32_32x32x16_bf16 v[" #Y ":" #Y "+15], v[68:71], v[64:67], v[16:31]\n\t"
#define RDV(D, off) "ds_read_b128 v[" #D ":" #D "+3], v90 offset:" #off "\n\t"
#define RDK(off) "ds_read_b128 v[68:71], v89 offset:" #off "\n\t"
#define STEP(X, Y, VA, VPB, VA2, VB2, KOFF, VOFF, VOFF2, PA, PB, EXTRA) \
  PA E1(X, 0) E1(X, 1) SMF(VPB, Y, 8) RDV(VA2, VOFF) \
  PB E1(X, 2) E1(X, 3) E1(X, 4) E1(X, 5) \
  "s_waitcnt lgkmcnt(1)\n\t" QK(Y) RDV(VB2, VOFF2) RDK(KOFF) \
  E1(X, 6) E1(X, 7) E1(X, 8) E1(X, 9) \
  SMF(VA, X, 0) EXTRA \
  E1(X, 10) E1(X, 11) E1(X, 12) E1(X, 13) E1(X, 14) E1(X, 15)
#define ATTN_LOOP(L1, L2, PHI, PLO) \
        L1 ":\n\t" \
        "s_bitcmp1_b32 0x824, %[cnt]\n\t" \
        "s_cbranch_scc0 " L2 "f\n\t" \
        "s_barrier\n\t" \
        L2 ":\n\t" \
        STEP(32, 48, 72, 84, 80, 84, 4096, 2048, 2560, PHI, "", "") \
        STEP(48, 32, 80, 76, 72, 76, 6144, 4096, 4608, "", "", "") \
        STEP(32, 48, 72, 84, 80, 84, 8192, 6144, 6656, "", "", "v_add_u32 v90, v90, v91\n\t") \
        STEP(48, 32, 80, 76, 72, 76, 10240, 0, 512, "", PLO, "v_add_u32 v89, 0x2000, v89\n\ts_add_u32 %[cnt], %[cnt], 1\n\ts_cmp_lg_u32 %[cnt], 16\n\t") \
        "s_cbranch_scc1 " L1 "b\n\t"

__global__ __launch_bounds__(576, 3) void fused_block(const u16* __restrict__ x, const u16* __restrict__ wqkv, const u16* __restrict__ wo,
                                                      const u16* __restrict__ wr, const u16* __restrict__ wg, const u16* __restrict__ wu,
                                                      const u16* __restrict__ wd, char* __restrict__ ws, u16* __restrict__ out) {
  extern __shared__ __attribute__((aligned(16))) char smem[];
  LAS char* lds = (LAS char*)smem;
  const unsigned lds0 = (unsigned)(uintptr_t)lds;
  {
    unsigned long long pc; unsigned s0, s1, v0;
    asm volatile("v_readfirstlane_b32 %1, %4\n\ts_lshr_b32 %2, %5, 3\n\ts_getpc_b64 %0\n\ts_and_b64 %0, %0, -16\n\t"
                 "s_cmp_lt_u32 %1, 64\n\ts_cbranch_scc0 9f\n\t"
                 "s_cmp_lt_u32 %2, %6\n\ts_cbranch_scc0 9f\n\t"
                 "s_lshl_b32 %2, %2, 10\n\tv_lshlrev_b32 %3, 4, %4\n\tv_add_u32 %3, %2, %3\n\t"
                 "s_mov_b32 %1, m0\n\ts_mov_b32 m0, %7\n\ts_nop 0\n\t"
                 "global_load_lds_dwordx4 %3, %0\n\ts_mov_b32 m0, %1\n\t9:"
                 : "=&s"(pc), "=&s"(s0), "=&s"(s1), "=&v"(v0) : "v"(threadIdx.x), "s"(blockIdx.x), "i"(CODE_KB), "s"(lds0 + 131072) : "scc", "memory");
  }
  const int tid = threadIdx.x, lane = tid & 63, t = lane & 31, h = lane >> 5;
  const int wid = __builtin_amdgcn_readfirstlane(tid >> 6);
  const int b = blockIdx.x;
  const int rb1 = 4 * (b & 3) + ((b >> 3) & 3), cb1 = 8 * ((b >> 2) & 1) + (b >> 5);
  const int head = (b & 7) * 4 + ((b >> 3) & 3), qb = b >> 5;
  const int pm = head, cb = qb;
  const unsigned l16 = lane * 16;
  const int lw = wid - 4;
  const int rot = ((((b >> 5) + ((b >> 3) & 3)) & 1)) * 4;
  unsigned xoff[2], woff[3];
  auto stage = [&](int c) {
    const int kc = (c + rot) & 7;
    const char* xs = (const char*)x + kc * 128;
    const char* wsrc = (const char*)wqkv + kc * (64 * 3072);
    const unsigned base = lds0 + (c & 3) * K1_SLOT;
#pragma unroll
    for (int p = 0; p < 4; ++p) glds16(xs + (p >> 1) * 16384, xoff[p & 1], base + lw * 4096 + p * 1024);
#pragma unroll
    for (int p = 0; p < 3; ++p) glds16(wsrc, woff[p], base + 16384 + (lw * 3 + p) * 1024);
  };
  if (lw >= 0 && lw < 4) {
#pragma unroll
    for (int p = 0; p < 2; ++p) {
      const int row = p * 8 + (lane >> 3), slot = lane & 7;
      xoff[p] = (unsigned)((rb1 * 128 + lw * 32 + row) * 1024 + ((slot ^ ((row >> 1) & 7)) * 16));
    }
#pragma unroll
    for (int p = 0; p < 3; ++p) {
      const int idx = (lw * 3 + p) * 64 + lane, k = idx / 12, c = idx - k * 12;
      woff[p] = (unsigned)(k * 3072 + cb1 * 192 + c * 16);
    }
    stage(0); stage(1);
  }
  FENCE();
  const bool isgu = b < NGU, isdn = b >= DN0 && b < DN0 + NDN;
  const __amdgpu_buffer_rsrc_t r_ws = mkrsrc(ws, WS_BYTES), r_out = mkrsrc(out, 2u << 20);
  STAMP_DECL STAMP(0);

  if (wid == 8) {
    __builtin_amdgcn_s_setprio(3);
    const u32x2 ep = LD64(0, WS_EPOCH);
    const u32x4 w4 = *(const u32x4*)(wr + lane * 8);
    if (lane < 16) { const unsigned v = (lane & 1) ? 0u : ONE2; *(LAS u32x4*)(lds + L_ONE + (lane >> 2) * 2048 + ((lane >> 1) & 1) * 512 + (lane & 1) * 16) = u32x4{v, v, v, v}; }
#pragma unroll 1
    for (int c = 0; c < 5; ++c) BAR();
    const unsigned long long e64 = (((unsigned long long)ep[1] << 32) | ep[0]) + 1ull;
    const u32x2 tag = u32x2{(unsigned)e64, (unsigned)(e64 >> 32)};
    float sw;
    sw = dot2bf(w4[0], ONE2, dot2bf(w4[1], ONE2, 0.f)) + dot2bf(w4[2], ONE2, dot2bf(w4[3], ONE2, 0.f));
    sw = sum8(sw); sw += dppf<0x140>(sw);
    sw = (rdlf(sw, 0) + rdlf(sw, 16)) + (rdlf(sw, 32) + rdlf(sw, 48));
    const int hp = head >> 1;
    const int fidx = lane < 16 ? lane * 16 + (16 + hp) / 3 : lane < 32 ? (lane - 16) * 16 + (32 + hp) / 3 : (qb * 2 + (lane & 1)) * 16 + hp / 3;
    const char* kvsrc = ws + WS_KV + (size_t)head * 131072;
    LDS_BAR();
#pragma unroll 1
    for (int c = 6; c < 10; ++c) BAR();
    BAR();
    if (lane == 0) ST64(tag, 0, WS_FLAG + (rb1 * 16 + cb1) * 128);
    STAMP(1);
    unsigned long long seen = 0;
    auto wait_flags = [&](unsigned long long need) {
#pragma unroll 1
      for (unsigned spins = 0; spins < SPIN_CAP && (seen & need) != need; ++spins) {
        FENCE();
        const u32x2 f = LD64(fidx * 128, WS_FLAG);
        seen = __ballot((f[0] == tag[0]) & (f[1] == tag[1]));
      }
      FENCE();
    };
    wait_flags(0x300070007ull);
    STAMP(5);
    glds8c(ws + WS_QF + head * 65536 + qb * 8192, l16, lds0 + L_Q);
#pragma unroll
    for (int p = 0; p < 3; ++p) glds8c(kvsrc + p * 8192, l16, lds0 + L_KV + p * 8192);
    VM_BAR(0);
    wait_flags(0xFFFFFFFFull);
#pragma unroll 1
    for (int p = 3; p < 10; ++p) glds8c(kvsrc + p * 8192, l16, lds0 + L_KV + p * 8192);
    VM_BAR(32);
    WAIT_VM(8);
#pragma unroll 1
    for (int p = 10; p < 16; ++p) glds8c(kvsrc + p * 8192, l16, lds0 + L_KV + p * 8192);
    VM_BAR(0);
    {
      const u32x4 s4 = u32x4{SENT, SENT, SENT, SENT};
#pragma unroll
      for (int e = 0; e < 8; ++e) ST128(s4, l16, WS_OF + (qb * 4 + (e >> 1)) * 65536 + head * 2048 + (e & 1) * 1024);
#pragma unroll
      for (int e = 0; e < 8; ++e) {
        const int off = (pm * 64 + e * 8 + (lane >> 3)) * 1024 + cb * 128 + (lane & 7) * 16;
        ST128(s4, off, WS_HN);
        __builtin_amdgcn_raw_buffer_store_b128(s4, r_out, off, 0, SC1);
      }
      ST128(s4, l16, WS_XS + (pm * 8 + cb) * 1024);
      if (lane < 8 && b * 8 + lane < (22016 + 8192) / 16) ST128(s4, l16, WS_ACT + b * 128);
      WAIT_VM(0);
      if (lane == 0) ST64(tag, 0, WS_FLAG2 + b * 128);
    }
    unsigned foff[4];
    const unsigned fstr = isgu ? 32 * F_ * 2 : 32768;
#pragma unroll
    for (int j = 0; j < 4; ++j)
      foff[j] = (isgu ? (unsigned)((lane >> 1) * (F_ * 2) + b * 32) : (unsigned)((lane >> 1) * 1024 + (b - DN0) * 32)) + (lane & 1) * 16 + j * (fstr - 1024);
    auto ffn = [&](int q0, int q1) {
      if (isgu) {
#pragma unroll 1
        for (int p = q0; p < min(q1, 8); ++p) glds4((const char*)(p < 4 ? wg : wu) + (p & 3) * (128 * F_ * 2), foff, lds0 + L_F + p * 4096);
      } else if (isdn) {
#pragma unroll 1
        for (int p = q0; p < min(q1, 10); ++p) glds4((const char*)wd + p * 131072, foff, lds0 + L_F + p * 4096);
        if (q1 > 10) {
#pragma unroll 1
          for (int p = 40; p < 43; ++p) glds16((const char*)wd + p * 32768, foff[0], lds0 + L_F + p * 1024);
        }
      }
    };
    ffn(0, 6);
#pragma unroll 1
    for (unsigned spins = 0; spins < SPIN_CAP; ++spins) {
      FENCE();
      bool ok = true;
#pragma unroll
      for (int e = 0; e < 4; ++e) { const u32x2 f = LD64(lane * 128, WS_FLAG2 + e * 8192); ok &= (f[0] == tag[0]) & (f[1] == tag[1]); }
      if (__all(ok)) break;
      __builtin_amdgcn_s_sleep(16);
    }
    if (b == 0 && lane == 0) ST64(tag, 0, WS_EPOCH);
    unsigned wooff[4];
#pragma unroll
    for (int j = 0; j < 4; ++j) wooff[j] = (unsigned)((lane >> 3) * 1024 + cb * 128 + (((lane & 7) ^ (((lane >> 4) & 1) << 2)) * 16) + j * 7168);
    VM_BAR(0);
#pragma unroll 1
    for (int p = 0; p < 8; ++p) glds4((const char*)wo + p * 32768, wooff, lds0 + L_WO + p * 4096);
    WAIT_VM(8);
#pragma unroll 1
    for (int p = 8; p < 16; ++p) glds4((const char*)wo + p * 32768, wooff, lds0 + L_WO + p * 4096);
    ffn(6, 11);
    WAIT_VM(0);
    STAMP(2);
    BAR();
    BAR(); BAR(); BAR();
    u32x4 pv[8];
    {
      const u32x4 own = nosent4(ldsr128(lds + L_ST + l16));
      ST128(own, l16, WS_XS + (pm * 8 + cb) * 1024);
      STAMP(6);
      __builtin_amdgcn_s_sleep(6);
#pragma unroll
      for (int e = 0; e < 8; ++e) pv[e] = LD128(l16, WS_XS + (pm * 8 + e) * 1024);
#pragma unroll 1
      for (unsigned spins = 0; spins < SPIN_CAP; ++spins) {
        bool miss[8], any = false;
#pragma unroll
        for (int e = 0; e < 8; ++e) { miss[e] = __any(umax4(pv[e]) == SENT); any |= miss[e]; }
        if (!any) break;
        FENCE();
#pragma unroll
        for (int e = 0; e < 8; ++e) if (miss[e]) pv[e] = LD128(l16, WS_XS + (pm * 8 + e) * 1024);
      }
    }
    STAMP(3);
#define SUM8(c) (((__uint_as_float(pv[0][c]) + __uint_as_float(pv[1][c])) + (__uint_as_float(pv[2][c]) + __uint_as_float(pv[3][c]))) + \
                 ((__uint_as_float(pv[4][c]) + __uint_as_float(pv[5][c])) + (__uint_as_float(pv[6][c]) + __uint_as_float(pv[7][c]))))
    const float mean = SUM8(0) * (1.f / D_);
    const float rstd = __builtin_amdgcn_rsqf(__builtin_fmaxf(SUM8(1) * (1.f / D_) - mean * mean, 0.f) + LN_EPS);
    if (cb == 0) {
      const unsigned u = __float_as_uint(rstd * (SUM8(2) - mean * sw));
      const unsigned ordv = (u & 0x80000000u) ? ~u : (u | 0x80000000u);
      __builtin_amdgcn_raw_buffer_store_b32(max((ordv >> 1) & 0x7FFFF800u, 0x800u) | (unsigned)(2047 - (pm * 64 + lane)), r_ws, lane * 4, WS_KEY + pm * 256, SC1);
    }
    *(LAS u32x2*)(lds + L_ST + 1024 + lane * 8) = u32x2{__float_as_uint(rstd), __float_as_uint(mean * rstd)};
    STAMP(4);
    LDS_BAR();
    STAMP_FLUSH();
    return;
  }

  if (wid >= 4) {
    const int wrd = 16384 + lw * 3072 + (8 * h + ((lane >> 2) & 3)) * 192 + 32 * ((lane >> 4) & 1) + 8 * (lane & 3);
    float cs[3] = {0.f, 0.f, 0.f};
#pragma unroll 1
    for (int c = 0; c < 8; ++c) {
      if (c < 7) WAIT_BAR(7); else WAIT_BAR(0);
      const LAS char* sb = lds + (c & 3) * K1_SLOT + wrd;
      u32x4 wf[3];
#pragma unroll
      for (int j = 0; j < 3; ++j) wf[j] = ldstr2(sb + j * 64, 768);
      if (c < 6) stage(c + 2);
#pragma unroll
      for (int e = 0; e < 4; ++e)
#pragma unroll
        for (int j = 0; j < 3; ++j) cs[j] = dot2bf(wf[j][e], ONE2, cs[j]);
    }
#pragma unroll
    for (int j = 0; j < 3; ++j) *(LAS float*)(lds + K1_CSP + (lw * 2 + h) * 384 + (32 * j + t) * 4) = cs[j];
    LDS_BAR();
    if (lw == 0 && lane < 24) {
      const LAS char* pp = lds + K1_CSP + l16;
      f32x4 sacc = *(const LAS f32x4*)pp;
#pragma unroll
      for (int e = 1; e < 8; ++e) sacc += *(const LAS f32x4*)(pp + e * 384);
      *(LAS f32x4*)(lds + K1_CS + l16) = sacc;
    }
    LDS_BAR();
    BAR();
  } else {
    if (cb1 < 10) qkv_body<0>(r_ws, lds, rb1, cb1, wid STAMP_PASS);
    else if (cb1 == 10) qkv_body<1>(r_ws, lds, rb1, cb1, wid STAMP_PASS);
    else qkv_body<3>(r_ws, lds, rb1, cb1, wid STAMP_PASS);
    WAIT_BAR(0);
    STAMP(4);
  }

  const bool onel = (lane & 16) != 0;
  unsigned kaddr = lds0 + L_KV + t * 32 + h * 16;
  unsigned vaddr = lds0 + (onel ? L_ONE + ((lane & 11) ? 16 : 0) : L_KV + 1024 + h * 256 + (lane & 15) * 16);
  const int vstep = onel ? 0 : 8192;
  const int IDX = 0xDDDDDDDD;
  const int row = pm * 64 + wid * 8 + (lane >> 3), col = cb * 64 + (lane & 7) * 8;
  f32x16 negm, sx, sy = f32x16{}, o = f32x16{};
  u32x4 vpb = u32x4{0, 0, 0, 0};
  asm volatile("" : "+{v[0:15]}"(o), "+{v[48:63]}"(sy), "+{v[84:87]}"(vpb));

  BAR();
  STAMP(5);
  const u32x4 xres = *(const u32x4*)(x + row * D_ + col), wrv = *(const u32x4*)(wr + col);
  const u32x4 qf = ldsr128(lds + L_Q + wid * 1024 + t * 32 + h * 16);
  u32x4 kn, va, vb;
  {
    const u32x4 k0 = ldsr128((const LAS char*)(uintptr_t)kaddr);
    kn = ldsr128((const LAS char*)(uintptr_t)kaddr + 2048);
    va = ldsr128((const LAS char*)(uintptr_t)vaddr); vb = ldsr128((const LAS char*)(uintptr_t)vaddr + 512);
    const float m = -rowmax16(mfma32(k0, qf, f32x16{}));
#pragma unroll
    for (int r = 0; r < 16; ++r) negm[r] = m;
    sx = mfma32(k0, qf, negm);
  }
  STAMP(6);
  {
    unsigned cnt;
    asm volatile(
        "s_mov_b32 %[cnt], 0\n\t"
        "s_nop 7\n\t"
        "s_cmp_gt_u32 %[wid], 3\n\t"
        "s_cbranch_scc1 3f\n\t"
        "s_setprio 1\n\t"
        ATTN_LOOP("1", "2", "", "")
        "s_setprio 0\n\t"
        "s_branch 5f\n\t"
        ATTN_LOOP("3", "4", "s_setprio 2\n\t", "s_setprio 0\n\t")
        "5:\n\t"
        "s_nop 1\n\t"
        SMF(84, 48, 8)
        "s_waitcnt lgkmcnt(0)\n\t"
        "s_nop 11\n\t"
        : "+{v[0:15]}"(o), "+{v[32:47]}"(sx), "+{v[48:63]}"(sy), "+{v[68:71]}"(kn), "+{v[72:75]}"(va), "+{v[76:79]}"(vb),
          "+{v[84:87]}"(vpb), "+{v89}"(kaddr), "+{v90}"(vaddr), [cnt] "=&s"(cnt)
        : "{v[16:31]}"(negm), "{v[64:67]}"(qf), "{v88}"(IDX), "{v91}"(vstep), [wid] "s"(wid)
        : "v80", "v81", "v82", "v83", "scc", "memory");
  }
  STAMP(7);
  {
    const float chk = ((__builtin_fabsf(o[0]) + __builtin_fabsf(o[1])) + (__builtin_fabsf(o[2]) + __builtin_fabsf(o[3]))) +
                      ((__builtin_fabsf(o[4]) + __builtin_fabsf(o[5])) + (__builtin_fabsf(o[6]) + __builtin_fabsf(o[7])));
    const bool bad = !(o[8] < 1.8446744e19f) || !(chk < 3.0e38f);
    if (__builtin_expect(__any(bad), 0)) {
      float m = -INFINITY;
      o = f32x16{};
      const int kq = head * 131072 + t * 32 + h * 16, vq = head * 131072 + 1024 + h * 256 + (lane & 15) * 16;
      const u32x4 ones = u32x4{ONE2, ONE2, ONE2, ONE2};
#pragma unroll 1
      for (int i = 0; i < 64; ++i) {
        f32x16 s = mfma32(LD128(kq, WS_KV + i * 2048), qf, f32x16{});
        u32x4 v0 = LD128(vq, WS_KV + i * 2048), v1 = LD128(vq + 512, WS_KV + i * 2048);
        if (onel) { v0 = ones; v1 = ones; }
        const float mn = __builtin_fmaxf(m, rowmax16(s));
        const float al = __builtin_amdgcn_exp2f(m - mn);
        m = mn;
#pragma unroll
        for (int r = 0; r < 16; ++r) { o[r] *= al; s[r] = __builtin_amdgcn_exp2f(s[r] - mn); }
        o = smfmac32(v0, lo8(s), o, IDX);
        o = smfmac32(v1, hi8(s), o, IDX);
      }
    }
  }
  {
    const float inv = __builtin_amdgcn_rcpf(o[8]);
    ST128(nosent4(u32x4{pkbf(o[0] * inv, o[1] * inv), pkbf(o[2] * inv, o[3] * inv), pkbf(o[4] * inv, o[5] * inv), pkbf(o[6] * inv, o[7] * inv)}),
          t * 32 + h * 16, WS_OF + (qb * 4 + (wid >> 1)) * 65536 + head * 2048 + (wid & 1) * 1024);
  }
  BAR();
  STAMP(8);

  const int mh = wid & 1, kq = wid >> 1;
  const int g = lane >> 4, q = (lane >> 2) & 3, p4 = lane & 3;
  f32x4 a0 = f32x4{bflo(xres[0]), bfhi(xres[0]), bflo(xres[1]), bfhi(xres[1])}, a1 = f32x4{bflo(xres[2]), bfhi(xres[2]), bflo(xres[3]), bfhi(xres[3])};
  unsigned ra0, ra1;
  {
    const int lr = wid * 8 + (lane >> 3);
    const int sl = ((lane & 7) ^ ((lr & 7) | ((lr & 1) << 3))) * 16;
    ra0 = lds0 + L_WO + lr * 256 + sl; ra1 = lds0 + L_WO + lr * 256 + (sl ^ 128);
  }
  LAS unsigned* cand = (LAS unsigned*)(lds + L_SEL);
  {
    u32x4 of[8], wf0[8], wf1[8];
    const int flip = ((q >> 1) & 1) << 6;
    const LAS char* wb = lds + L_WO + kq * 16384 + (8 * h + q) * 128 + 32 * (g & 1) + 8 * p4;
#pragma unroll
    for (int e = 0; e < 8; ++e) { wf0[e] = ldstr2(wb + flip + e * 2048, 512); wf1[e] = ldstr2(wb + (64 ^ flip) + e * 2048, 512); }
    const int ooff = t * 32 + h * 16, osoff = WS_OF + pm * 65536 + kq * 16384 + mh * 1024;
#pragma unroll
    for (int e = 0; e < 8; ++e) of[e] = LD128(ooff, osoff + e * 2048);
    if (tid < 80) cand[tid] = 0;
    unsigned pa[8];
    {
      const unsigned rb = lds0 + L_WO + kq * 16384 + (mh * 32 + t) * 256;
      const int swz = (t & 7) | ((t & 1) << 3);
#pragma unroll
      for (int gq = 0; gq < 8; ++gq) { pa[gq] = rb + (((h * 8 + gq) ^ swz) * 16); asm volatile("" : "+v"(pa[gq])); }
    }
    asm volatile("" : "+v"(a0), "+v"(a1), "+v"(ra0), "+v"(ra1));
    LDS_BAR();
    f32x16 acc0 = f32x16{}, acc1 = f32x16{};
#pragma unroll 1
    for (unsigned spins = 0; spins < SPIN_CAP; ++spins) {
      bool miss[8], any = false;
#pragma unroll
      for (int e = 0; e < 8; ++e) { miss[e] = __any(umax4(of[e]) == SENT); any |= miss[e]; }
      if (!any) break;
      FENCE();
#pragma unroll
      for (int e = 0; e < 8; ++e) if (miss[e]) of[e] = LD128(ooff, osoff + e * 2048);
    }
#pragma unroll
    for (int e = 0; e < 8; ++e) { acc0 = mfma32(wf0[e], of[e], acc0); acc1 = mfma32(wf1[e], of[e], acc1); }
    STAMP(9);
#pragma unroll
    for (int gq = 0; gq < 4; ++gq) {
      *(LAS f32x4*)(uintptr_t)pa[gq] = f32x4{acc0[4 * gq], acc0[4 * gq + 1], acc0[4 * gq + 2], acc0[4 * gq + 3]};
      *(LAS f32x4*)(uintptr_t)pa[4 + gq] = f32x4{acc1[4 * gq], acc1[4 * gq + 1], acc1[4 * gq + 2], acc1[4 * gq + 3]};
    }
  }
  LDS_BAR();
  STAMP(10);
  float hv[8];
  {
    const LAS char* r0 = (const LAS char*)(uintptr_t)ra0;
    const LAS char* r1 = (const LAS char*)(uintptr_t)ra1;
    f32x4 t0[4], t1[4];
#pragma unroll
    for (int e = 0; e < 4; ++e) { t0[e] = *(const LAS f32x4*)(r0 + e * 16384); t1[e] = *(const LAS f32x4*)(r1 + e * 16384); }
    __builtin_amdgcn_sched_barrier(0);
    a0 += (t0[0] + t0[1]) + (t0[2] + t0[3]);
    a1 += (t1[0] + t1[1]) + (t1[2] + t1[3]);
    const u32x4 hb = u32x4{pkbf(a0[0], a0[1]), pkbf(a0[2], a0[3]), pkbf(a1[0], a1[1]), pkbf(a1[2], a1[3])};
    float s1 = 0.f, s2 = 0.f, sd = 0.f;
#pragma unroll
    for (int e = 0; e < 4; ++e) { s1 = dot2bf(hb[e], ONE2, s1); s2 = dot2bf(hb[e], hb[e], s2); sd = dot2bf(hb[e], wrv[e], sd); }
    s1 = sum8(s1); s2 = sum8(s2); sd = sum8(sd);
    if ((lane & 7) == 0) *(LAS u32x4*)(lds + L_ST + wid * 128 + lane * 2) = u32x4{__float_as_uint(s1), __float_as_uint(s2), __float_as_uint(sd), 0u};
    LDS_BAR();
#pragma unroll
    for (int e = 0; e < 4; ++e) { hv[2 * e] = bflo(hb[e]); hv[2 * e + 1] = bfhi(hb[e]); }
  }
  STAMP(11);
  const int trd = (8 * g + q) * 32 + 8 * p4;
  u32x4 fw[8];
  if (isgu || isdn) {
#pragma unroll
    for (int p = 0; p < 8; ++p) {
      const int pc = isgu ? (p >> 2) * 16 + (wid & 3) * 4 + (p & 3) : min(p * 8 + wid, 42);
      fw[p] = ldstr2(lds + L_F + pc * 1024 + trd, 128);
    }
  }
  u32x4 k4;
  BAR();
  {
    STAMP(12);
    const u32x2 st = *(const LAS u32x2*)(lds + L_ST + 1024 + wid * 64 + (lane >> 3) * 8);
    const float rstd = __uint_as_float(st[0]), mr = __uint_as_float(st[1]);
    u32x4 hp, op;
#pragma unroll
    for (int e = 0; e < 4; ++e) {
      hp[e] = pkbf(hv[2 * e] * rstd - mr, hv[2 * e + 1] * rstd - mr);
      op[e] = pkbf(hv[2 * e] + bflo(hp[e]), hv[2 * e + 1] + bfhi(hp[e]));
    }
    ST128(nosent4(hp), row * 1024 + col * 2, WS_HN);
    __builtin_amdgcn_raw_buffer_store_b128(nosent4(op), r_out, row * 1024 + col * 2, 0, SC1);
  }
  STAMP(13);
  if (!isgu && !isdn) { STAMP_FLUSH(); return; }
#pragma unroll 1
  for (unsigned spins = 0; spins < SPIN_CAP; ++spins) {
    FENCE();
    k4 = LD128(tid * 16, WS_KEY);
    if (__all(umax4(k4) != SENT)) break;
  }
  STAMP(14);
  {
    unsigned m = umax4(k4);
    m = max(m, dppu<0xB1>(m)); m = max(m, dppu<0x4E>(m)); m = max(m, dppu<0x141>(m)); m = max(m, dppu<0x140>(m));
    m = max(m, dppu<0x142>(m)); m = max(m, dppu<0x143>(m));
    if (lane == 63) cand[64 + wid] = m;
  }
  LDS_BAR();
  {
    const u32x4 g0 = *(LAS u32x4*)(cand + 64), g1 = *(LAS u32x4*)(cand + 68);
    const unsigned T = min(min(min(g0[0], g0[1]), min(g0[2], g0[3])), min(min(g1[0], g1[1]), min(g1[2], g1[3])));
    const unsigned c0 = k4[0] >= T, c1 = k4[1] >= T, c2 = k4[2] >= T, c3 = k4[3] >= T;
    const unsigned cnt = (c0 + c1) + (c2 + c3);
    if (cnt) {
      unsigned pos;
      asm volatile("ds_add_rtn_u32 %0, %1, %2\n\ts_waitcnt lgkmcnt(0)" : "=v"(pos) : "v"((unsigned)(lds0 + L_SEL + 72 * 4)), "v"(cnt) : "memory");
      cand[c0 && pos < 64 ? pos : 73] = k4[0]; pos += c0;
      cand[c1 && pos < 64 ? pos : 73] = k4[1]; pos += c1;
      cand[c2 && pos < 64 ? pos : 73] = k4[2]; pos += c2;
      cand[c3 && pos < 64 ? pos : 73] = k4[3];
    }
  }
  LDS_BAR();
  LAS unsigned* sel = cand + 80 + wid * 8;
  {
    unsigned nv = cand[72];
    unsigned c = cand[t];
    u32x4 o4[4];
#pragma unroll
    for (int e = 0; e < 4; ++e) o4[e] = *(LAS u32x4*)(cand + h * 16 + e * 4);
    asm volatile("" : "+v"(nv), "+v"(c), "+v"(o4[0]), "+v"(o4[1]), "+v"(o4[2]), "+v"(o4[3]));
    const unsigned n = __builtin_amdgcn_readfirstlane(nv);
    if (n <= 64 && wid >= (isgu ? 4 : 1)) {
      if (isgu) { STAMP_FLUSH(); return; }
    } else if (__builtin_expect(n <= 32, 1)) {
      unsigned ba = 0, bb = 0;
#pragma unroll
      for (int e = 0; e < 4; ++e) {
        ba = __builtin_amdgcn_alignbit(ba, c - o4[e][0], 31); bb = __builtin_amdgcn_alignbit(bb, c - o4[e][1], 31);
        ba = __builtin_amdgcn_alignbit(ba, c - o4[e][2], 31); bb = __builtin_amdgcn_alignbit(bb, c - o4[e][3], 31);
      }
      unsigned rk = __builtin_popcount(ba) + __builtin_popcount(bb);
      auto r2 = __builtin_amdgcn_permlane32_swap(rk, rk, false, false);
      rk = r2[0] + r2[1];
      if (rk < 8) sel[rk] = c;
    } else if (n <= 64) {
      const unsigned c2 = cand[lane];
      unsigned rk = 0;
#pragma unroll 1
      for (int j = 0; j < 64; j += 4) { const u32x4 v = *(LAS u32x4*)(cand + j); rk += (v[0] > c2) + (v[1] > c2) + (v[2] > c2) + (v[3] > c2); }
      if (rk < 8) sel[rk] = c2;
    } else {
      LAS unsigned* all = cand + 256;
      *(LAS u32x4*)(all + tid * 4) = k4;
      LDS_BAR();
      unsigned rk[4] = {0, 0, 0, 0};
#pragma unroll 1
      for (int j = 0; j < 2048; ++j) {
        const unsigned v = all[j];
#pragma unroll
        for (int e = 0; e < 4; ++e) rk[e] += v > k4[e];
      }
#pragma unroll
      for (int e = 0; e < 4; ++e) if (rk[e] < 8) cand[160 + rk[e]] = k4[e];
      LDS_BAR();
      sel = cand + 160;
      if (isgu && wid >= 4) { STAMP_FLUSH(); return; }
    }
  }
  const int tok = lane & 15;
  const unsigned key = sel[tok & 7];
  const int trow = 2047 - (int)(key & 2047u);
  STAMP(15);
  LAS f32x4* red = (LAS f32x4*)(lds + L_WO);
  if (isgu) {
    u32x4 xb[4];
#pragma unroll 1
    for (unsigned spins = 0; spins < SPIN_CAP; ++spins) {
      FENCE();
#pragma unroll
      for (int u = 0; u < 4; ++u) xb[u] = LD128(trow * 1024 + u * 64 + g * 16, WS_HN + wid * 256);
      if (__all(max(max(umax4(xb[0]), umax4(xb[1])), max(umax4(xb[2]), umax4(xb[3]))) != SENT)) break;
    }
    STAMP(16);
    f32x4 ag = f32x4{}, au = f32x4{};
#pragma unroll
    for (int u = 0; u < 4; ++u) { ag = mfma16(fw[u], xb[u], ag); au = mfma16(fw[4 + u], xb[u], au); }
    const f32x4 mine = tok < 8 ? ag : au;
    if (wid != 0) red[wid * 64 + lane] = mine;
    LDS_BAR();
    if (wid == 0) {
      const f32x4 s = (mine + red[64 + lane]) + (red[128 + lane] + red[192 + lane]);
      float av[4];
#pragma unroll
      for (int r = 0; r < 4; ++r) {
        const float gg = rbf(s[r]);
        const float uu = dppf<0x128>(gg);
        const float sl = rbf(gg * __builtin_amdgcn_rcpf(1.f + __expf(-gg)));
        av[r] = sl * uu;
      }
      if (tok < 8) ST64((u32x2{nosent(pkbf(av[0], av[1])), nosent(pkbf(av[2], av[3]))}), tok * 32 + g * 8, WS_ACT + b * 256);
    }
    STAMP(17);
    STAMP_FLUSH();
    return;
  }
  const int nb = b - DN0;
  u32x2 o0 = u32x2{0, 0};
  float sg = 0.f;
  if (wid == 0) {
#pragma unroll 1
    for (unsigned spins = 0; spins < SPIN_CAP; ++spins) {
      FENCE();
      o0 = __builtin_amdgcn_raw_buffer_load_b64(r_out, trow * 1024 + g * 8, nb * 32, SC1);
      if (__all(max(o0[0], o0[1]) != SENT)) break;
    }
    const unsigned ov = ((key & 0x7FFFF800u) << 1) | 0x800u;
    const float r = rbf(__uint_as_float((ov & 0x80000000u) ? (ov & 0x7FFFFFFFu) : ~ov));
    sg = rbf(__builtin_amdgcn_rcpf(1.f + __expf(-r)));
  }
  const __amdgpu_buffer_rsrc_t r_act = mkrsrc(ws + WS_ACT, 22016);
  u32x4 ab[6];
  const int aoff = wid * 512 + (g >> 1) * 256 + (tok & 7) * 32 + (g & 1) * 16;
#pragma unroll
  for (int p = 0; p < 6; ++p) ab[p] = __builtin_amdgcn_raw_buffer_load_b128(r_act, aoff, p * 4096, SC1);
#pragma unroll 1
  for (unsigned spins = 0; spins < SPIN_CAP; ++spins) {
    bool miss[6], any = false;
#pragma unroll
    for (int p = 0; p < 6; ++p) { miss[p] = __any(umax4(ab[p]) == SENT); any |= miss[p]; }
    if (!any) break;
    FENCE();
#pragma unroll
    for (int p = 0; p < 6; ++p) if (miss[p]) ab[p] = __builtin_amdgcn_raw_buffer_load_b128(r_act, aoff, p * 4096, SC1);
  }
  STAMP(16);
  f32x4 ay = f32x4{};
#pragma unroll
  for (int p = 0; p < 6; ++p) ay = mfma16(fw[p], ab[p], ay);
  red[wid * 64 + lane] = ay;
  LDS_BAR();
  if (wid == 0 && tok < 8) {
    ay = ((ay + red[64 + lane]) + (red[128 + lane] + red[192 + lane])) + ((red[256 + lane] + red[320 + lane]) + (red[384 + lane] + red[448 + lane]));
    const float y0 = rbf(rbf(ay[0]) * sg), y1 = rbf(rbf(ay[1]) * sg), y2 = rbf(rbf(ay[2]) * sg), y3 = rbf(rbf(ay[3]) * sg);
    __builtin_amdgcn_raw_buffer_store_b64(u32x2{pkbf(bflo(o0[0]) + y0, bfhi(o0[0]) + y1), pkbf(bflo(o0[1]) + y2, bfhi(o0[1]) + y3)}, r_out,
                                          trow * 1024 + g * 8, nb * 32, SC1);
  }
  STAMP(17);
}

extern "C" void kernel_launch(void* const* d_in, const int* in_sizes, int n_in, void* d_out, int out_size, void* d_ws, size_t ws_size,
                              hipStream_t stream) {
  (void)hipFuncSetAttribute((const void*)fused_block, hipFuncAttributeMaxDynamicSharedMemorySize, LDS_BYTES);
  fused_block<<<256, 576, LDS_BYTES, stream>>>((const u16*)d_in[0], (const u16*)d_in[1], (const u16*)d_in[2], (const u16*)d_in[4],
                                               (const u16*)d_in[5], (const u16*)d_in[6], (const u16*)d_in[7], (char*)d_ws, (u16*)d_out);
}
```

```cpp
#define DIAG 1
#include <hip/hip_runtime.h>
#include <hip/hip_bf16.h>
#define STAMP_DECL unsigned long long tk = 0;
#define STAMP(s) do { if ((s) == DIAG) tk = __builtin_amdgcn_s_memrealtime(); } while (0)
#define STAMP_FLUSH()
#define STAMP_ARG , unsigned long long& tk
#define STAMP_PASS , tk

#define PROG 0
#define CODEPF 1
#define CODE_KB 25
typedef unsigned short u16;
typedef __bf16 bf16x2_t __attribute__((ext_vector_type(2)));
typedef __bf16 bf16x8_t __attribute__((ext_vector_type(8)));
typedef __bf16 bf16x16_t __attribute__((ext_vector_type(16)));
typedef short s16x4 __attribute__((ext_vector_type(4)));
typedef float f32x4 __attribute__((ext_vector_type(4)));
typedef float f32x8 __attribute__((ext_vector_type(8)));
typedef float f32x16 __attribute__((ext_vector_type(16)));
typedef unsigned u32x2 __attribute__((ext_vector_type(2)));
typedef unsigned u32x4 __attribute__((ext_vector_type(4)));
#define LAS __attribute__((address_space(3)))

constexpr int D_ = 512, F_ = 1376;
constexpr unsigned SENT = 0xFFFFFFFFu;
constexpr float LN_EPS = 1e-5f;
constexpr float QSCALE = 0.25f * 1.4426950408889634f;
constexpr unsigned SPIN_CAP = 1u << 16;
constexpr int SC1 = 16;
constexpr unsigned ONE2 = 0x3F803F80u;

constexpr unsigned WS_KV = 0;
constexpr unsigned WS_QF = 4u << 20;
constexpr unsigned WS_OF = 6u << 20;
constexpr unsigned WS_HN = 8u << 20;
constexpr unsigned WS_XS = 10u << 20;
constexpr unsigned WS_ACT = WS_XS + (256u << 10);
constexpr unsigned WS_KEY = WS_ACT + 22016;
constexpr unsigned WS_FLAG = WS_XS + (320u << 10);
constexpr unsigned WS_FLAG2 = WS_FLAG + 32768;
constexpr unsigned WS_EPOCH = WS_FLAG2 + 32768;
constexpr unsigned WS_BYTES = WS_EPOCH + 128;

__device__ __forceinline__ f32x16 mfma32(u32x4 a, u32x4 b, f32x16 c) {
  return __builtin_amdgcn_mfma_f32_32x32x16_bf16(__builtin_bit_cast(bf16x8_t, a), __builtin_bit_cast(bf16x8_t, b), c, 0, 0, 0);
}
__device__ __forceinline__ f32x4 mfma16(u32x4 a, u32x4 b, f32x4 c) {
  return __builtin_amdgcn_mfma_f32_16x16x32_bf16(__builtin_bit_cast(bf16x8_t, a), __builtin_bit_cast(bf16x8_t, b), c, 0, 0, 0);
}
__device__ __forceinline__ f32x16 smfmac32(u32x4 a, f32x8 b, f32x16 c, int idx) {
  return __builtin_amdgcn_smfmac_f32_32x32x32_bf16(__builtin_bit_cast(bf16x8_t, a), __builtin_bit_cast(bf16x16_t, b), c, idx, 0, 0);
}
__device__ __forceinline__ float dot2bf(unsigned a, unsigned b, float c) {
  return __builtin_amdgcn_fdot2_f32_bf16(__builtin_bit_cast(bf16x2_t, a), __builtin_bit_cast(bf16x2_t, b), c, false);
}
__device__ __forceinline__ float swap32_sum(float v) {
  auto r = __builtin_amdgcn_permlane32_swap(__float_as_uint(v), __float_as_uint(v), false, false);
  return __uint_as_float(r[0]) + __uint_as_float(r[1]);
}
__device__ __forceinline__ float swap32_max(float v) {
  auto r = __builtin_amdgcn_permlane32_swap(__float_as_uint(v), __float_as_uint(v), false, false);
  return __builtin_fmaxf(__uint_as_float(r[0]), __uint_as_float(r[1]));
}
__device__ __forceinline__ unsigned pkbf(float lo, float hi) {
  bf16x2_t v; v[0] = (__bf16)lo; v[1] = (__bf16)hi; return __builtin_bit_cast(unsigned, v);
}
__device__ __forceinline__ float bflo(unsigned u) { return __uint_as_float(u << 16); }
__device__ __forceinline__ float bfhi(unsigned u) { return __uint_as_float(u & 0xFFFF0000u); }
__device__ __forceinline__ float rbf(float f) { return (float)(__bf16)f; }
__device__ __forceinline__ void glds16(const void* sbase, unsigned voff, unsigned lds_base) {
  unsigned sv;
  asm volatile("s_mov_b32 %0, m0\n\ts_mov_b32 m0, %3\n\ts_nop 0\n\tglobal_load_lds_dwordx4 %1, %2\n\ts_mov_b32 m0, %0"
               : "=&s"(sv) : "v"(voff), "s"(sbase), "s"(lds_base) : "memory");
}
__device__ __forceinline__ void glds16c(const void* sbase, unsigned voff, unsigned lds_base) {
  unsigned sv;
  asm volatile("s_mov_b32 %0, m0\n\ts_mov_b32 m0, %3\n\ts_nop 0\n\tglobal_load_lds_dwordx4 %1, %2 sc1\n\ts_mov_b32 m0, %0"
               : "=&s"(sv) : "v"(voff), "s"(sbase), "s"(lds_base) : "memory");
}
__device__ __forceinline__ void glds8c(const void* sbase, unsigned voff, unsigned lds_base) {
  unsigned sv;
  asm volatile("s_mov_b32 %0, m0\n\ts_mov_b32 m0, %3\n\ts_nop 0\n\t"
               "global_load_lds_dwordx4 %1, %2 offset:-4096 sc1\n\tglobal_load_lds_dwordx4 %1, %2 offset:-3072 sc1\n\t"
               "global_load_lds_dwordx4 %1, %2 offset:-2048 sc1\n\tglobal_load_lds_dwordx4 %1, %2 offset:-1024 sc1\n\t"
               "global_load_lds_dwordx4 %1, %2 sc1\n\tglobal_load_lds_dwordx4 %1, %2 offset:1024 sc1\n\t"
               "global_load_lds_dwordx4 %1, %2 offset:2048 sc1\n\tglobal_load_lds_dwordx4 %1, %2 offset:3072 sc1\n\t"
               "s_mov_b32 m0, %0"
               : "=&s"(sv) : "v"(voff), "s"((const char*)sbase + 4096), "s"(lds_base + 4096) : "memory");
}
__device__ __forceinline__ void glds4(const void* sbase, const unsigned (&voff)[4], unsigned lds_base) {
  unsigned sv;
  asm volatile("s_mov_b32 %0, m0\n\ts_mov_b32 m0, %6\n\ts_nop 0\n\t"
               "global_load_lds_dwordx4 %1, %5\n\tglobal_load_lds_dwordx4 %2, %5 offset:1024\n\t"
               "global_load_lds_dwordx4 %3, %5 offset:2048\n\tglobal_load_lds_dwordx4 %4, %5 offset:3072\n\t"
               "s_mov_b32 m0, %0"
               : "=&s"(sv) : "v"(voff[0]), "v"(voff[1]), "v"(voff[2]), "v"(voff[3]), "s"(sbase), "s"(lds_base) : "memory");
}
#define FENCE() asm volatile("" ::: "memory")
#define WAIT_BAR(N) asm volatile("s_waitcnt vmcnt(" #N ") lgkmcnt(0)\n\ts_barrier" ::: "memory")
#define VM_BAR(N) asm volatile("s_waitcnt vmcnt(" #N ")\n\ts_barrier" ::: "memory")
#define LDS_BAR() do { FENCE(); __builtin_amdgcn_s_waitcnt(0xC07F); __builtin_amdgcn_s_barrier(); FENCE(); } while (0)
#define BAR() do { FENCE(); __builtin_amdgcn_s_barrier(); FENCE(); } while (0)
#define WAIT_VM(N) asm volatile("s_waitcnt vmcnt(" #N ")" ::: "memory")
#define LD128(off, soff) __builtin_amdgcn_raw_buffer_load_b128(r_ws, off, soff, SC1)
#define LD64(off, soff) __builtin_amdgcn_raw_buffer_load_b64(r_ws, off, soff, SC1)
#define ST128(v, off, soff) __builtin_amdgcn_raw_buffer_store_b128(v, r_ws, (off) + (soff), 0, SC1)
#define ST64(v, off, soff) __builtin_amdgcn_raw_buffer_store_b64(v, r_ws, off, soff, SC1)
__device__ __forceinline__ u32x4 ldsr128(const LAS char* p) { return *(const LAS u32x4*)p; }
__device__ __forceinline__ u32x2 ldstr(const LAS char* p) {
  return __builtin_bit_cast(u32x2, __builtin_amdgcn_ds_read_tr16_b64_v4i16((LAS s16x4*)p));
}
__device__ __forceinline__ u32x4 ldstr2(const LAS char* p, int off2) { const u32x2 a = ldstr(p), b = ldstr(p + off2); return u32x4{a[0], a[1], b[0], b[1]}; }
__device__ __forceinline__ int swapb23(int v) { return (v & ~12) | ((v & 4) << 1) | ((v & 8) >> 1); }
__device__ __forceinline__ unsigned nosent(unsigned v) { return min(v, 0xFFFFFFFEu); }
__device__ __forceinline__ u32x4 nosent4(u32x4 v) { return u32x4{nosent(v[0]), nosent(v[1]), nosent(v[2]), nosent(v[3])}; }
__device__ __forceinline__ unsigned umax4(u32x4 v) { return max(max(v[0], v[1]), max(v[2], v[3])); }
__device__ __forceinline__ __amdgpu_buffer_rsrc_t mkrsrc(const void* p, unsigned bytes) {
  return __builtin_amdgcn_make_buffer_rsrc((void*)p, 0, bytes, 0x00020000);
}
template <int CTRL> __device__ __forceinline__ float dppf(float v) {
  return __uint_as_float(__builtin_amdgcn_update_dpp(0, __float_as_uint(v), CTRL, 0xF, 0xF, true));
}
template <int CTRL> __device__ __forceinline__ unsigned dppu(unsigned v) { return __builtin_amdgcn_update_dpp(0, v, CTRL, 0xF, 0xF, true); }
__device__ __forceinline__ unsigned rdl(unsigned v, int l) { return (unsigned)__builtin_amdgcn_readlane((int)v, l); }
__device__ __forceinline__ float rdlf(float v, int l) { return __uint_as_float(rdl(__float_as_uint(v), l)); }
__device__ __forceinline__ float sum8(float v) { v += dppf<0xB1>(v); v += dppf<0x4E>(v); v += dppf<0x141>(v); return v; }
__device__ __forceinline__ u32x4 pack8(const float* v) { return u32x4{pkbf(v[0], v[1]), pkbf(v[2], v[3]), pkbf(v[4], v[5]), pkbf(v[6], v[7])}; }

constexpr int K1_SLOT = 16384 + 12288, K1_CS = 4 * K1_SLOT, K1_CSP = K1_CS + 512, K1_TAG = K1_CSP + 8 * 384;
constexpr int L_Q = 0, L_KV = 24576, L_ONE = L_KV + 131072, LDS_BYTES = 163840;
constexpr int L_F = 0, L_WO = 45056, L_SEL = 114688, L_ST = L_SEL + 12288;
constexpr int NGU = 86, DN0 = 128, NDN = 32;

template <int NV>
__device__ __forceinline__ void qkv_body(const __amdgpu_buffer_rsrc_t r_ws, LAS char* lds, int rb, int cb, int wid STAMP_ARG) {
  const int lane = threadIdx.x & 63, t = lane & 31, h = lane >> 5;
  int xr[4];
#pragma unroll
  for (int u = 0; u < 4; ++u) xr[u] = wid * 4096 + t * 128 + (((2 * u + h) ^ ((t >> 1) & 7)) * 16);
  const int wrd = 16384 + (8 * h + ((lane >> 2) & 3)) * 192 + 32 * ((lane >> 4) & 1) + 8 * (lane & 3);
  f32x16 acc[3];
#pragma unroll
  for (int j = 0; j < 3; ++j) acc[j] = f32x16{};
  float s1a = 0.f, s1b = 0.f, s2a = 0.f, s2b = 0.f;
  u32x4 xf = u32x4{0, 0, 0, 0}, wf[3];
#pragma unroll
  for (int j = 0; j < 3; ++j) wf[j] = u32x4{0, 0, 0, 0};
  auto mm = [&]() {
#pragma unroll
    for (int j = 0; j < 3; ++j) {
      if (j >= 3 - NV) acc[j] = mfma32(xf, wf[j], acc[j]);
      else acc[j] = mfma32(wf[j], xf, acc[j]);
    }
  };
  const int tile = rb * 4 + wid;
  const int voffV = tile * 2048 + 1024 + (t >> 4) * 131072 + h * 256 + swapb23(t & 15) * 16;
  const int voffK = tile * 2048 + swapb23(t) * 32 + h * 16;
  const int voffQ = tile * 1024 + t * 32 + h * 16;
#pragma unroll 1
  for (int c = 0; c < 8; ++c) {
    LDS_BAR();
    if (c == 0) STAMP(1);
    const LAS char* sb = lds + (c & 3) * K1_SLOT;
    u32x4 xn[4];
#pragma unroll
    for (int u = 0; u < 4; ++u) {
      xn[u] = ldsr128(sb + xr[u]);
      u32x4 wn[3];
#pragma unroll
      for (int j = 0; j < 3; ++j) wn[j] = ldstr2(sb + wrd + u * 3072 + j * 64, 768);
      mm();
#pragma unroll
      for (int z = 0; z < 3; ++z) { __builtin_amdgcn_sched_group_barrier(0x8, 1, 0); __builtin_amdgcn_sched_group_barrier(0x102, 3, 0); }
      __builtin_amdgcn_sched_barrier(0);
      xf = xn[u];
#pragma unroll
      for (int j = 0; j < 3; ++j) wf[j] = wn[j];
    }
#pragma unroll
    for (int u = 0; u < 4; ++u)
#pragma unroll
      for (int e = 0; e < 4; e += 2) {
        s1a = dot2bf(xn[u][e], ONE2, s1a); s2a = dot2bf(xn[u][e], xn[u][e], s2a);
        s1b = dot2bf(xn[u][e + 1], ONE2, s1b); s2b = dot2bf(xn[u][e + 1], xn[u][e + 1], s2b);
      }
    asm volatile("" : "+v"(s1a), "+v"(s1b), "+v"(s2a), "+v"(s2b));
    __builtin_amdgcn_sched_barrier(0);
  }
  __builtin_amdgcn_s_barrier();
  mm();
  STAMP(2);
  LAS float* scr = (LAS float*)(lds + wid * 4096);
  const LAS float* csum = (const LAS float*)(lds + K1_CS);
  const float s2 = swap32_sum(s2a + s2b);
  const float mean = swap32_sum(s1a + s1b) * (1.f / D_);
  const float var = __builtin_fmaxf(s2 * (1.f / D_) - mean * mean, 0.f);
  const float rstd = __builtin_amdgcn_rsqf(var + LN_EPS);
  if (NV > 0 && h == 0) { scr[t] = rstd; scr[32 + t] = mean * rstd; }
  BAR();
#pragma unroll
  for (int j = 0; j < 3; ++j) {
    const int gt = cb * 3 + j;
    float v[16];
    if (j >= 3 - NV) {
      const float cs = csum[32 * j + t];
#pragma unroll
      for (int gq = 0; gq < 4; ++gq) {
        const f32x4 a = *(const LAS f32x4*)(scr + 8 * gq + 4 * h), b = *(const LAS f32x4*)(scr + 32 + 8 * gq + 4 * h);
#pragma unroll
        for (int e = 0; e < 4; ++e) v[4 * gq + e] = acc[j][4 * gq + e] * a[e] - b[e] * cs;
      }
      const int so = WS_KV + (gt - 32) * 262144;
      ST128(pack8(v), voffV, so); ST128(pack8(v + 8), voffV + 512, so);
    } else {
      const bool isq = gt < 16;
      const float sc = isq ? QSCALE : 1.f;
      const float a = rstd * sc, b = mean * rstd * sc;
#pragma unroll
      for (int gq = 0; gq < 4; ++gq) {
        const f32x4 c4 = *(const LAS f32x4*)(csum + 32 * j + 8 * gq + 4 * h);
#pragma unroll
        for (int e = 0; e < 4; ++e) v[4 * gq + e] = acc[j][4 * gq + e] * a - b * c4[e];
      }
      const int vo = isq ? voffQ : voffK;
      const int so = isq ? WS_QF + gt * 131072 : WS_KV + (gt - 16) * 262144;
      ST128(pack8(v), vo, so); ST128(pack8(v + 8), vo, so + (isq ? 65536 : 131072));
    }
  }
  STAMP(3);
}

__device__ __forceinline__ float rowmax16(const f32x16& s) {
  float a = __builtin_fmaxf(__builtin_fmaxf(s[0], s[1]), s[2]), b = __builtin_fmaxf(__builtin_fmaxf(s[3], s[4]), s[5]);
  a = __builtin_fmaxf(__builtin_fmaxf(a, s[6]), s[7]); b = __builtin_fmaxf(__builtin_fmaxf(b, s[8]), s[9]);
  a = __builtin_fmaxf(__builtin_fmaxf(a, s[10]), s[11]); b = __builtin_fmaxf(__builtin_fmaxf(b, s[12]), s[13]);
  a = __builtin_fmaxf(__builtin_fmaxf(a, s[14]), s[15]);
  return swap32_max(__builtin_fmaxf(a, b));
}
__device__ __forceinline__ f32x8 lo8(const f32x16& s) { return __builtin_shufflevector(s, s, 0, 1, 2, 3, 4, 5, 6, 7); }
__device__ __forceinline__ f32x8 hi8(const f32x16& s) { return __builtin_shufflevector(s, s, 8, 9, 10, 11, 12, 13, 14, 15); }

#define E1(X, i) "v_exp_f32 v[" #X "+" #i "], v[" #X "+" #i "]\n\t"
#define SMF(A, B, off) "v_smfmac_f32_32x32x32_bf16 v[0:15], v[" #A ":" #A "+3], v[" #B "+" #off ":" #B "+" #off "+7], v88\n\t"
#define QK(Y) "v_mfma_f32_32x32x16_bf16 v[" #Y ":" #Y "+15], v[68:71], v[64:67], v[16:31]\n\t"
#define RDV(D, off) "ds_read_b128 v[" #D ":" #D "+3], v90 offset:" #off "\n\t"
#define RDK(off) "ds_read_b128 v[68:71], v89 offset:" #off "\n\t"
#define STEP(X, Y, VA, VPB, VA2, VB2, KOFF, VOFF, VOFF2, PA, PB, EXTRA) \
  PA E1(X, 0) E1(X, 1) SMF(VPB, Y, 8) RDV(VA2, VOFF) \
  PB E1(X, 2) E1(X, 3) E1(X, 4) E1(X, 5) \
  "s_waitcnt lgkmcnt(1)\n\t" QK(Y) RDV(VB2, VOFF2) RDK(KOFF) \
  E1(X, 6) E1(X, 7) E1(X, 8) E1(X, 9) \
  SMF(VA, X, 0) EXTRA \
  E1(X, 10) E1(X, 11) E1(X, 12) E1(X, 13) E1(X, 14) E1(X, 15)
#define ATTN_LOOP(L1, L2, PHI, PLO) \
        L1 ":\n\t" \
        "s_bitcmp1_b32 0x824, %[cnt]\n\t" \
        "s_cbranch_scc0 " L2 "f\n\t" \
        "s_barrier\n\t" \
        L2 ":\n\t" \
        STEP(32, 48, 72, 84, 80, 84, 4096, 2048, 2560, PHI, "", "") \
        STEP(48, 32, 80, 76, 72, 76, 6144, 4096, 4608, "", "", "") \
        STEP(32, 48, 72, 84, 80, 84, 8192, 6144, 6656, "", "", "v_add_u32 v90, v90, v91\n\t") \
        STEP(48, 32, 80, 76, 72, 76, 10240, 0, 512, "", PLO, "v_add_u32 v89, 0x2000, v89\n\ts_add_u32 %[cnt], %[cnt], 1\n\ts_cmp_lg_u32 %[cnt], 16\n\t") \
        "s_cbranch_scc1 " L1 "b\n\t"

__global__ __launch_bounds__(576, 3) void fused_block(const u16* __restrict__ x, const u16* __restrict__ wqkv, const u16* __restrict__ wo,
                                                      const u16* __restrict__ wr, const u16* __restrict__ wg, const u16* __restrict__ wu,
                                                      const u16* __restrict__ wd, char* __restrict__ ws, u16* __restrict__ out) {
  extern __shared__ __attribute__((aligned(16))) char smem[];
  LAS char* lds = (LAS char*)smem;
  const unsigned lds0 = (unsigned)(uintptr_t)lds;
  const unsigned long long t00 = __builtin_amdgcn_s_memrealtime();
  {
    unsigned long long pc; unsigned s0, s1, v0;
    asm volatile("v_readfirstlane_b32 %1, %4\n\ts_lshr_b32 %2, %5, 3\n\ts_getpc_b64 %0\n\ts_and_b64 %0, %0, -16\n\t"
                 "s_cmp_lt_u32 %1, 64\n\ts_cbranch_scc0 9f\n\t"
                 "s_cmp_lt_u32 %2, %6\n\ts_cbranch_scc0 9f\n\t"
                 "s_lshl_b32 %2, %2, 10\n\tv_lshlrev_b32 %3, 4, %4\n\tv_add_u32 %3, %2, %3\n\t"
                 "s_mov_b32 %1, m0\n\ts_mov_b32 m0, %7\n\ts_nop 0\n\t"
                 "global_load_lds_dwordx4 %3, %0\n\ts_mov_b32 m0, %1\n\t9:"
                 : "=&s"(pc), "=&s"(s0), "=&s"(s1), "=&v"(v0) : "v"(threadIdx.x), "s"(blockIdx.x), "i"(CODE_KB), "s"(lds0 + 131072) : "scc", "memory");
  }
  const int tid = threadIdx.x, lane = tid & 63, t = lane & 31, h = lane >> 5;
  const int wid = __builtin_amdgcn_readfirstlane(tid >> 6);
  const int b = blockIdx.x;
  const int rb1 = 4 * (b & 3) + ((b >> 3) & 3), cb1 = 8 * ((b >> 2) & 1) + (b >> 5);
  const int head = (b & 7) * 4 + ((b >> 3) & 3), qb = b >> 5;
  const int pm = head, cb = qb;
  const unsigned l16 = lane * 16;
  const int lw = wid - 4;
  const int rot = ((((b >> 5) + ((b >> 3) & 3)) & 1)) * 4;
  unsigned xoff[2], woff[3];
  auto stage = [&](int c) {
    const int kc = (c + rot) & 7;
    const char* xs = (const char*)x + kc * 128;
    const char* wsrc = (const char*)wqkv + kc * (64 * 3072);
    const unsigned base = lds0 + (c & 3) * K1_SLOT;
#pragma unroll
    for (int p = 0; p < 4; ++p) glds16(xs + (p >> 1) * 16384, xoff[p & 1], base + lw * 4096 + p * 1024);
#pragma unroll
    for (int p = 0; p < 3; ++p) glds16(wsrc, woff[p], base + 16384 + (lw * 3 + p) * 1024);
  };
  if (lw >= 0 && lw < 4) {
#pragma unroll
    for (int p = 0; p < 2; ++p) {
      const int row = p * 8 + (lane >> 3), slot = lane & 7;
      xoff[p] = (unsigned)((rb1 * 128 + lw * 32 + row) * 1024 + ((slot ^ ((row >> 1) & 7)) * 16));
    }
#pragma unroll
    for (int p = 0; p < 3; ++p) {
      const int idx = (lw * 3 + p) * 64 + lane, k = idx / 12, c = idx - k * 12;
      woff[p] = (unsigned)(k * 3072 + cb1 * 192 + c * 16);
    }
    stage(0); stage(1);
  }
  FENCE();
  const bool isgu = b < NGU, isdn = b >= DN0 && b < DN0 + NDN;
  const __amdgpu_buffer_rsrc_t r_ws = mkrsrc(ws, WS_BYTES), r_out = mkrsrc(out, 2u << 20);
  STAMP_DECL STAMP(0);

  if (wid == 8) {
    __builtin_amdgcn_s_setprio(3);
    const u32x2 ep = LD64(0, WS_EPOCH);
    const u32x4 w4 = *(const u32x4*)(wr + lane * 8);
    if (lane < 16) { const unsigned v = (lane & 1) ? 0u : ONE2; *(LAS u32x4*)(lds + L_ONE + (lane >> 2) * 2048 + ((lane >> 1) & 1) * 512 + (lane & 1) * 16) = u32x4{v, v, v, v}; }
#pragma unroll 1
    for (int c = 0; c < 5; ++c) BAR();
    const unsigned long long e64 = (((unsigned long long)ep[1] << 32) | ep[0]) + 1ull;
    const u32x2 tag = u32x2{(unsigned)e64, (unsigned)(e64 >> 32)};
    float sw;
    sw = dot2bf(w4[0], ONE2, dot2bf(w4[1], ONE2, 0.f)) + dot2bf(w4[2], ONE2, dot2bf(w4[3], ONE2, 0.f));
    sw = sum8(sw); sw += dppf<0x140>(sw);
    sw = (rdlf(sw, 0) + rdlf(sw, 16)) + (rdlf(sw, 32) + rdlf(sw, 48));
    const int hp = head >> 1;
    const int fidx = lane < 16 ? lane * 16 + (16 + hp) / 3 : lane < 32 ? (lane - 16) * 16 + (32 + hp) / 3 : (qb * 2 + (lane & 1)) * 16 + hp / 3;
    const char* kvsrc = ws + WS_KV + (size_t)head * 131072;
    LDS_BAR();
#pragma unroll 1
    for (int c = 6; c < 10; ++c) BAR();
    BAR();
    if (lane == 0) ST64(tag, 0, WS_FLAG + (rb1 * 16 + cb1) * 128);
    STAMP(1);
    unsigned long long seen = 0;
    auto wait_flags = [&](unsigned long long need) {
#pragma unroll 1
      for (unsigned spins = 0; spins < SPIN_CAP && (seen & need) != need; ++spins) {
        FENCE();
        const u32x2 f = LD64(fidx * 128, WS_FLAG);
        seen = __ballot((f[0] == tag[0]) & (f[1] == tag[1]));
      }
      FENCE();
    };
    wait_flags(0x300070007ull);
    STAMP(5);
    glds8c(ws + WS_QF + head * 65536 + qb * 8192, l16, lds0 + L_Q);
#pragma unroll
    for (int p = 0; p < 3; ++p) glds8c(kvsrc + p * 8192, l16, lds0 + L_KV + p * 8192);
    VM_BAR(0);
    wait_flags(0xFFFFFFFFull);
#pragma unroll 1
    for (int p = 3; p < 10; ++p) glds8c(kvsrc + p * 8192, l16, lds0 + L_KV + p * 8192);
    VM_BAR(32);
    WAIT_VM(8);
#pragma unroll 1
    for (int p = 10; p < 16; ++p) glds8c(kvsrc + p * 8192, l16, lds0 + L_KV + p * 8192);
    VM_BAR(0);
    {
      const u32x4 s4 = u32x4{SENT, SENT, SENT, SENT};
#pragma unroll
      for (int e = 0; e < 8; ++e) ST128(s4, l16, WS_OF + (qb * 4 + (e >> 1)) * 65536 + head * 2048 + (e & 1) * 1024);
#pragma unroll
      for (int e = 0; e < 8; ++e) {
        const int off = (pm * 64 + e * 8 + (lane >> 3)) * 1024 + cb * 128 + (lane & 7) * 16;
        ST128(s4, off, WS_HN);
        __builtin_amdgcn_raw_buffer_store_b128(s4, r_out, off, 0, SC1);
      }
      ST128(s4, l16, WS_XS + (pm * 8 + cb) * 1024);
      if (lane < 8 && b * 8 + lane < (22016 + 8192) / 16) ST128(s4, l16, WS_ACT + b * 128);
      WAIT_VM(0);
      if (lane == 0) ST64(tag, 0, WS_FLAG2 + b * 128);
    }
    unsigned foff[4];
    const unsigned fstr = isgu ? 32 * F_ * 2 : 32768;
#pragma unroll
    for (int j = 0; j < 4; ++j)
      foff[j] = (isgu ? (unsigned)((lane >> 1) * (F_ * 2) + b * 32) : (unsigned)((lane >> 1) * 1024 + (b - DN0) * 32)) + (lane & 1) * 16 + j * (fstr - 1024);
    auto ffn = [&](int q0, int q1) {
      if (isgu) {
#pragma unroll 1
        for (int p = q0; p < min(q1, 8); ++p) glds4((const char*)(p < 4 ? wg : wu) + (p & 3) * (128 * F_ * 2), foff, lds0 + L_F + p * 4096);
      } else if (isdn) {
#pragma unroll 1
        for (int p = q0; p < min(q1, 10); ++p) glds4((const char*)wd + p * 131072, foff, lds0 + L_F + p * 4096);
        if (q1 > 10) {
#pragma unroll 1
          for (int p = 40; p < 43; ++p) glds16((const char*)wd + p * 32768, foff[0], lds0 + L_F + p * 1024);
        }
      }
    };
    ffn(0, 6);
#pragma unroll 1
    for (unsigned spins = 0; spins < SPIN_CAP; ++spins) {
      FENCE();
      bool ok = true;
#pragma unroll
      for (int e = 0; e < 4; ++e) { const u32x2 f = LD64(lane * 128, WS_FLAG2 + e * 8192); ok &= (f[0] == tag[0]) & (f[1] == tag[1]); }
      if (__all(ok)) break;
      __builtin_amdgcn_s_sleep(16);
    }
    if (b == 0 && lane == 0) ST64(tag, 0, WS_EPOCH);
    unsigned wooff[4];
#pragma unroll
    for (int j = 0; j < 4; ++j) wooff[j] = (unsigned)((lane >> 3) * 1024 + cb * 128 + (((lane & 7) ^ (((lane >> 4) & 1) << 2)) * 16) + j * 7168);
    VM_BAR(0);
#pragma unroll 1
    for (int p = 0; p < 8; ++p) glds4((const char*)wo + p * 32768, wooff, lds0 + L_WO + p * 4096);
    WAIT_VM(8);
#pragma unroll 1
    for (int p = 8; p < 16; ++p) glds4((const char*)wo + p * 32768, wooff, lds0 + L_WO + p * 4096);
    ffn(6, 11);
    WAIT_VM(0);
    STAMP(2);
    BAR();
    BAR(); BAR(); BAR();
    u32x4 pv[8];
    {
      const u32x4 own = nosent4(ldsr128(lds + L_ST + l16));
      ST128(own, l16, WS_XS + (pm * 8 + cb) * 1024);
      STAMP(6);
      __builtin_amdgcn_s_sleep(6);
#pragma unroll
      for (int e = 0; e < 8; ++e) pv[e] = LD128(l16, WS_XS + (pm * 8 + e) * 1024);
#pragma unroll 1
      for (unsigned spins = 0; spins < SPIN_CAP; ++spins) {
        bool miss[8], any = false;
#pragma unroll
        for (int e = 0; e < 8; ++e) { miss[e] = __any(umax4(pv[e]) == SENT); any |= miss[e]; }
        if (!any) break;
        FENCE();
#pragma unroll
        for (int e = 0; e < 8; ++e) if (miss[e]) pv[e] = LD128(l16, WS_XS + (pm * 8 + e) * 1024);
      }
    }
    STAMP(3);
#define SUM8(c) (((__uint_as_float(pv[0][c]) + __uint_as_float(pv[1][c])) + (__uint_as_float(pv[2][c]) + __uint_as_float(pv[3][c]))) + \
                 ((__uint_as_float(pv[4][c]) + __uint_as_float(pv[5][c])) + (__uint_as_float(pv[6][c]) + __uint_as_float(pv[7][c]))))
    const float mean = SUM8(0) * (1.f / D_);
    const float rstd = __builtin_amdgcn_rsqf(__builtin_fmaxf(SUM8(1) * (1.f / D_) - mean * mean, 0.f) + LN_EPS);
    if (cb == 0) {
      const unsigned u = __float_as_uint(rstd * (SUM8(2) - mean * sw));
      const unsigned ordv = (u & 0x80000000u) ? ~u : (u | 0x80000000u);
      __builtin_amdgcn_raw_buffer_store_b32(max((ordv >> 1) & 0x7FFFF800u, 0x800u) | (unsigned)(2047 - (pm * 64 + lane)), r_ws, lane * 4, WS_KEY + pm * 256, SC1);
    }
    *(LAS u32x2*)(lds + L_ST + 1024 + lane * 8) = u32x2{__float_as_uint(rstd), __float_as_uint(mean * rstd)};
    STAMP(4);
    LDS_BAR();
    STAMP_FLUSH();
    return;
  }

  if (wid >= 4) {
    const int wrd = 16384 + lw * 3072 + (8 * h + ((lane >> 2) & 3)) * 192 + 32 * ((lane >> 4) & 1) + 8 * (lane & 3);
    float cs[3] = {0.f, 0.f, 0.f};
#pragma unroll 1
    for (int c = 0; c < 8; ++c) {
      if (c < 7) WAIT_BAR(7); else WAIT_BAR(0);
      const LAS char* sb = lds + (c & 3) * K1_SLOT + wrd;
      u32x4 wf[3];
#pragma unroll
      for (int j = 0; j < 3; ++j) wf[j] = ldstr2(sb + j * 64, 768);
      if (c < 6) stage(c + 2);
#pragma unroll
      for (int e = 0; e < 4; ++e)
#pragma unroll
        for (int j = 0; j < 3; ++j) cs[j] = dot2bf(wf[j][e], ONE2, cs[j]);
    }
#pragma unroll
    for (int j = 0; j < 3; ++j) *(LAS float*)(lds + K1_CSP + (lw * 2 + h) * 384 + (32 * j + t) * 4) = cs[j];
    LDS_BAR();
    if (lw == 0 && lane < 24) {
      const LAS char* pp = lds + K1_CSP + l16;
      f32x4 sacc = *(const LAS f32x4*)pp;
#pragma unroll
      for (int e = 1; e < 8; ++e) sacc += *(const LAS f32x4*)(pp + e * 384);
      *(LAS f32x4*)(lds + K1_CS + l16) = sacc;
    }
    LDS_BAR();
    BAR();
  } else {
    if (cb1 < 10) qkv_body<0>(r_ws, lds, rb1, cb1, wid STAMP_PASS);
    else if (cb1 == 10) qkv_body<1>(r_ws, lds, rb1, cb1, wid STAMP_PASS);
    else qkv_body<3>(r_ws, lds, rb1, cb1, wid STAMP_PASS);
    WAIT_BAR(0);
    STAMP(4);
  }

  const bool onel = (lane & 16) != 0;
  unsigned kaddr = lds0 + L_KV + t * 32 + h * 16;
  unsigned vaddr = lds0 + (onel ? L_ONE + ((lane & 11) ? 16 : 0) : L_KV + 1024 + h * 256 + (lane & 15) * 16);
  const int vstep = onel ? 0 : 8192;
  const int IDX = 0xDDDDDDDD;
  const int row = pm * 64 + wid * 8 + (lane >> 3), col = cb * 64 + (lane & 7) * 8;
  f32x16 negm, sx, sy = f32x16{}, o = f32x16{};
  u32x4 vpb = u32x4{0, 0, 0, 0};
  asm volatile("" : "+{v[0:15]}"(o), "+{v[48:63]}"(sy), "+{v[84:87]}"(vpb));

  BAR();
  STAMP(5);
  const u32x4 xres = *(const u32x4*)(x + row * D_ + col), wrv = *(const u32x4*)(wr + col);
  const u32x4 qf = ldsr128(lds + L_Q + wid * 1024 + t * 32 + h * 16);
  u32x4 kn, va, vb;
  {
    const u32x4 k0 = ldsr128((const LAS char*)(uintptr_t)kaddr);
    kn = ldsr128((const LAS char*)(uintptr_t)kaddr + 2048);
    va = ldsr128((const LAS char*)(uintptr_t)vaddr); vb = ldsr128((const LAS char*)(uintptr_t)vaddr + 512);
    const float m = -rowmax16(mfma32(k0, qf, f32x16{}));
#pragma unroll
    for (int r = 0; r < 16; ++r) negm[r] = m;
    sx = mfma32(k0, qf, negm);
  }
  STAMP(6);
  {
    unsigned cnt;
    asm volatile(
        "s_mov_b32 %[cnt], 0\n\t"
        "s_nop 7\n\t"
        "s_cmp_gt_u32 %[wid], 3\n\t"
        "s_cbranch_scc1 3f\n\t"
        "s_setprio 1\n\t"
        ATTN_LOOP("1", "2", "", "")
        "s_setprio 0\n\t"
        "s_branch 5f\n\t"
        ATTN_LOOP("3", "4", "s_setprio 2\n\t", "s_setprio 0\n\t")
        "5:\n\t"
        "s_nop 1\n\t"
        SMF(84, 48, 8)
        "s_waitcnt lgkmcnt(0)\n\t"
        "s_nop 11\n\t"
        : "+{v[0:15]}"(o), "+{v[32:47]}"(sx), "+{v[48:63]}"(sy), "+{v[68:71]}"(kn), "+{v[72:75]}"(va), "+{v[76:79]}"(vb),
          "+{v[84:87]}"(vpb), "+{v89}"(kaddr), "+{v90}"(vaddr), [cnt] "=&s"(cnt)
        : "{v[16:31]}"(negm), "{v[64:67]}"(qf), "{v88}"(IDX), "{v91}"(vstep), [wid] "s"(wid)
        : "v80", "v81", "v82", "v83", "scc", "memory");
  }
  STAMP(7);
  {
    const float chk = ((__builtin_fabsf(o[0]) + __builtin_fabsf(o[1])) + (__builtin_fabsf(o[2]) + __builtin_fabsf(o[3]))) +
                      ((__builtin_fabsf(o[4]) + __builtin_fabsf(o[5])) + (__builtin_fabsf(o[6]) + __builtin_fabsf(o[7])));
    const bool bad = !(o[8] < 1.8446744e19f) || !(chk < 3.0e38f);
    if (__builtin_expect(__any(bad), 0)) {
      float m = -INFINITY;
      o = f32x16{};
      const int kq = head * 131072 + t * 32 + h * 16, vq = head * 131072 + 1024 + h * 256 + (lane & 15) * 16;
      const u32x4 ones = u32x4{ONE2, ONE2, ONE2, ONE2};
#pragma unroll 1
      for (int i = 0; i < 64; ++i) {
        f32x16 s = mfma32(LD128(kq, WS_KV + i * 2048), qf, f32x16{});
        u32x4 v0 = LD128(vq, WS_KV + i * 2048), v1 = LD128(vq + 512, WS_KV + i * 2048);
        if (onel) { v0 = ones; v1 = ones; }
        const float mn = __builtin_fmaxf(m, rowmax16(s));
        const float al = __builtin_amdgcn_exp2f(m - mn);
        m = mn;
#pragma unroll
        for (int r = 0; r < 16; ++r) { o[r] *= al; s[r] = __builtin_amdgcn_exp2f(s[r] - mn); }
        o = smfmac32(v0, lo8(s), o, IDX);
        o = smfmac32(v1, hi8(s), o, IDX);
      }
    }
  }
  {
    const float inv = __builtin_amdgcn_rcpf(o[8]);
    ST128(nosent4(u32x4{pkbf(o[0] * inv, o[1] * inv), pkbf(o[2] * inv, o[3] * inv), pkbf(o[4] * inv, o[5] * inv), pkbf(o[6] * inv, o[7] * inv)}),
          t * 32 + h * 16, WS_OF + (qb * 4 + (wid >> 1)) * 65536 + head * 2048 + (wid & 1) * 1024);
  }
  BAR();
  STAMP(8);

  const int mh = wid & 1, kq = wid >> 1;
  const int g = lane >> 4, q = (lane >> 2) & 3, p4 = lane & 3;
  f32x4 a0 = f32x4{bflo(xres[0]), bfhi(xres[0]), bflo(xres[1]), bfhi(xres[1])}, a1 = f32x4{bflo(xres[2]), bfhi(xres[2]), bflo(xres[3]), bfhi(xres[3])};
  unsigned ra0, ra1;
  {
    const int lr = wid * 8 + (lane >> 3);
    const int sl = ((lane & 7) ^ ((lr & 7) | ((lr & 1) << 3))) * 16;
    ra0 = lds0 + L_WO + lr * 256 + sl; ra1 = lds0 + L_WO + lr * 256 + (sl ^ 128);
  }
  LAS unsigned* cand = (LAS unsigned*)(lds + L_SEL);
  {
    u32x4 of[8], wf0[8], wf1[8];
    const int flip = ((q >> 1) & 1) << 6;
    const LAS char* wb = lds + L_WO + kq * 16384 + (8 * h + q) * 128 + 32 * (g & 1) + 8 * p4;
#pragma unroll
    for (int e = 0; e < 8; ++e) { wf0[e] = ldstr2(wb + flip + e * 2048, 512); wf1[e] = ldstr2(wb + (64 ^ flip) + e * 2048, 512); }
    const int ooff = t * 32 + h * 16, osoff = WS_OF + pm * 65536 + kq * 16384 + mh * 1024;
#pragma unroll
    for (int e = 0; e < 8; ++e) of[e] = LD128(ooff, osoff + e * 2048);
    if (tid < 80) cand[tid] = 0;
    unsigned pa[8];
    {
      const unsigned rb = lds0 + L_WO + kq * 16384 + (mh * 32 + t) * 256;
      const int swz = (t & 7) | ((t & 1) << 3);
#pragma unroll
      for (int gq = 0; gq < 8; ++gq) { pa[gq] = rb + (((h * 8 + gq) ^ swz) * 16); asm volatile("" : "+v"(pa[gq])); }
    }
    asm volatile("" : "+v"(a0), "+v"(a1), "+v"(ra0), "+v"(ra1));
    LDS_BAR();
    f32x16 acc0 = f32x16{}, acc1 = f32x16{};
#pragma unroll 1
    for (unsigned spins = 0; spins < SPIN_CAP; ++spins) {
      bool miss[8], any = false;
#pragma unroll
      for (int e = 0; e < 8; ++e) { miss[e] = __any(umax4(of[e]) == SENT); any |= miss[e]; }
      if (!any) break;
      FENCE();
#pragma unroll
      for (int e = 0; e < 8; ++e) if (miss[e]) of[e] = LD128(ooff, osoff + e * 2048);
    }
#pragma unroll
    for (int e = 0; e < 8; ++e) { acc0 = mfma32(wf0[e], of[e], acc0); acc1 = mfma32(wf1[e], of[e], acc1); }
    STAMP(9);
#pragma unroll
    for (int gq = 0; gq < 4; ++gq) {
      *(LAS f32x4*)(uintptr_t)pa[gq] = f32x4{acc0[4 * gq], acc0[4 * gq + 1], acc0[4 * gq + 2], acc0[4 * gq + 3]};
      *(LAS f32x4*)(uintptr_t)pa[4 + gq] = f32x4{acc1[4 * gq], acc1[4 * gq + 1], acc1[4 * gq + 2], acc1[4 * gq + 3]};
    }
  }
  LDS_BAR();
  STAMP(10);
  float hv[8];
  {
    const LAS char* r0 = (const LAS char*)(uintptr_t)ra0;
    const LAS char* r1 = (const LAS char*)(uintptr_t)ra1;
    f32x4 t0[4], t1[4];
#pragma unroll
    for (int e = 0; e < 4; ++e) { t0[e] = *(const LAS f32x4*)(r0 + e * 16384); t1[e] = *(const LAS f32x4*)(r1 + e * 16384); }
    __builtin_amdgcn_sched_barrier(0);
    a0 += (t0[0] + t0[1]) + (t0[2] + t0[3]);
    a1 += (t1[0] + t1[1]) + (t1[2] + t1[3]);
    const u32x4 hb = u32x4{pkbf(a0[0], a0[1]), pkbf(a0[2], a0[3]), pkbf(a1[0], a1[1]), pkbf(a1[2], a1[3])};
    float s1 = 0.f, s2 = 0.f, sd = 0.f;
#pragma unroll
    for (int e = 0; e < 4; ++e) { s1 = dot2bf(hb[e], ONE2, s1); s2 = dot2bf(hb[e], hb[e], s2); sd = dot2bf(hb[e], wrv[e], sd); }
    s1 = sum8(s1); s2 = sum8(s2); sd = sum8(sd);
    if ((lane & 7) == 0) *(LAS u32x4*)(lds + L_ST + wid * 128 + lane * 2) = u32x4{__float_as_uint(s1), __float_as_uint(s2), __float_as_uint(sd), 0u};
    LDS_BAR();
#pragma unroll
    for (int e = 0; e < 4; ++e) { hv[2 * e] = bflo(hb[e]); hv[2 * e + 1] = bfhi(hb[e]); }
  }
  STAMP(11);
  const int trd = (8 * g + q) * 32 + 8 * p4;
  u32x4 fw[8];
  if (isgu || isdn) {
#pragma unroll
    for (int p = 0; p < 8; ++p) {
      const int pc = isgu ? (p >> 2) * 16 + (wid & 3) * 4 + (p & 3) : min(p * 8 + wid, 42);
      fw[p] = ldstr2(lds + L_F + pc * 1024 + trd, 128);
    }
  }
  u32x4 k4;
  BAR();
  {
    STAMP(12);
    const u32x2 st = *(const LAS u32x2*)(lds + L_ST + 1024 + wid * 64 + (lane >> 3) * 8);
    const float rstd = __uint_as_float(st[0]), mr = __uint_as_float(st[1]);
    u32x4 hp, op;
#pragma unroll
    for (int e = 0; e < 4; ++e) {
      hp[e] = pkbf(hv[2 * e] * rstd - mr, hv[2 * e + 1] * rstd - mr);
      op[e] = pkbf(hv[2 * e] + bflo(hp[e]), hv[2 * e + 1] + bfhi(hp[e]));
    }
    ST128(nosent4(hp), row * 1024 + col * 2, WS_HN);
    __builtin_amdgcn_raw_buffer_store_b128(nosent4(op), r_out, row * 1024 + col * 2, 0, SC1);
  }
  STAMP(13);
  if (!isgu && !isdn) { STAMP_FLUSH(); return; }
#pragma unroll 1
  for (unsigned spins = 0; spins < SPIN_CAP; ++spins) {
    FENCE();
    k4 = LD128(tid * 16, WS_KEY);
    if (__all(umax4(k4) != SENT)) break;
  }
  STAMP(14);
  {
    unsigned m = umax4(k4);
    m = max(m, dppu<0xB1>(m)); m = max(m, dppu<0x4E>(m)); m = max(m, dppu<0x141>(m)); m = max(m, dppu<0x140>(m));
    m = max(m, dppu<0x142>(m)); m = max(m, dppu<0x143>(m));
    if (lane == 63) cand[64 + wid] = m;
  }
  LDS_BAR();
  {
    const u32x4 g0 = *(LAS u32x4*)(cand + 64), g1 = *(LAS u32x4*)(cand + 68);
    const unsigned T = min(min(min(g0[0], g0[1]), min(g0[2], g0[3])), min(min(g1[0], g1[1]), min(g1[2], g1[3])));
    const unsigned c0 = k4[0] >= T, c1 = k4[1] >= T, c2 = k4[2] >= T, c3 = k4[3] >= T;
    const unsigned cnt = (c0 + c1) + (c2 + c3);
    if (cnt) {
      unsigned pos;
      asm volatile("ds_add_rtn_u32 %0, %1, %2\n\ts_waitcnt lgkmcnt(0)" : "=v"(pos) : "v"((unsigned)(lds0 + L_SEL + 72 * 4)), "v"(cnt) : "memory");
      cand[c0 && pos < 64 ? pos : 73] = k4[0]; pos += c0;
      cand[c1 && pos < 64 ? pos : 73] = k4[1]; pos += c1;
      cand[c2 && pos < 64 ? pos : 73] = k4[2]; pos += c2;
      cand[c3 && pos < 64 ? pos : 73] = k4[3];
    }
  }
  LDS_BAR();
  LAS unsigned* sel = cand + 80 + wid * 8;
  {
    unsigned nv = cand[72];
    unsigned c = cand[t];
    u32x4 o4[4];
#pragma unroll
    for (int e = 0; e < 4; ++e) o4[e] = *(LAS u32x4*)(cand + h * 16 + e * 4);
    asm volatile("" : "+v"(nv), "+v"(c), "+v"(o4[0]), "+v"(o4[1]), "+v"(o4[2]), "+v"(o4[3]));
    const unsigned n = __builtin_amdgcn_readfirstlane(nv);
    if (n <= 64 && wid >= (isgu ? 4 : 1)) {
      if (isgu) { STAMP_FLUSH(); return; }
    } else if (__builtin_expect(n <= 32, 1)) {
      unsigned ba = 0, bb = 0;
#pragma unroll
      for (int e = 0; e < 4; ++e) {
        ba = __builtin_amdgcn_alignbit(ba, c - o4[e][0], 31); bb = __builtin_amdgcn_alignbit(bb, c - o4[e][1], 31);
        ba = __builtin_amdgcn_alignbit(ba, c - o4[e][2], 31); bb = __builtin_amdgcn_alignbit(bb, c - o4[e][3], 31);
      }
      unsigned rk = __builtin_popcount(ba) + __builtin_popcount(bb);
      auto r2 = __builtin_amdgcn_permlane32_swap(rk, rk, false, false);
      rk = r2[0] + r2[1];
      if (rk < 8) sel[rk] = c;
    } else if (n <= 64) {
      const unsigned c2 = cand[lane];
      unsigned rk = 0;
#pragma unroll 1
      for (int j = 0; j < 64; j += 4) { const u32x4 v = *(LAS u32x4*)(cand + j); rk += (v[0] > c2) + (v[1] > c2) + (v[2] > c2) + (v[3] > c2); }
      if (rk < 8) sel[rk] = c2;
    } else {
      LAS unsigned* all = cand + 256;
      *(LAS u32x4*)(all + tid * 4) = k4;
      LDS_BAR();
      unsigned rk[4] = {0, 0, 0, 0};
#pragma unroll 1
      for (int j = 0; j < 2048; ++j) {
        const unsigned v = all[j];
#pragma unroll
        for (int e = 0; e < 4; ++e) rk[e] += v > k4[e];
      }
#pragma unroll
      for (int e = 0; e < 4; ++e) if (rk[e] < 8) cand[160 + rk[e]] = k4[e];
      LDS_BAR();
      sel = cand + 160;
      if (isgu && wid >= 4) { STAMP_FLUSH(); return; }
    }
  }
  const int tok = lane & 15;
  const unsigned key = sel[tok & 7];
  const int trow = 2047 - (int)(key & 2047u);
  STAMP(15);
  LAS f32x4* red = (LAS f32x4*)(lds + L_WO);
  if (isgu) {
    u32x4 xb[4];
#pragma unroll 1
    for (unsigned spins = 0; spins < SPIN_CAP; ++spins) {
      FENCE();
#pragma unroll
      for (int u = 0; u < 4; ++u) xb[u] = LD128(trow * 1024 + u * 64 + g * 16, WS_HN + wid * 256);
      if (__all(max(max(umax4(xb[0]), umax4(xb[1])), max(umax4(xb[2]), umax4(xb[3]))) != SENT)) break;
    }
    STAMP(16);
    f32x4 ag = f32x4{}, au = f32x4{};
#pragma unroll
    for (int u = 0; u < 4; ++u) { ag = mfma16(fw[u], xb[u], ag); au = mfma16(fw[4 + u], xb[u], au); }
    const f32x4 mine = tok < 8 ? ag : au;
    if (wid != 0) red[wid * 64 + lane] = mine;
    LDS_BAR();
    if (wid == 0) {
      const f32x4 s = (mine + red[64 + lane]) + (red[128 + lane] + red[192 + lane]);
      float av[4];
#pragma unroll
      for (int r = 0; r < 4; ++r) {
        const float gg = rbf(s[r]);
        const float uu = dppf<0x128>(gg);
        const float sl = rbf(gg * __builtin_amdgcn_rcpf(1.f + __expf(-gg)));
        av[r] = sl * uu;
      }
      if (tok < 8) ST64((u32x2{nosent(pkbf(av[0], av[1])), nosent(pkbf(av[2], av[3]))}), tok * 32 + g * 8, WS_ACT + b * 256);
    }
    STAMP(17);
    STAMP_FLUSH();
    return;
  }
  const int nb = b - DN0;
  u32x2 o0 = u32x2{0, 0};
  float sg = 0.f;
  if (wid == 0) {
#pragma unroll 1
    for (unsigned spins = 0; spins < SPIN_CAP; ++spins) {
      FENCE();
      o0 = __builtin_amdgcn_raw_buffer_load_b64(r_out, trow * 1024 + g * 8, nb * 32, SC1);
      if (__all(max(o0[0], o0[1]) != SENT)) break;
    }
    const unsigned ov = ((key & 0x7FFFF800u) << 1) | 0x800u;
    const float r = rbf(__uint_as_float((ov & 0x80000000u) ? (ov & 0x7FFFFFFFu) : ~ov));
    sg = rbf(__builtin_amdgcn_rcpf(1.f + __expf(-r)));
  }
  const __amdgpu_buffer_rsrc_t r_act = mkrsrc(ws + WS_ACT, 22016);
  u32x4 ab[6];
  const int aoff = wid * 512 + (g >> 1) * 256 + (tok & 7) * 32 + (g & 1) * 16;
#pragma unroll
  for (int p = 0; p < 6; ++p) ab[p] = __builtin_amdgcn_raw_buffer_load_b128(r_act, aoff, p * 4096, SC1);
#pragma unroll 1
  for (unsigned spins = 0; spins < SPIN_CAP; ++spins) {
    bool miss[6], any = false;
#pragma unroll
    for (int p = 0; p < 6; ++p) { miss[p] = __any(umax4(ab[p]) == SENT); any |= miss[p]; }
    if (!any) break;
    FENCE();
#pragma unroll
    for (int p = 0; p < 6; ++p) if (miss[p]) ab[p] = __builtin_amdgcn_raw_buffer_load_b128(r_act, aoff, p * 4096, SC1);
  }
  STAMP(16);
  f32x4 ay = f32x4{};
#pragma unroll
  for (int p = 0; p < 6; ++p) ay = mfma16(fw[p], ab[p], ay);
  red[wid * 64 + lane] = ay;
  LDS_BAR();
  if (wid == 0 && tok < 8) {
    ay = ((ay + red[64 + lane]) + (red[128 + lane] + red[192 + lane])) + ((red[256 + lane] + red[320 + lane]) + (red[384 + lane] + red[448 + lane]));
    const float y0 = rbf(rbf(ay[0]) * sg), y1 = rbf(rbf(ay[1]) * sg), y2 = rbf(rbf(ay[2]) * sg), y3 = rbf(rbf(ay[3]) * sg);
    __builtin_amdgcn_raw_buffer_store_b64(u32x2{pkbf(bflo(o0[0]) + y0, bfhi(o0[0]) + y1), pkbf(bflo(o0[1]) + y2, bfhi(o0[1]) + y3)}, r_out,
                                          trow * 1024 + g * 8, nb * 32, SC1);
  }
  STAMP(17);
  if (DIAG > 0 && wid == 0 && b == DN0) {
    const unsigned long long te = __builtin_amdgcn_s_memrealtime() + 4ull * min(tk - t00, 20000ull);
    while (__builtin_amdgcn_s_memrealtime() < te) __builtin_amdgcn_s_sleep(1);
  }
}

extern "C" void kernel_launch(void* const* d_in, const int* in_sizes, int n_in, void* d_out, int out_size, void* d_ws, size_t ws_size,
                              hipStream_t stream) {
  (void)hipFuncSetAttribute((const void*)fused_block, hipFuncAttributeMaxDynamicSharedMemorySize, LDS_BYTES);
  fused_block<<<256, 576, LDS_BYTES, stream>>>((const u16*)d_in[0], (const u16*)d_in[1], (const u16*)d_in[2], (const u16*)d_in[4],
                                               (const u16*)d_in[5], (const u16*)d_in[6], (const u16*)d_in[7], (char*)d_ws, (u16*)d_out);
}
```

```cpp
#include <hip/hip_runtime.h>
#include <hip/hip_bf16.h>
#define STAMP_DECL
#define STAMP(s)
#define STAMP_FLUSH()
#define STAMP_ARG
#define STAMP_PASS

#define PROG 0
#define CODEPF 1
#define CODE_KB 25
#define LATEO 4
typedef unsigned short u16;
typedef __bf16 bf16x2_t __attribute__((ext_vector_type(2)));
typedef __bf16 bf16x8_t __attribute__((ext_vector_type(8)));
typedef __bf16 bf16x16_t __attribute__((ext_vector_type(16)));
typedef short s16x4 __attribute__((ext_vector_type(4)));
typedef float f32x4 __attribute__((ext_vector_type(4)));
typedef float f32x8 __attribute__((ext_vector_type(8)));
typedef float f32x16 __attribute__((ext_vector_type(16)));
typedef unsigned u32x2 __attribute__((ext_vector_type(2)));
typedef unsigned u32x4 __attribute__((ext_vector_type(4)));
#define LAS __attribute__((address_space(3)))

constexpr int D_ = 512, F_ = 1376;
constexpr unsigned SENT = 0xFFFFFFFFu;
constexpr float LN_EPS = 1e-5f;
constexpr float QSCALE = 0.25f * 1.4426950408889634f;
constexpr unsigned SPIN_CAP = 1u << 16;
constexpr int SC1 = 16;
constexpr unsigned ONE2 = 0x3F803F80u;

constexpr unsigned WS_KV = 0;
constexpr unsigned WS_QF = 4u << 20;
constexpr unsigned WS_OF = 6u << 20;
constexpr unsigned WS_HN = 8u << 20;
constexpr unsigned WS_XS = 10u << 20;
constexpr unsigned WS_ACT = WS_XS + (256u << 10);
constexpr unsigned WS_KEY = WS_ACT + 22016;
constexpr unsigned WS_FLAG = WS_XS + (320u << 10);
constexpr unsigned WS_FLAG2 = WS_FLAG + 32768;
constexpr unsigned WS_EPOCH = WS_FLAG2 + 32768;
constexpr unsigned WS_BYTES = WS_EPOCH + 128;

__device__ __forceinline__ f32x16 mfma32(u32x4 a, u32x4 b, f32x16 c) {
  return __builtin_amdgcn_mfma_f32_32x32x16_bf16(__builtin_bit_cast(bf16x8_t, a), __builtin_bit_cast(bf16x8_t, b), c, 0, 0, 0);
}
__device__ __forceinline__ f32x4 mfma16(u32x4 a, u32x4 b, f32x4 c) {
  return __builtin_amdgcn_mfma_f32_16x16x32_bf16(__builtin_bit_cast(bf16x8_t, a), __builtin_bit_cast(bf16x8_t, b), c, 0, 0, 0);
}
__device__ __forceinline__ f32x16 smfmac32(u32x4 a, f32x8 b, f32x16 c, int idx) {
  return __builtin_amdgcn_smfmac_f32_32x32x32_bf16(__builtin_bit_cast(bf16x8_t, a), __builtin_bit_cast(bf16x16_t, b), c, idx, 0, 0);
}
__device__ __forceinline__ float dot2bf(unsigned a, unsigned b, float c) {
  return __builtin_amdgcn_fdot2_f32_bf16(__builtin_bit_cast(bf16x2_t, a), __builtin_bit_cast(bf16x2_t, b), c, false);
}
__device__ __forceinline__ float swap32_sum(float v) {
  auto r = __builtin_amdgcn_permlane32_swap(__float_as_uint(v), __float_as_uint(v), false, false);
  return __uint_as_float(r[0]) + __uint_as_float(r[1]);
}
__device__ __forceinline__ float swap32_max(float v) {
  auto r = __builtin_amdgcn_permlane32_swap(__float_as_uint(v), __float_as_uint(v), false, false);
  return __builtin_fmaxf(__uint_as_float(r[0]), __uint_as_float(r[1]));
}
__device__ __forceinline__ unsigned pkbf(float lo, float hi) {
  bf16x2_t v; v[0] = (__bf16)lo; v[1] = (__bf16)hi; return __builtin_bit_cast(unsigned, v);
}
__device__ __forceinline__ float bflo(unsigned u) { return __uint_as_float(u << 16); }
__device__ __forceinline__ float bfhi(unsigned u) { return __uint_as_float(u & 0xFFFF0000u); }
__device__ __forceinline__ float rbf(float f) { return (float)(__bf16)f; }
__device__ __forceinline__ void glds16(const void* sbase, unsigned voff, unsigned lds_base) {
  unsigned sv;
  asm volatile("s_mov_b32 %0, m0\n\ts_mov_b32 m0, %3\n\ts_nop 0\n\tglobal_load_lds_dwordx4 %1, %2\n\ts_mov_b32 m0, %0"
               : "=&s"(sv) : "v"(voff), "s"(sbase), "s"(lds_base) : "memory");
}
__device__ __forceinline__ void glds16c(const void* sbase, unsigned voff, unsigned lds_base) {
  unsigned sv;
  asm volatile("s_mov_b32 %0, m0\n\ts_mov_b32 m0, %3\n\ts_nop 0\n\tglobal_load_lds_dwordx4 %1, %2 sc1\n\ts_mov_b32 m0, %0"
               : "=&s"(sv) : "v"(voff), "s"(sbase), "s"(lds_base) : "memory");
}
__device__ __forceinline__ void glds8c(const void* sbase, unsigned voff, unsigned lds_base) {
  unsigned sv;
  asm volatile("s_mov_b32 %0, m0\n\ts_mov_b32 m0, %3\n\ts_nop 0\n\t"
               "global_load_lds_dwordx4 %1, %2 offset:-4096 sc1\n\tglobal_load_lds_dwordx4 %1, %2 offset:-3072 sc1\n\t"
               "global_load_lds_dwordx4 %1, %2 offset:-2048 sc1\n\tglobal_load_lds_dwordx4 %1, %2 offset:-1024 sc1\n\t"
               "global_load_lds_dwordx4 %1, %2 sc1\n\tglobal_load_lds_dwordx4 %1, %2 offset:1024 sc1\n\t"
               "global_load_lds_dwordx4 %1, %2 offset:2048 sc1\n\tglobal_load_lds_dwordx4 %1, %2 offset:3072 sc1\n\t"
               "s_mov_b32 m0, %0"
               : "=&s"(sv) : "v"(voff), "s"((const char*)sbase + 4096), "s"(lds_base + 4096) : "memory");
}
__device__ __forceinline__ void glds4(const void* sbase, const unsigned (&voff)[4], unsigned lds_base) {
  unsigned sv;
  asm volatile("s_mov_b32 %0, m0\n\ts_mov_b32 m0, %6\n\ts_nop 0\n\t"
               "global_load_lds_dwordx4 %1, %5\n\tglobal_load_lds_dwordx4 %2, %5 offset:1024\n\t"
               "global_load_lds_dwordx4 %3, %5 offset:2048\n\tglobal_load_lds_dwordx4 %4, %5 offset:3072\n\t"
               "s_mov_b32 m0, %0"
               : "=&s"(sv) : "v"(voff[0]), "v"(voff[1]), "v"(voff[2]), "v"(voff[3]), "s"(sbase), "s"(lds_base) : "memory");
}
#define FENCE() asm volatile("" ::: "memory")
#define WAIT_BAR(N) asm volatile("s_waitcnt vmcnt(" #N ") lgkmcnt(0)\n\ts_barrier" ::: "memory")
#define VM_BAR(N) asm volatile("s_waitcnt vmcnt(" #N ")\n\ts_barrier" ::: "memory")
#define LDS_BAR() do { FENCE(); __builtin_amdgcn_s_waitcnt(0xC07F); __builtin_amdgcn_s_barrier(); FENCE(); } while (0)
#define BAR() do { FENCE(); __builtin_amdgcn_s_barrier(); FENCE(); } while (0)
#define WAIT_VM(N) asm volatile("s_waitcnt vmcnt(" #N ")" ::: "memory")
#define LD128(off, soff) __builtin_amdgcn_raw_buffer_load_b128(r_ws, off, soff, SC1)
#define LD64(off, soff) __builtin_amdgcn_raw_buffer_load_b64(r_ws, off, soff, SC1)
#define ST128(v, off, soff) __builtin_amdgcn_raw_buffer_store_b128(v, r_ws, (off) + (soff), 0, SC1)
#define ST64(v, off, soff) __builtin_amdgcn_raw_buffer_store_b64(v, r_ws, off, soff, SC1)
__device__ __forceinline__ u32x4 ldsr128(const LAS char* p) { return *(const LAS u32x4*)p; }
__device__ __forceinline__ u32x2 ldstr(const LAS char* p) {
  return __builtin_bit_cast(u32x2, __builtin_amdgcn_ds_read_tr16_b64_v4i16((LAS s16x4*)p));
}
__device__ __forceinline__ u32x4 ldstr2(const LAS char* p, int off2) { const u32x2 a = ldstr(p), b = ldstr(p + off2); return u32x4{a[0], a[1], b[0], b[1]}; }
__device__ __forceinline__ int swapb23(int v) { return (v & ~12) | ((v & 4) << 1) | ((v & 8) >> 1); }
__device__ __forceinline__ unsigned nosent(unsigned v) { return min(v, 0xFFFFFFFEu); }
__device__ __forceinline__ u32x4 nosent4(u32x4 v) { return u32x4{nosent(v[0]), nosent(v[1]), nosent(v[2]), nosent(v[3])}; }
__device__ __forceinline__ unsigned umax4(u32x4 v) { return max(max(v[0], v[1]), max(v[2], v[3])); }
__device__ __forceinline__ __amdgpu_buffer_rsrc_t mkrsrc(const void* p, unsigned bytes) {
  return __builtin_amdgcn_make_buffer_rsrc((void*)p, 0, bytes, 0x00020000);
}
template <int CTRL> __device__ __forceinline__ float dppf(float v) {
  return __uint_as_float(__builtin_amdgcn_update_dpp(0, __float_as_uint(v), CTRL, 0xF, 0xF, true));
}
template <int CTRL> __device__ __forceinline__ unsigned dppu(unsigned v) { return __builtin_amdgcn_update_dpp(0, v, CTRL, 0xF, 0xF, true); }
__device__ __forceinline__ unsigned rdl(unsigned v, int l) { return (unsigned)__builtin_amdgcn_readlane((int)v, l); }
__device__ __forceinline__ float rdlf(float v, int l) { return __uint_as_float(rdl(__float_as_uint(v), l)); }
__device__ __forceinline__ float sum8(float v) { v += dppf<0xB1>(v); v += dppf<0x4E>(v); v += dppf<0x141>(v); return v; }
__device__ __forceinline__ u32x4 pack8(const float* v) { return u32x4{pkbf(v[0], v[1]), pkbf(v[2], v[3]), pkbf(v[4], v[5]), pkbf(v[6], v[7])}; }

constexpr int K1_SLOT = 16384 + 12288, K1_CS = 4 * K1_SLOT, K1_CSP = K1_CS + 512, K1_TAG = K1_CSP + 8 * 384;
constexpr int L_Q = 0, L_KV = 24576, L_ONE = L_KV + 131072, LDS_BYTES = 163840;
constexpr int L_F = 0, L_F40 = L_ONE + 1024, L_WO = 40960, L_PA = L_WO + 65536, L_SEL = L_PA + 32768, L_ST = L_SEL + 12288, L_WF = L_ONE + 64;
constexpr int NGU = 86, DN0 = 128, NDN = 32;

template <int NV>
__device__ __forceinline__ void qkv_body(const __amdgpu_buffer_rsrc_t r_ws, LAS char* lds, int rb, int cb, int wid STAMP_ARG) {
  const int lane = threadIdx.x & 63, t = lane & 31, h = lane >> 5;
  int xr[4];
#pragma unroll
  for (int u = 0; u < 4; ++u) xr[u] = wid * 4096 + t * 128 + (((2 * u + h) ^ ((t >> 1) & 7)) * 16);
  const int wrd = 16384 + (8 * h + ((lane >> 2) & 3)) * 192 + 32 * ((lane >> 4) & 1) + 8 * (lane & 3);
  f32x16 acc[3];
#pragma unroll
  for (int j = 0; j < 3; ++j) acc[j] = f32x16{};
  float s1a = 0.f, s1b = 0.f, s2a = 0.f, s2b = 0.f;
  u32x4 xf = u32x4{0, 0, 0, 0}, wf[3];
#pragma unroll
  for (int j = 0; j < 3; ++j) wf[j] = u32x4{0, 0, 0, 0};
  auto mm = [&]() {
#pragma unroll
    for (int j = 0; j < 3; ++j) {
      if (j >= 3 - NV) acc[j] = mfma32(xf, wf[j], acc[j]);
      else acc[j] = mfma32(wf[j], xf, acc[j]);
    }
  };
  const int tile = rb * 4 + wid;
  const int voffV = tile * 2048 + 1024 + (t >> 4) * 131072 + h * 256 + swapb23(t & 15) * 16;
  const int voffK = tile * 2048 + swapb23(t) * 32 + h * 16;
  const int voffQ = tile * 1024 + t * 32 + h * 16;
#pragma unroll 1
  for (int c = 0; c < 8; ++c) {
    LDS_BAR();
    if (c == 0) STAMP(1);
    const LAS char* sb = lds + (c & 3) * K1_SLOT;
    u32x4 xn[4];
#pragma unroll
    for (int u = 0; u < 4; ++u) {
      xn[u] = ldsr128(sb + xr[u]);
      u32x4 wn[3];
#pragma unroll
      for (int j = 0; j < 3; ++j) wn[j] = ldstr2(sb + wrd + u * 3072 + j * 64, 768);
      mm();
#pragma unroll
      for (int z = 0; z < 3; ++z) { __builtin_amdgcn_sched_group_barrier(0x8, 1, 0); __builtin_amdgcn_sched_group_barrier(0x102, 3, 0); }
      __builtin_amdgcn_sched_barrier(0);
      xf = xn[u];
#pragma unroll
      for (int j = 0; j < 3; ++j) wf[j] = wn[j];
    }
#pragma unroll
    for (int u = 0; u < 4; ++u)
#pragma unroll
      for (int e = 0; e < 4; e += 2) {
        s1a = dot2bf(xn[u][e], ONE2, s1a); s2a = dot2bf(xn[u][e], xn[u][e], s2a);
        s1b = dot2bf(xn[u][e + 1], ONE2, s1b); s2b = dot2bf(xn[u][e + 1], xn[u][e + 1], s2b);
      }
    asm volatile("" : "+v"(s1a), "+v"(s1b), "+v"(s2a), "+v"(s2b));
    __builtin_amdgcn_sched_barrier(0);
  }
  __builtin_amdgcn_s_barrier();
  mm();
  STAMP(2);
  LAS float* scr = (LAS float*)(lds + wid * 4096);
  const LAS float* csum = (const LAS float*)(lds + K1_CS);
  const float s2 = swap32_sum(s2a + s2b);
  const float mean = swap32_sum(s1a + s1b) * (1.f / D_);
  const float var = __builtin_fmaxf(s2 * (1.f / D_) - mean * mean, 0.f);
  const float rstd = __builtin_amdgcn_rsqf(var + LN_EPS);
  if (NV > 0 && h == 0) { scr[t] = rstd; scr[32 + t] = mean * rstd; }
  BAR();
#pragma unroll
  for (int j = 0; j < 3; ++j) {
    const int gt = cb * 3 + j;
    float v[16];
    if (j >= 3 - NV) {
      const float cs = csum[32 * j + t];
#pragma unroll
      for (int gq = 0; gq < 4; ++gq) {
        const f32x4 a = *(const LAS f32x4*)(scr + 8 * gq + 4 * h), b = *(const LAS f32x4*)(scr + 32 + 8 * gq + 4 * h);
#pragma unroll
        for (int e = 0; e < 4; ++e) v[4 * gq + e] = acc[j][4 * gq + e] * a[e] - b[e] * cs;
      }
      const int so = WS_KV + (gt - 32) * 262144;
      ST128(pack8(v), voffV, so); ST128(pack8(v + 8), voffV + 512, so);
    } else {
      const bool isq = gt < 16;
      const float sc = isq ? QSCALE : 1.f;
      const float a = rstd * sc, b = mean * rstd * sc;
#pragma unroll
      for (int gq = 0; gq < 4; ++gq) {
        const f32x4 c4 = *(const LAS f32x4*)(csum + 32 * j + 8 * gq + 4 * h);
#pragma unroll
        for (int e = 0; e < 4; ++e) v[4 * gq + e] = acc[j][4 * gq + e] * a - b * c4[e];
      }
      const int vo = isq ? voffQ : voffK;
      const int so = isq ? WS_QF + gt * 131072 : WS_KV + (gt - 16) * 262144;
      ST128(pack8(v), vo, so); ST128(pack8(v + 8), vo, so + (isq ? 65536 : 131072));
    }
  }
  STAMP(3);
}

__device__ __forceinline__ float rowmax16(const f32x16& s) {
  float a = __builtin_fmaxf(__builtin_fmaxf(s[0], s[1]), s[2]), b = __builtin_fmaxf(__builtin_fmaxf(s[3], s[4]), s[5]);
  a = __builtin_fmaxf(__builtin_fmaxf(a, s[6]), s[7]); b = __builtin_fmaxf(__builtin_fmaxf(b, s[8]), s[9]);
  a = __builtin_fmaxf(__builtin_fmaxf(a, s[10]), s[11]); b = __builtin_fmaxf(__builtin_fmaxf(b, s[12]), s[13]);
  a = __builtin_fmaxf(__builtin_fmaxf(a, s[14]), s[15]);
  return swap32_max(__builtin_fmaxf(a, b));
}
__device__ __forceinline__ f32x8 lo8(const f32x16& s) { return __builtin_shufflevector(s, s, 0, 1, 2, 3, 4, 5, 6, 7); }
__device__ __forceinline__ f32x8 hi8(const f32x16& s) { return __builtin_shufflevector(s, s, 8, 9, 10, 11, 12, 13, 14, 15); }

#define E1(X, i) "v_exp_f32 v[" #X "+" #i "], v[" #X "+" #i "]\n\t"
#define SMF(A, B, off) "v_smfmac_f32_32x32x32_bf16 v[0:15], v[" #A ":" #A "+3], v[" #B "+" #off ":" #B "+" #off "+7], v88\n\t"
#define QK(Y) "v_mfma_f32_32x32x16_bf16 v[" #Y ":" #Y "+15], v[68:71], v[64:67], v[16:31]\n\t"
#define RDV(D, off) "ds_read_b128 v[" #D ":" #D "+3], v90 offset:" #off "\n\t"
#define RDK(off) "ds_read_b128 v[68:71], v89 offset:" #off "\n\t"
#define STEP(X, Y, VA, VPB, VA2, VB2, KOFF, VOFF, VOFF2, PA, PB, EXTRA) \
  PA E1(X, 0) E1(X, 1) SMF(VPB, Y, 8) RDV(VA2, VOFF) \
  PB E1(X, 2) E1(X, 3) E1(X, 4) E1(X, 5) \
  "s_waitcnt lgkmcnt(1)\n\t" QK(Y) RDV(VB2, VOFF2) RDK(KOFF) \
  E1(X, 6) E1(X, 7) E1(X, 8) E1(X, 9) \
  SMF(VA, X, 0) EXTRA \
  E1(X, 10) E1(X, 11) E1(X, 12) E1(X, 13) E1(X, 14) E1(X, 15)
#define ATTN_LOOP(L1, L2, P0, P2, P3) \
        L1 ":\n\t" \
        "s_bitcmp1_b32 0x4424, %[cnt]\n\t" \
        "s_cbranch_scc0 " L2 "f\n\t" \
        "s_barrier\n\t" \
        L2 ":\n\t" \
        STEP(32, 48, 72, 84, 80, 84, 4096, 2048, 2560, P0, "", "") \
        STEP(48, 32, 80, 76, 72, 76, 6144, 4096, 4608, "", "", "") \
        STEP(32, 48, 72, 84, 80, 84, 8192, 6144, 6656, P2, "", "v_add_u32 v90, v90, v91\n\t") \
        STEP(48, 32, 80, 76, 72, 76, 10240, 0, 512, "", P3, "v_add_u32 v89, 0x2000, v89\n\ts_add_u32 %[cnt], %[cnt], 1\n\ts_cmp_lg_u32 %[cnt], 16\n\t") \
        "s_cbranch_scc1 " L1 "b\n\t"

__global__ __launch_bounds__(576, 3) void fused_block(const u16* __restrict__ x, const u16* __restrict__ wqkv, const u16* __restrict__ wo,
                                                      const u16* __restrict__ wr, const u16* __restrict__ wg, const u16* __restrict__ wu,
                                                      const u16* __restrict__ wd, char* __restrict__ ws, u16* __restrict__ out) {
  extern __shared__ __attribute__((aligned(16))) char smem[];
  LAS char* lds = (LAS char*)smem;
  const unsigned lds0 = (unsigned)(uintptr_t)lds;
  {
    unsigned long long pc; unsigned s0, s1, v0;
    asm volatile("v_readfirstlane_b32 %1, %4\n\ts_lshr_b32 %2, %5, 3\n\ts_getpc_b64 %0\n\ts_and_b64 %0, %0, -16\n\t"
                 "s_cmp_lt_u32 %1, 64\n\ts_cbranch_scc0 9f\n\t"
                 "s_cmp_lt_u32 %2, %6\n\ts_cbranch_scc0 9f\n\t"
                 "s_lshl_b32 %2, %2, 10\n\tv_lshlrev_b32 %3, 4, %4\n\tv_add_u32 %3, %2, %3\n\t"
                 "s_mov_b32 %1, m0\n\ts_mov_b32 m0, %7\n\ts_nop 0\n\t"
                 "global_load_lds_dwordx4 %3, %0\n\ts_mov_b32 m0, %1\n\t9:"
                 : "=&s"(pc), "=&s"(s0), "=&s"(s1), "=&v"(v0) : "v"(threadIdx.x), "s"(blockIdx.x), "i"(CODE_KB), "s"(lds0 + 131072) : "scc", "memory");
  }
  const int tid = threadIdx.x, lane = tid & 63, t = lane & 31, h = lane >> 5;
  const int wid = __builtin_amdgcn_readfirstlane(tid >> 6);
  const int b = blockIdx.x;
  const int rb1 = 4 * (b & 3) + ((b >> 3) & 3), cb1 = 8 * ((b >> 2) & 1) + (b >> 5);
  const int head = (b & 7) * 4 + ((b >> 3) & 3), qb = b >> 5;
  const int pm = head, cb = qb;
  const unsigned l16 = lane * 16;
  const int lw = wid - 4;
  const int rot = ((((b >> 5) + ((b >> 3) & 3)) & 1)) * 4;
  unsigned xoff[2], woff[3];
  auto stage = [&](int c) {
    const int kc = (c + rot) & 7;
    const char* xs = (const char*)x + kc * 128;
    const char* wsrc = (const char*)wqkv + kc * (64 * 3072);
    const unsigned base = lds0 + (c & 3) * K1_SLOT;
#pragma unroll
    for (int p = 0; p < 4; ++p) glds16(xs + (p >> 1) * 16384, xoff[p & 1], base + lw * 4096 + p * 1024);
#pragma unroll
    for (int p = 0; p < 3; ++p) glds16(wsrc, woff[p], base + 16384 + (lw * 3 + p) * 1024);
  };
  if (lw >= 0 && lw < 4) {
#pragma unroll
    for (int p = 0; p < 2; ++p) {
      const int row = p * 8 + (lane >> 3), slot = lane & 7;
      xoff[p] = (unsigned)((rb1 * 128 + lw * 32 + row) * 1024 + ((slot ^ ((row >> 1) & 7)) * 16));
    }
#pragma unroll
    for (int p = 0; p < 3; ++p) {
      const int idx = (lw * 3 + p) * 64 + lane, k = idx / 12, c = idx - k * 12;
      woff[p] = (unsigned)(k * 3072 + cb1 * 192 + c * 16);
    }
    stage(0); stage(1);
  }
  FENCE();
  const bool isgu = b < NGU, isdn = b >= DN0 && b < DN0 + NDN;
  const __amdgpu_buffer_rsrc_t r_ws = mkrsrc(ws, WS_BYTES), r_out = mkrsrc(out, 2u << 20);
  STAMP_DECL STAMP(0);

  if (wid == 8) {
    __builtin_amdgcn_s_setprio(3);
    const u32x2 ep = LD64(0, WS_EPOCH);
    const u32x4 w4 = *(const u32x4*)(wr + lane * 8);
    if (lane < 16) { const unsigned v = (lane & 1) ? 0u : ONE2; *(LAS u32x4*)(lds + L_ONE + (lane >> 2) * 2048 + ((lane >> 1) & 1) * 512 + (lane & 1) * 16) = u32x4{v, v, v, v}; }
    if (lane == 16) *(LAS u32x4*)(lds + L_WF) = u32x4{0, 0, 0, 0};
#pragma unroll 1
    for (int c = 0; c < 5; ++c) BAR();
    const unsigned long long e64 = (((unsigned long long)ep[1] << 32) | ep[0]) + 1ull;
    const u32x2 tag = u32x2{(unsigned)e64, (unsigned)(e64 >> 32)};
    float sw;
    sw = dot2bf(w4[0], ONE2, dot2bf(w4[1], ONE2, 0.f)) + dot2bf(w4[2], ONE2, dot2bf(w4[3], ONE2, 0.f));
    sw = sum8(sw); sw += dppf<0x140>(sw);
    sw = (rdlf(sw, 0) + rdlf(sw, 16)) + (rdlf(sw, 32) + rdlf(sw, 48));
    const int hp = head >> 1;
    const int fidx = lane < 16 ? lane * 16 + (16 + hp) / 3 : lane < 32 ? (lane - 16) * 16 + (32 + hp) / 3 : (qb * 2 + (lane & 1)) * 16 + hp / 3;
    const char* kvsrc = ws + WS_KV + (size_t)head * 131072;
    LDS_BAR();
#pragma unroll 1
    for (int c = 6; c < 10; ++c) BAR();
    BAR();
    if (lane == 0) ST64(tag, 0, WS_FLAG + (rb1 * 16 + cb1) * 128);
    STAMP(1);
    unsigned long long seen = 0;
    auto wait_flags = [&](unsigned long long need) {
#pragma unroll 1
      for (unsigned spins = 0; spins < SPIN_CAP && (seen & need) != need; ++spins) {
        FENCE();
        const u32x2 f = LD64(fidx * 128, WS_FLAG);
        seen = __ballot((f[0] == tag[0]) & (f[1] == tag[1]));
      }
      FENCE();
    };
    wait_flags(0x300070007ull);
    STAMP(5);
    glds8c(ws + WS_QF + head * 65536 + qb * 8192, l16, lds0 + L_Q);
#pragma unroll
    for (int p = 0; p < 3; ++p) glds8c(kvsrc + p * 8192, l16, lds0 + L_KV + p * 8192);
    VM_BAR(0);
    wait_flags(0xFFFFFFFFull);
#pragma unroll 1
    for (int p = 3; p < 10; ++p) glds8c(kvsrc + p * 8192, l16, lds0 + L_KV + p * 8192);
    VM_BAR(32);
    WAIT_VM(8);
#pragma unroll 1
    for (int p = 10; p < 16; ++p) glds8c(kvsrc + p * 8192, l16, lds0 + L_KV + p * 8192);
    WAIT_VM(0);
    {
      const u32x4 s4 = u32x4{SENT, SENT, SENT, SENT};
#pragma unroll
      for (int e = 0; e < 8; ++e) ST128(s4, l16, WS_OF + (qb * 4 + (e >> 1)) * 65536 + head * 2048 + (e & 1) * 1024);
#pragma unroll
      for (int e = 0; e < 8; ++e) {
        const int off = (pm * 64 + e * 8 + (lane >> 3)) * 1024 + cb * 128 + (lane & 7) * 16;
        ST128(s4, off, WS_HN);
        __builtin_amdgcn_raw_buffer_store_b128(s4, r_out, off, 0, SC1);
      }
      ST128(s4, l16, WS_XS + (pm * 8 + cb) * 1024);
      if (lane < 8 && b * 8 + lane < (22016 + 8192) / 16) ST128(s4, l16, WS_ACT + b * 128);
    }
    BAR();
    WAIT_VM(0);
    if (lane == 0) ST64(tag, 0, WS_FLAG2 + b * 128);
    unsigned foff[4];
    const unsigned fstr = isgu ? 32 * F_ * 2 : 32768;
#pragma unroll
    for (int j = 0; j < 4; ++j)
      foff[j] = (isgu ? (unsigned)((lane >> 1) * (F_ * 2) + b * 32) : (unsigned)((lane >> 1) * 1024 + (b - DN0) * 32)) + (lane & 1) * 16 + j * (fstr - 1024);
    auto ffn = [&](int q0, int q1) {
      if (isgu) {
#pragma unroll 1
        for (int p = q0; p < min(q1, 8); ++p) glds4((const char*)(p < 4 ? wg : wu) + (p & 3) * (128 * F_ * 2), foff, lds0 + L_F + p * 4096);
      } else if (isdn) {
#pragma unroll 1
        for (int p = q0; p < min(q1, 10); ++p) glds4((const char*)wd + p * 131072, foff, lds0 + L_F + p * 4096);
        if (q1 > 10) {
#pragma unroll 1
          for (int p = 40; p < 43; ++p) glds16((const char*)wd + p * 32768, foff[0], lds0 + L_F40 + (p - 40) * 2048);
        }
      }
    };
    ffn(0, 11);
    unsigned wooff[4];
#pragma unroll
    for (int j = 0; j < 4; ++j) wooff[j] = (unsigned)((lane >> 3) * 1024 + cb * 128 + (((lane & 7) ^ (((lane >> 4) & 1) << 2)) * 16) + j * 7168);
    BAR();
    WAIT_VM(24);
#pragma unroll 1
    for (int p = 0; p < 8; ++p) glds4((const char*)wo + p * 32768, wooff, lds0 + L_WO + p * 4096);
    WAIT_VM(24);
#pragma unroll 1
    for (int p = 8; p < 16; ++p) glds4((const char*)wo + p * 32768, wooff, lds0 + L_WO + p * 4096);
#pragma unroll 1
    for (unsigned spins = 0; spins < SPIN_CAP; ++spins) {
      FENCE();
      bool ok = true;
#pragma unroll
      for (int e = 0; e < 4; ++e) { const u32x2 f = LD64(lane * 128, WS_FLAG2 + e * 8192); ok &= (f[0] == tag[0]) & (f[1] == tag[1]); }
      if (__all(ok)) break;
      __builtin_amdgcn_s_sleep(16);
    }
    if (b == 0 && lane == 0) ST64(tag, 0, WS_EPOCH);
    WAIT_VM(0);
    STAMP(2);
    BAR();
    BAR(); BAR();
    u32x4 pv[8];
    {
      const u32x4 own = nosent4(ldsr128(lds + L_ST + l16));
      ST128(own, l16, WS_XS + (pm * 8 + cb) * 1024);
      STAMP(6);
      __builtin_amdgcn_s_sleep(6);
#pragma unroll
      for (int e = 0; e < 8; ++e) pv[e] = LD128(l16, WS_XS + (pm * 8 + e) * 1024);
#pragma unroll 1
      for (unsigned spins = 0; spins < SPIN_CAP; ++spins) {
        bool miss[8], any = false;
#pragma unroll
        for (int e = 0; e < 8; ++e) { miss[e] = __any(umax4(pv[e]) == SENT); any |= miss[e]; }
        if (!any) break;
        FENCE();
#pragma unroll
        for (int e = 0; e < 8; ++e) if (miss[e]) pv[e] = LD128(l16, WS_XS + (pm * 8 + e) * 1024);
      }
    }
    STAMP(3);
#define SUM8(c) (((__uint_as_float(pv[0][c]) + __uint_as_float(pv[1][c])) + (__uint_as_float(pv[2][c]) + __uint_as_float(pv[3][c]))) + \
                 ((__uint_as_float(pv[4][c]) + __uint_as_float(pv[5][c])) + (__uint_as_float(pv[6][c]) + __uint_as_float(pv[7][c]))))
    const float mean = SUM8(0) * (1.f / D_);
    const float rstd = __builtin_amdgcn_rsqf(__builtin_fmaxf(SUM8(1) * (1.f / D_) - mean * mean, 0.f) + LN_EPS);
    if (cb == 0) {
      const unsigned u = __float_as_uint(rstd * (SUM8(2) - mean * sw));
      const unsigned ordv = (u & 0x80000000u) ? ~u : (u | 0x80000000u);
      __builtin_amdgcn_raw_buffer_store_b32(max((ordv >> 1) & 0x7FFFF800u, 0x800u) | (unsigned)(2047 - (pm * 64 + lane)), r_ws, lane * 4, WS_KEY + pm * 256, SC1);
    }
    *(LAS u32x2*)(lds + L_ST + 1024 + lane * 8) = u32x2{__float_as_uint(rstd), __float_as_uint(mean * rstd)};
    STAMP(4);
    LDS_BAR();
    STAMP_FLUSH();
    return;
  }

  if (wid >= 4) {
    const int wrd = 16384 + lw * 3072 + (8 * h + ((lane >> 2) & 3)) * 192 + 32 * ((lane >> 4) & 1) + 8 * (lane & 3);
    float cs[3] = {0.f, 0.f, 0.f};
#pragma unroll 1
    for (int c = 0; c < 8; ++c) {
      if (c < 7) WAIT_BAR(7); else WAIT_BAR(0);
      const LAS char* sb = lds + (c & 3) * K1_SLOT + wrd;
      u32x4 wf[3];
#pragma unroll
      for (int j = 0; j < 3; ++j) wf[j] = ldstr2(sb + j * 64, 768);
      if (c < 6) stage(c + 2);
#pragma unroll
      for (int e = 0; e < 4; ++e)
#pragma unroll
        for (int j = 0; j < 3; ++j) cs[j] = dot2bf(wf[j][e], ONE2, cs[j]);
    }
#pragma unroll
    for (int j = 0; j < 3; ++j) *(LAS float*)(lds + K1_CSP + (lw * 2 + h) * 384 + (32 * j + t) * 4) = cs[j];
    LDS_BAR();
    if (lw == 0 && lane < 24) {
      const LAS char* pp = lds + K1_CSP + l16;
      f32x4 sacc = *(const LAS f32x4*)pp;
#pragma unroll
      for (int e = 1; e < 8; ++e) sacc += *(const LAS f32x4*)(pp + e * 384);
      *(LAS f32x4*)(lds + K1_CS + l16) = sacc;
    }
    LDS_BAR();
    BAR();
  } else {
    if (cb1 < 10) qkv_body<0>(r_ws, lds, rb1, cb1, wid STAMP_PASS);
    else if (cb1 == 10) qkv_body<1>(r_ws, lds, rb1, cb1, wid STAMP_PASS);
    else qkv_body<3>(r_ws, lds, rb1, cb1, wid STAMP_PASS);
    WAIT_BAR(0);
    STAMP(4);
  }

  const bool onel = (lane & 16) != 0;
  unsigned kaddr = lds0 + L_KV + t * 32 + h * 16;
  unsigned vaddr = lds0 + (onel ? L_ONE + ((lane & 11) ? 16 : 0) : L_KV + 1024 + h * 256 + (lane & 15) * 16);
  const int vstep = onel ? 0 : 8192;
  const int IDX = 0xDDDDDDDD;
  const int row = pm * 64 + wid * 8 + (lane >> 3), col = cb * 64 + (lane & 7) * 8;
  f32x16 negm, sx, sy = f32x16{}, o = f32x16{};
  u32x4 vpb = u32x4{0, 0, 0, 0};
  asm volatile("" : "+{v[0:15]}"(o), "+{v[48:63]}"(sy), "+{v[84:87]}"(vpb));

  BAR();
  STAMP(5);
  const u32x4 xres = *(const u32x4*)(x + row * D_ + col), wrv = *(const u32x4*)(wr + col);
  const int mh = wid >> 2, kq = wid & 3;
  const u32x4 qf = ldsr128(lds + L_Q + kq * 2048 + mh * 1024 + t * 32 + h * 16);
  u32x4 kn, va, vb;
  {
    const u32x4 k0 = ldsr128((const LAS char*)(uintptr_t)kaddr);
    kn = ldsr128((const LAS char*)(uintptr_t)kaddr + 2048);
    va = ldsr128((const LAS char*)(uintptr_t)vaddr); vb = ldsr128((const LAS char*)(uintptr_t)vaddr + 512);
    const float m = -rowmax16(mfma32(k0, qf, f32x16{}));
#pragma unroll
    for (int r = 0; r < 16; ++r) negm[r] = m;
    sx = mfma32(k0, qf, negm);
  }
  STAMP(6);
  {
    unsigned cnt;
    asm volatile(
        "s_mov_b32 %[cnt], 0\n\t"
        "s_nop 7\n\t"
        "s_cmp_gt_u32 %[wid], 3\n\t"
        "s_cbranch_scc1 3f\n\t"
        "s_setprio 1\n\t"
        ATTN_LOOP("1", "2", "", "", "")
        "s_setprio 0\n\t"
        "s_branch 5f\n\t"
        ATTN_LOOP("3", "4", "s_setprio 2\n\t", "", "s_setprio 0\n\t")
        "5:\n\t"
        "s_nop 1\n\t"
        SMF(84, 48, 8)
        "s_waitcnt lgkmcnt(0)\n\t"
        "s_nop 11\n\t"
        : "+{v[0:15]}"(o), "+{v[32:47]}"(sx), "+{v[48:63]}"(sy), "+{v[68:71]}"(kn), "+{v[72:75]}"(va), "+{v[76:79]}"(vb),
          "+{v[84:87]}"(vpb), "+{v89}"(kaddr), "+{v90}"(vaddr), [cnt] "=&s"(cnt)
        : "{v[16:31]}"(negm), "{v[64:67]}"(qf), "{v88}"(IDX), "{v91}"(vstep), [wid] "s"(wid)
        : "v80", "v81", "v82", "v83", "scc", "memory");
  }
  STAMP(7);
  {
    const float chk = ((__builtin_fabsf(o[0]) + __builtin_fabsf(o[1])) + (__builtin_fabsf(o[2]) + __builtin_fabsf(o[3]))) +
                      ((__builtin_fabsf(o[4]) + __builtin_fabsf(o[5])) + (__builtin_fabsf(o[6]) + __builtin_fabsf(o[7])));
    const bool bad = !(o[8] < 1.8446744e19f) || !(chk < 3.0e38f);
    if (__builtin_expect(__any(bad), 0)) {
      float m = -INFINITY;
      o = f32x16{};
      const int kqo = head * 131072 + t * 32 + h * 16, vq = head * 131072 + 1024 + h * 256 + (lane & 15) * 16;
      const u32x4 ones = u32x4{ONE2, ONE2, ONE2, ONE2};
#pragma unroll 1
      for (int i = 0; i < 64; ++i) {
        f32x16 s = mfma32(LD128(kqo, WS_KV + i * 2048), qf, f32x16{});
        u32x4 v0 = LD128(vq, WS_KV + i * 2048), v1 = LD128(vq + 512, WS_KV + i * 2048);
        if (onel) { v0 = ones; v1 = ones; }
        const float mn = __builtin_fmaxf(m, rowmax16(s));
        const float al = __builtin_amdgcn_exp2f(m - mn);
        m = mn;
#pragma unroll
        for (int r = 0; r < 16; ++r) { o[r] *= al; s[r] = __builtin_amdgcn_exp2f(s[r] - mn); }
        o = smfmac32(v0, lo8(s), o, IDX);
        o = smfmac32(v1, hi8(s), o, IDX);
      }
    }
  }
  {
    const float inv = __builtin_amdgcn_rcpf(o[8]);
    ST128(nosent4(u32x4{pkbf(o[0] * inv, o[1] * inv), pkbf(o[2] * inv, o[3] * inv), pkbf(o[4] * inv, o[5] * inv), pkbf(o[6] * inv, o[7] * inv)}),
          t * 32 + h * 16, WS_OF + (qb * 4 + kq) * 65536 + head * 2048 + mh * 1024);
  }
  STAMP(8);

  const int g = lane >> 4, q = (lane >> 2) & 3, p4 = lane & 3;
  f32x4 a0 = f32x4{bflo(xres[0]), bfhi(xres[0]), bflo(xres[1]), bfhi(xres[1])}, a1 = f32x4{bflo(xres[2]), bfhi(xres[2]), bflo(xres[3]), bfhi(xres[3])};
  unsigned ra0, ra1;
  {
    const int lr = wid * 8 + (lane >> 3);
    const int sl = ((lane & 7) ^ ((lr & 7) | ((lr & 1) << 3))) * 16;
    const unsigned rbase = lds0 + (mh ? L_WO : L_PA) + (lr & 31) * 256;
    ra0 = rbase + sl; ra1 = rbase + (sl ^ 128);
  }
  LAS unsigned* cand = (LAS unsigned*)(lds + L_SEL);
  {
    u32x4 of[8], wf0[8], wf1[8];
    const int flip = ((q >> 1) & 1) << 6;
    const LAS char* wb = lds + L_WO + kq * 16384 + (8 * h + q) * 128 + 32 * (g & 1) + 8 * p4;
#pragma unroll
    for (int e = 0; e < 8; ++e) { wf0[e] = ldstr2(wb + flip + e * 2048, 512); wf1[e] = ldstr2(wb + (64 ^ flip) + e * 2048, 512); }
    const int ooff = t * 32 + h * 16, osoff = WS_OF + pm * 65536 + kq * 16384 + mh * 1024;
    unsigned pa[8];
    {
      const unsigned rb = lds0 + (mh ? L_WO + kq * 16384 : L_PA + kq * 8192) + t * 256;
      const int swz = (t & 7) | ((t & 1) << 3);
#pragma unroll
      for (int gq = 0; gq < 8; ++gq) { pa[gq] = rb + (((h * 8 + gq) ^ swz) * 16); asm volatile("" : "+v"(pa[gq])); }
    }
    asm volatile("" : "+v"(a0), "+v"(a1), "+v"(ra0), "+v"(ra1));
    __builtin_amdgcn_s_waitcnt(0xC07F);
    FENCE();
    if (!mh) *(volatile LAS unsigned*)(lds + L_WF + kq * 4) = 1u;
    else {
#pragma unroll 1
      for (unsigned spins = 0; spins < (1u << 24); ++spins) {
        if (__builtin_amdgcn_readfirstlane(*(volatile LAS unsigned*)(lds + L_WF + kq * 4))) break;
        __builtin_amdgcn_s_sleep(1);
      }
    }
    FENCE();
    if (LATEO > 1) __builtin_amdgcn_s_sleep(LATEO);
#pragma unroll
    for (int e = 0; e < 8; ++e) of[e] = LD128(ooff, osoff + e * 2048);
    f32x16 acc0 = f32x16{}, acc1 = f32x16{};
#pragma unroll 1
    for (unsigned spins = 0; spins < SPIN_CAP; ++spins) {
      bool miss[8], any = false;
#pragma unroll
      for (int e = 0; e < 8; ++e) { miss[e] = __any(umax4(of[e]) == SENT); any |= miss[e]; }
      if (!any) break;
      FENCE();
#pragma unroll
      for (int e = 0; e < 8; ++e) if (miss[e]) of[e] = LD128(ooff, osoff + e * 2048);
    }
#pragma unroll
    for (int e = 0; e < 8; ++e) { acc0 = mfma32(wf0[e], of[e], acc0); acc1 = mfma32(wf1[e], of[e], acc1); }
    STAMP(9);
#pragma unroll
    for (int gq = 0; gq < 4; ++gq) {
      *(LAS f32x4*)(uintptr_t)pa[gq] = f32x4{acc0[4 * gq], acc0[4 * gq + 1], acc0[4 * gq + 2], acc0[4 * gq + 3]};
      *(LAS f32x4*)(uintptr_t)pa[4 + gq] = f32x4{acc1[4 * gq], acc1[4 * gq + 1], acc1[4 * gq + 2], acc1[4 * gq + 3]};
    }
  }
  LDS_BAR();
  STAMP(10);
  float hv[8];
  {
    const LAS char* r0 = (const LAS char*)(uintptr_t)ra0;
    const LAS char* r1 = (const LAS char*)(uintptr_t)ra1;
    f32x4 t0[4], t1[4];
    if (mh) {
#pragma unroll
      for (int e = 0; e < 4; ++e) { t0[e] = *(const LAS f32x4*)(r0 + e * 16384); t1[e] = *(const LAS f32x4*)(r1 + e * 16384); }
    } else {
#pragma unroll
      for (int e = 0; e < 4; ++e) { t0[e] = *(const LAS f32x4*)(r0 + e * 8192); t1[e] = *(const LAS f32x4*)(r1 + e * 8192); }
    }
    __builtin_amdgcn_sched_barrier(0);
    a0 += (t0[0] + t0[1]) + (t0[2] + t0[3]);
    a1 += (t1[0] + t1[1]) + (t1[2] + t1[3]);
    const u32x4 hb = u32x4{pkbf(a0[0], a0[1]), pkbf(a0[2], a0[3]), pkbf(a1[0], a1[1]), pkbf(a1[2], a1[3])};
    float s1 = 0.f, s2 = 0.f, sd = 0.f;
#pragma unroll
    for (int e = 0; e < 4; ++e) { s1 = dot2bf(hb[e], ONE2, s1); s2 = dot2bf(hb[e], hb[e], s2); sd = dot2bf(hb[e], wrv[e], sd); }
    s1 = sum8(s1); s2 = sum8(s2); sd = sum8(sd);
    if ((lane & 7) == 0) *(LAS u32x4*)(lds + L_ST + wid * 128 + lane * 2) = u32x4{__float_as_uint(s1), __float_as_uint(s2), __float_as_uint(sd), 0u};
    LDS_BAR();
    if (tid < 80) cand[tid] = 0;
#pragma unroll
    for (int e = 0; e < 4; ++e) { hv[2 * e] = bflo(hb[e]); hv[2 * e + 1] = bfhi(hb[e]); }
  }
  STAMP(11);
  const int trd = (8 * g + q) * 32 + 8 * p4;
  u32x4 fw[8];
  if (isgu || isdn) {
#pragma unroll
    for (int p = 0; p < 8; ++p) {
      const int po = isgu ? L_F + ((p >> 2) * 16 + (wid & 3) * 4 + (p & 3)) * 1024 : p < 5 ? L_F + (p * 8 + wid) * 1024 : L_F40 + min(wid, 2) * 2048;
      fw[p] = ldstr2(lds + po + trd, 128);
    }
  }
  u32x4 k4;
  LDS_BAR();
  {
    STAMP(12);
    const u32x2 st = *(const LAS u32x2*)(lds + L_ST + 1024 + wid * 64 + (lane >> 3) * 8);
    const float rstd = __uint_as_float(st[0]), mr = __uint_as_float(st[1]);
    u32x4 hp, op;
#pragma unroll
    for (int e = 0; e < 4; ++e) {
      hp[e] = pkbf(hv[2 * e] * rstd - mr, hv[2 * e + 1] * rstd - mr);
      op[e] = pkbf(hv[2 * e] + bflo(hp[e]), hv[2 * e + 1] + bfhi(hp[e]));
    }
    ST128(nosent4(hp), row * 1024 + col * 2, WS_HN);
    __builtin_amdgcn_raw_buffer_store_b128(nosent4(op), r_out, row * 1024 + col * 2, 0, SC1);
  }
  STAMP(13);
  if (!isgu && !isdn) { STAMP_FLUSH(); return; }
#pragma unroll 1
  for (unsigned spins = 0; spins < SPIN_CAP; ++spins) {
    FENCE();
    k4 = LD128(tid * 16, WS_KEY);
    if (__all(umax4(k4) != SENT)) break;
  }
  STAMP(14);
  {
    unsigned m = umax4(k4);
    m = max(m, dppu<0xB1>(m)); m = max(m, dppu<0x4E>(m)); m = max(m, dppu<0x141>(m)); m = max(m, dppu<0x140>(m));
    m = max(m, dppu<0x142>(m)); m = max(m, dppu<0x143>(m));
    if (lane == 63) cand[64 + wid] = m;
  }
  LDS_BAR();
  {
    const u32x4 g0 = *(LAS u32x4*)(cand + 64), g1 = *(LAS u32x4*)(cand + 68);
    const unsigned T = min(min(min(g0[0], g0[1]), min(g0[2], g0[3])), min(min(g1[0], g1[1]), min(g1[2], g1[3])));
    const unsigned c0 = k4[0] >= T, c1 = k4[1] >= T, c2 = k4[2] >= T, c3 = k4[3] >= T;
    const unsigned cnt = (c0 + c1) + (c2 + c3);
    if (cnt) {
      unsigned pos;
      asm volatile("ds_add_rtn_u32 %0, %1, %2\n\ts_waitcnt lgkmcnt(0)" : "=v"(pos) : "v"((unsigned)(lds0 + L_SEL + 72 * 4)), "v"(cnt) : "memory");
      cand[c0 && pos < 64 ? pos : 73] = k4[0]; pos += c0;
      cand[c1 && pos < 64 ? pos : 73] = k4[1]; pos += c1;
      cand[c2 && pos < 64 ? pos : 73] = k4[2]; pos += c2;
      cand[c3 && pos < 64 ? pos : 73] = k4[3];
    }
  }
  LDS_BAR();
  LAS unsigned* sel = cand + 80 + wid * 8;
  {
    unsigned nv = cand[72];
    unsigned c = cand[t];
    u32x4 o4[4];
#pragma unroll
    for (int e = 0; e < 4; ++e) o4[e] = *(LAS u32x4*)(cand + h * 16 + e * 4);
    asm volatile("" : "+v"(nv), "+v"(c), "+v"(o4[0]), "+v"(o4[1]), "+v"(o4[2]), "+v"(o4[3]));
    const unsigned n = __builtin_amdgcn_readfirstlane(nv);
    if (n <= 64 && wid >= (isgu ? 4 : 1)) {
      if (isgu) { STAMP_FLUSH(); return; }
    } else if (__builtin_expect(n <= 32, 1)) {
      unsigned ba = 0, bb = 0;
#pragma unroll
      for (int e = 0; e < 4; ++e) {
        ba = __builtin_amdgcn_alignbit(ba, c - o4[e][0], 31); bb = __builtin_amdgcn_alignbit(bb, c - o4[e][1], 31);
        ba = __builtin_amdgcn_alignbit(ba, c - o4[e][2], 31); bb = __builtin_amdgcn_alignbit(bb, c - o4[e][3], 31);
      }
      unsigned rk = __builtin_popcount(ba) + __builtin_popcount(bb);
      auto r2 = __builtin_amdgcn_permlane32_swap(rk, rk, false, false);
      rk = r2[0] + r2[1];
      if (rk < 8) sel[rk] = c;
    } else if (n <= 64) {
      const unsigned c2 = cand[lane];
      unsigned rk = 0;
#pragma unroll 1
      for (int j = 0; j < 64; j += 4) { const u32x4 v = *(LAS u32x4*)(cand + j); rk += (v[0] > c2) + (v[1] > c2) + (v[2] > c2) + (v[3] > c2); }
      if (rk < 8) sel[rk] = c2;
    } else {
      LAS unsigned* all = cand + 256;
      *(LAS u32x4*)(all + tid * 4) = k4;
      LDS_BAR();
      unsigned rk[4] = {0, 0, 0, 0};
#pragma unroll 1
      for (int j = 0; j < 2048; ++j) {
        const unsigned v = all[j];
#pragma unroll
        for (int e = 0; e < 4; ++e) rk[e] += v > k4[e];
      }
#pragma unroll
      for (int e = 0; e < 4; ++e) if (rk[e] < 8) cand[160 + rk[e]] = k4[e];
      LDS_BAR();
      sel = cand + 160;
      if (isgu && wid >= 4) { STAMP_FLUSH(); return; }
    }
  }
  const int tok = lane & 15;
  const unsigned key = sel[tok & 7];
  const int trow = 2047 - (int)(key & 2047u);
  STAMP(15);
  LAS f32x4* red = (LAS f32x4*)(lds + L_WO);
  if (isgu) {
    u32x4 xb[4];
#pragma unroll 1
    for (unsigned spins = 0; spins < SPIN_CAP; ++spins) {
      FENCE();
#pragma unroll
      for (int u = 0; u < 4; ++u) xb[u] = LD128(trow * 1024 + u * 64 + g * 16, WS_HN + wid * 256);
      if (__all(max(max(umax4(xb[0]), umax4(xb[1])), max(umax4(xb[2]), umax4(xb[3]))) != SENT)) break;
    }
    STAMP(16);
    f32x4 ag = f32x4{}, au = f32x4{};
#pragma unroll
    for (int u = 0; u < 4; ++u) { ag = mfma16(fw[u], xb[u], ag); au = mfma16(fw[4 + u], xb[u], au); }
    const f32x4 mine = tok < 8 ? ag : au;
    if (wid != 0) red[wid * 64 + lane] = mine;
    LDS_BAR();
    if (wid == 0) {
      const f32x4 s = (mine + red[64 + lane]) + (red[128 + lane] + red[192 + lane]);
      float av[4];
#pragma unroll
      for (int r = 0; r < 4; ++r) {
        const float gg = rbf(s[r]);
        const float uu = dppf<0x128>(gg);
        const float sl = rbf(gg * __builtin_amdgcn_rcpf(1.f + __expf(-gg)));
        av[r] = sl * uu;
      }
      if (tok < 8) ST64((u32x2{nosent(pkbf(av[0], av[1])), nosent(pkbf(av[2], av[3]))}), tok * 32 + g * 8, WS_ACT + b * 256);
    }
    STAMP(17);
    STAMP_FLUSH();
    return;
  }
  const int nb = b - DN0;
  u32x2 o0 = u32x2{0, 0};
  float sg = 0.f;
  if (wid == 0) {
#pragma unroll 1
    for (unsigned spins = 0; spins < SPIN_CAP; ++spins) {
      FENCE();
      o0 = __builtin_amdgcn_raw_buffer_load_b64(r_out, trow * 1024 + g * 8, nb * 32, SC1);
      if (__all(max(o0[0], o0[1]) != SENT)) break;
    }
    const unsigned ov = ((key & 0x7FFFF800u) << 1) | 0x800u;
    const float r = rbf(__uint_as_float((ov & 0x80000000u) ? (ov & 0x7FFFFFFFu) : ~ov));
    sg = rbf(__builtin_amdgcn_rcpf(1.f + __expf(-r)));
  }
  const __amdgpu_buffer_rsrc_t r_act = mkrsrc(ws + WS_ACT, 22016);
  u32x4 ab[6];
  const int aoff = wid * 512 + (g >> 1) * 256 + (tok & 7) * 32 + (g & 1) * 16;
#pragma unroll
  for (int p = 0; p < 6; ++p) ab[p] = __builtin_amdgcn_raw_buffer_load_b128(r_act, aoff, p * 4096, SC1);
#pragma unroll 1
  for (unsigned spins = 0; spins < SPIN_CAP; ++spins) {
    bool miss[6], any = false;
#pragma unroll
    for (int p = 0; p < 6; ++p) { miss[p] = __any(umax4(ab[p]) == SENT); any |= miss[p]; }
    if (!any) break;
    FENCE();
#pragma unroll
    for (int p = 0; p < 6; ++p) if (miss[p]) ab[p] = __builtin_amdgcn_raw_buffer_load_b128(r_act, aoff, p * 4096, SC1);
  }
  STAMP(16);
  f32x4 ay = f32x4{};
#pragma unroll
  for (int p = 0; p < 6; ++p) ay = mfma16(fw[p], ab[p], ay);
  red[wid * 64 + lane] = ay;
  LDS_BAR();
  if (wid == 0 && tok < 8) {
    ay = ((ay + red[64 + lane]) + (red[128 + lane] + red[192 + lane])) + ((red[256 + lane] + red[320 + lane]) + (red[384 + lane] + red[448 + lane]));
    const float y0 = rbf(rbf(ay[0]) * sg), y1 = rbf(rbf(ay[1]) * sg), y2 = rbf(rbf(ay[2]) * sg), y3 = rbf(rbf(ay[3]) * sg);
    __builtin_amdgcn_raw_buffer_store_b64(u32x2{pkbf(bflo(o0[0]) + y0, bfhi(o0[0]) + y1), pkbf(bflo(o0[1]) + y2, bfhi(o0[1]) + y3)}, r_out,
                                          trow * 1024 + g * 8, nb * 32, SC1);
  }
  STAMP(17);
}

extern "C" void kernel_launch(void* const* d_in, const int* in_sizes, int n_in, void* d_out, int out_size, void* d_ws, size_t ws_size,
                              hipStream_t stream) {
  (void)hipFuncSetAttribute((const void*)fused_block, hipFuncAttributeMaxDynamicSharedMemorySize, LDS_BYTES);
  fused_block<<<256, 576, LDS_BYTES, stream>>>((const u16*)d_in[0], (const u16*)d_in[1], (const u16*)d_in[2], (const u16*)d_in[4],
                                               (const u16*)d_in[5], (const u16*)d_in[6], (const u16*)d_in[7], (char*)d_ws, (u16*)d_out);
}
```

```cpp
#include <hip/hip_runtime.h>
#include <hip/hip_bf16.h>
#define STAMP_DECL
#define STAMP(s)
#define STAMP_FLUSH()
#define STAMP_ARG
#define STAMP_PASS

#define PROG 0
#define CODEPF 1
#define CODE_KB 25
#define K1ROT 3
#define DNDELAY 0
#define KEYDELAY 0
#define XSDELAY 10
#define FLDELAY 0
#define LATEO 4
typedef unsigned short u16;
typedef __bf16 bf16x2_t __attribute__((ext_vector_type(2)));
typedef __bf16 bf16x8_t __attribute__((ext_vector_type(8)));
typedef __bf16 bf16x16_t __attribute__((ext_vector_type(16)));
typedef short s16x4 __attribute__((ext_vector_type(4)));
typedef float f32x4 __attribute__((ext_vector_type(4)));
typedef float f32x8 __attribute__((ext_vector_type(8)));
typedef float f32x16 __attribute__((ext_vector_type(16)));
typedef unsigned u32x2 __attribute__((ext_vector_type(2)));
typedef unsigned u32x4 __attribute__((ext_vector_type(4)));
#define LAS __attribute__((address_space(3)))

constexpr int D_ = 512, F_ = 1376;
constexpr unsigned SENT = 0xFFFFFFFFu;
constexpr float LN_EPS = 1e-5f;
constexpr float QSCALE = 0.25f * 1.4426950408889634f;
constexpr unsigned SPIN_CAP = 1u << 16;
constexpr int SC1 = 16;
constexpr unsigned ONE2 = 0x3F803F80u;

constexpr unsigned WS_KV = 0;
constexpr unsigned WS_QF = 4u << 20;
constexpr unsigned WS_OF = 6u << 20;
constexpr unsigned WS_HN = 8u << 20;
constexpr unsigned WS_XS = 10u << 20;
constexpr unsigned WS_ACT = WS_XS + (256u << 10);
constexpr unsigned WS_KEY = WS_ACT + 22016;
constexpr unsigned WS_FLAG = WS_XS + (320u << 10);
constexpr unsigned WS_FLAG2 = WS_FLAG + 32768;
constexpr unsigned WS_EPOCH = WS_FLAG2 + 32768;
constexpr unsigned WS_BYTES = WS_EPOCH + 128;

__device__ __forceinline__ f32x16 mfma32(u32x4 a, u32x4 b, f32x16 c) {
  return __builtin_amdgcn_mfma_f32_32x32x16_bf16(__builtin_bit_cast(bf16x8_t, a), __builtin_bit_cast(bf16x8_t, b), c, 0, 0, 0);
}
__device__ __forceinline__ f32x4 mfma16(u32x4 a, u32x4 b, f32x4 c) {
  return __builtin_amdgcn_mfma_f32_16x16x32_bf16(__builtin_bit_cast(bf16x8_t, a), __builtin_bit_cast(bf16x8_t, b), c, 0, 0, 0);
}
__device__ __forceinline__ f32x16 smfmac32(u32x4 a, f32x8 b, f32x16 c, int idx) {
  return __builtin_amdgcn_smfmac_f32_32x32x32_bf16(__builtin_bit_cast(bf16x8_t, a), __builtin_bit_cast(bf16x16_t, b), c, idx, 0, 0);
}
__device__ __forceinline__ float dot2bf(unsigned a, unsigned b, float c) {
  return __builtin_amdgcn_fdot2_f32_bf16(__builtin_bit_cast(bf16x2_t, a), __builtin_bit_cast(bf16x2_t, b), c, false);
}
__device__ __forceinline__ float swap32_sum(float v) {
  auto r = __builtin_amdgcn_permlane32_swap(__float_as_uint(v), __float_as_uint(v), false, false);
  return __uint_as_float(r[0]) + __uint_as_float(r[1]);
}
__device__ __forceinline__ float swap32_max(float v) {
  auto r = __builtin_amdgcn_permlane32_swap(__float_as_uint(v), __float_as_uint(v), false, false);
  return __builtin_fmaxf(__uint_as_float(r[0]), __uint_as_float(r[1]));
}
__device__ __forceinline__ unsigned pkbf(float lo, float hi) {
  bf16x2_t v; v[0] = (__bf16)lo; v[1] = (__bf16)hi; return __builtin_bit_cast(unsigned, v);
}
__device__ __forceinline__ float bflo(unsigned u) { return __uint_as_float(u << 16); }
__device__ __forceinline__ float bfhi(unsigned u) { return __uint_as_float(u & 0xFFFF0000u); }
__device__ __forceinline__ float rbf(float f) { return (float)(__bf16)f; }
__device__ __forceinline__ void glds16(const void* sbase, unsigned voff, unsigned lds_base) {
  unsigned sv;
  asm volatile("s_mov_b32 %0, m0\n\ts_mov_b32 m0, %3\n\ts_nop 0\n\tglobal_load_lds_dwordx4 %1, %2\n\ts_mov_b32 m0, %0"
               : "=&s"(sv) : "v"(voff), "s"(sbase), "s"(lds_base) : "memory");
}
__device__ __forceinline__ void glds16c(const void* sbase, unsigned voff, unsigned lds_base) {
  unsigned sv;
  asm volatile("s_mov_b32 %0, m0\n\ts_mov_b32 m0, %3\n\ts_nop 0\n\tglobal_load_lds_dwordx4 %1, %2 sc1\n\ts_mov_b32 m0, %0"
               : "=&s"(sv) : "v"(voff), "s"(sbase), "s"(lds_base) : "memory");
}
__device__ __forceinline__ void glds8c(const void* sbase, unsigned voff, unsigned lds_base) {
  unsigned sv;
  asm volatile("s_mov_b32 %0, m0\n\ts_mov_b32 m0, %3\n\ts_nop 0\n\t"
               "global_load_lds_dwordx4 %1, %2 offset:-4096 sc1\n\tglobal_load_lds_dwordx4 %1, %2 offset:-3072 sc1\n\t"
               "global_load_lds_dwordx4 %1, %2 offset:-2048 sc1\n\tglobal_load_lds_dwordx4 %1, %2 offset:-1024 sc1\n\t"
               "global_load_lds_dwordx4 %1, %2 sc1\n\tglobal_load_lds_dwordx4 %1, %2 offset:1024 sc1\n\t"
               "global_load_lds_dwordx4 %1, %2 offset:2048 sc1\n\tglobal_load_lds_dwordx4 %1, %2 offset:3072 sc1\n\t"
               "s_mov_b32 m0, %0"
               : "=&s"(sv) : "v"(voff), "s"((const char*)sbase + 4096), "s"(lds_base + 4096) : "memory");
}
__device__ __forceinline__ void glds4(const void* sbase, const unsigned (&voff)[4], unsigned lds_base) {
  unsigned sv;
  asm volatile("s_mov_b32 %0, m0\n\ts_mov_b32 m0, %6\n\ts_nop 0\n\t"
               "global_load_lds_dwordx4 %1, %5\n\tglobal_load_lds_dwordx4 %2, %5 offset:1024\n\t"
               "global_load_lds_dwordx4 %3, %5 offset:2048\n\tglobal_load_lds_dwordx4 %4, %5 offset:3072\n\t"
               "s_mov_b32 m0, %0"
               : "=&s"(sv) : "v"(voff[0]), "v"(voff[1]), "v"(voff[2]), "v"(voff[3]), "s"(sbase), "s"(lds_base) : "memory");
}
#define FENCE() asm volatile("" ::: "memory")
#define WAIT_BAR(N) asm volatile("s_waitcnt vmcnt(" #N ") lgkmcnt(0)\n\ts_barrier" ::: "memory")
#define VM_BAR(N) asm volatile("s_waitcnt vmcnt(" #N ")\n\ts_barrier" ::: "memory")
#define LDS_BAR() do { FENCE(); __builtin_amdgcn_s_waitcnt(0xC07F); __builtin_amdgcn_s_barrier(); FENCE(); } while (0)
#define BAR() do { FENCE(); __builtin_amdgcn_s_barrier(); FENCE(); } while (0)
#define WAIT_VM(N) asm volatile("s_waitcnt vmcnt(" #N ")" ::: "memory")
#define LD128(off, soff) __builtin_amdgcn_raw_buffer_load_b128(r_ws, off, soff, SC1)
#define LD64(off, soff) __builtin_amdgcn_raw_buffer_load_b64(r_ws, off, soff, SC1)
#define ST128(v, off, soff) __builtin_amdgcn_raw_buffer_store_b128(v, r_ws, (off) + (soff), 0, SC1)
#define ST64(v, off, soff) __builtin_amdgcn_raw_buffer_store_b64(v, r_ws, off, soff, SC1)
__device__ __forceinline__ u32x4 ldsr128(const LAS char* p) { return *(const LAS u32x4*)p; }
__device__ __forceinline__ u32x2 ldstr(const LAS char* p) {
  return __builtin_bit_cast(u32x2, __builtin_amdgcn_ds_read_tr16_b64_v4i16((LAS s16x4*)p));
}
__device__ __forceinline__ u32x4 ldstr2(const LAS char* p, int off2) { const u32x2 a = ldstr(p), b = ldstr(p + off2); return u32x4{a[0], a[1], b[0], b[1]}; }
__device__ __forceinline__ int swapb23(int v) { return (v & ~12) | ((v & 4) << 1) | ((v & 8) >> 1); }
__device__ __forceinline__ unsigned nosent(unsigned v) { return min(v, 0xFFFFFFFEu); }
__device__ __forceinline__ u32x4 nosent4(u32x4 v) { return u32x4{nosent(v[0]), nosent(v[1]), nosent(v[2]), nosent(v[3])}; }
__device__ __forceinline__ unsigned umax4(u32x4 v) { return max(max(v[0], v[1]), max(v[2], v[3])); }
__device__ __forceinline__ __amdgpu_buffer_rsrc_t mkrsrc(const void* p, unsigned bytes) {
  return __builtin_amdgcn_make_buffer_rsrc((void*)p, 0, bytes, 0x00020000);
}
template <int CTRL> __device__ __forceinline__ float dppf(float v) {
  return __uint_as_float(__builtin_amdgcn_update_dpp(0, __float_as_uint(v), CTRL, 0xF, 0xF, true));
}
template <int CTRL> __device__ __forceinline__ unsigned dppu(unsigned v) { return __builtin_amdgcn_update_dpp(0, v, CTRL, 0xF, 0xF, true); }
__device__ __forceinline__ unsigned rdl(unsigned v, int l) { return (unsigned)__builtin_amdgcn_readlane((int)v, l); }
__device__ __forceinline__ float rdlf(float v, int l) { return __uint_as_float(rdl(__float_as_uint(v), l)); }
__device__ __forceinline__ float sum8(float v) { v += dppf<0xB1>(v); v += dppf<0x4E>(v); v += dppf<0x141>(v); return v; }
__device__ __forceinline__ u32x4 pack8(const float* v) { return u32x4{pkbf(v[0], v[1]), pkbf(v[2], v[3]), pkbf(v[4], v[5]), pkbf(v[6], v[7])}; }

constexpr int K1_SLOT = 16384 + 12288, K1_CS = 4 * K1_SLOT, K1_CSP = K1_CS + 512, K1_TAG = K1_CSP + 8 * 384;
constexpr int L_Q = 0, L_KV = 24576, L_ONE = L_KV + 131072, LDS_BYTES = 163840;
constexpr int L_F = 0, L_F40 = L_ONE + 1024, L_WO = 40960, L_PA = L_WO + 65536, L_SEL = L_PA + 32768, L_ST = L_SEL + 12288, L_WF = L_ONE + 64;
constexpr int NGU = 86, DN0 = 128, NDN = 32;

template <int NV>
__device__ __forceinline__ void qkv_body(const __amdgpu_buffer_rsrc_t r_ws, LAS char* lds, int rb, int cb, int wid STAMP_ARG) {
  const int lane = threadIdx.x & 63, t = lane & 31, h = lane >> 5;
  int xr[4];
#pragma unroll
  for (int u = 0; u < 4; ++u) xr[u] = wid * 4096 + t * 128 + (((2 * u + h) ^ ((t >> 1) & 7)) * 16);
  const int wrd = 16384 + (8 * h + ((lane >> 2) & 3)) * 192 + 32 * ((lane >> 4) & 1) + 8 * (lane & 3);
  f32x16 acc[3];
#pragma unroll
  for (int j = 0; j < 3; ++j) acc[j] = f32x16{};
  float s1a = 0.f, s1b = 0.f, s2a = 0.f, s2b = 0.f;
  u32x4 xf = u32x4{0, 0, 0, 0}, wf[3];
#pragma unroll
  for (int j = 0; j < 3; ++j) wf[j] = u32x4{0, 0, 0, 0};
  auto mm = [&]() {
#pragma unroll
    for (int j = 0; j < 3; ++j) {
      if (j >= 3 - NV) acc[j] = mfma32(xf, wf[j], acc[j]);
      else acc[j] = mfma32(wf[j], xf, acc[j]);
    }
  };
  const int tile = rb * 4 + wid;
  const int voffV = tile * 2048 + 1024 + (t >> 4) * 131072 + h * 256 + swapb23(t & 15) * 16;
  const int voffK = tile * 2048 + swapb23(t) * 32 + h * 16;
  const int voffQ = tile * 1024 + t * 32 + h * 16;
#pragma unroll 1
  for (int c = 0; c < 8; ++c) {
    LDS_BAR();
    if (c == 0) STAMP(1);
    const LAS char* sb = lds + (c & 3) * K1_SLOT;
    u32x4 xn[4];
#pragma unroll
    for (int u = 0; u < 4; ++u) {
      xn[u] = ldsr128(sb + xr[u]);
      u32x4 wn[3];
#pragma unroll
      for (int j = 0; j < 3; ++j) wn[j] = ldstr2(sb + wrd + u * 3072 + j * 64, 768);
      mm();
#pragma unroll
      for (int z = 0; z < 3; ++z) { __builtin_amdgcn_sched_group_barrier(0x8, 1, 0); __builtin_amdgcn_sched_group_barrier(0x102, 3, 0); }
      __builtin_amdgcn_sched_barrier(0);
      xf = xn[u];
#pragma unroll
      for (int j = 0; j < 3; ++j) wf[j] = wn[j];
    }
#pragma unroll
    for (int u = 0; u < 4; ++u)
#pragma unroll
      for (int e = 0; e < 4; e += 2) {
        s1a = dot2bf(xn[u][e], ONE2, s1a); s2a = dot2bf(xn[u][e], xn[u][e], s2a);
        s1b = dot2bf(xn[u][e + 1], ONE2, s1b); s2b = dot2bf(xn[u][e + 1], xn[u][e + 1], s2b);
      }
    asm volatile("" : "+v"(s1a), "+v"(s1b), "+v"(s2a), "+v"(s2b));
    __builtin_amdgcn_sched_barrier(0);
  }
  __builtin_amdgcn_s_barrier();
  mm();
  STAMP(2);
  LAS float* scr = (LAS float*)(lds + wid * 4096);
  const LAS float* csum = (const LAS float*)(lds + K1_CS);
  const float s2 = swap32_sum(s2a + s2b);
  const float mean = swap32_sum(s1a + s1b) * (1.f / D_);
  const float var = __builtin_fmaxf(s2 * (1.f / D_) - mean * mean, 0.f);
  const float rstd = __builtin_amdgcn_rsqf(var + LN_EPS);
  if (NV > 0 && h == 0) { scr[t] = rstd; scr[32 + t] = mean * rstd; }
  BAR();
#pragma unroll
  for (int j = 0; j < 3; ++j) {
    const int gt = cb * 3 + j;
    float v[16];
    if (j >= 3 - NV) {
      const float cs = csum[32 * j + t];
#pragma unroll
      for (int gq = 0; gq < 4; ++gq) {
        const f32x4 a = *(const LAS f32x4*)(scr + 8 * gq + 4 * h), b = *(const LAS f32x4*)(scr + 32 + 8 * gq + 4 * h);
#pragma unroll
        for (int e = 0; e < 4; ++e) v[4 * gq + e] = acc[j][4 * gq + e] * a[e] - b[e] * cs;
      }
      const int so = WS_KV + (gt - 32) * 262144;
      ST128(pack8(v), voffV, so); ST128(pack8(v + 8), voffV + 512, so);
    } else {
      const bool isq = gt < 16;
      const float sc = isq ? QSCALE : 1.f;
      const float a = rstd * sc, b = mean * rstd * sc;
#pragma unroll
      for (int gq = 0; gq < 4; ++gq) {
        const f32x4 c4 = *(const LAS f32x4*)(csum + 32 * j + 8 * gq + 4 * h);
#pragma unroll
        for (int e = 0; e < 4; ++e) v[4 * gq + e] = acc[j][4 * gq + e] * a - b * c4[e];
      }
      const int vo = isq ? voffQ : voffK;
      const int so = isq ? WS_QF + gt * 131072 : WS_KV + (gt - 16) * 262144;
      ST128(pack8(v), vo, so); ST128(pack8(v + 8), vo, so + (isq ? 65536 : 131072));
    }
  }
  STAMP(3);
}

__device__ __forceinline__ float rowmax16(const f32x16& s) {
  float a = __builtin_fmaxf(__builtin_fmaxf(s[0], s[1]), s[2]), b = __builtin_fmaxf(__builtin_fmaxf(s[3], s[4]), s[5]);
  a = __builtin_fmaxf(__builtin_fmaxf(a, s[6]), s[7]); b = __builtin_fmaxf(__builtin_fmaxf(b, s[8]), s[9]);
  a = __builtin_fmaxf(__builtin_fmaxf(a, s[10]), s[11]); b = __builtin_fmaxf(__builtin_fmaxf(b, s[12]), s[13]);
  a = __builtin_fmaxf(__builtin_fmaxf(a, s[14]), s[15]);
  return swap32_max(__builtin_fmaxf(a, b));
}
__device__ __forceinline__ f32x8 lo8(const f32x16& s) { return __builtin_shufflevector(s, s, 0, 1, 2, 3, 4, 5, 6, 7); }
__device__ __forceinline__ f32x8 hi8(const f32x16& s) { return __builtin_shufflevector(s, s, 8, 9, 10, 11, 12, 13, 14, 15); }

#define E1(X, i) "v_exp_f32 v[" #X "+" #i "], v[" #X "+" #i "]\n\t"
#define SMF(A, B, off) "v_smfmac_f32_32x32x32_bf16 v[0:15], v[" #A ":" #A "+3], v[" #B "+" #off ":" #B "+" #off "+7], v88\n\t"
#define QK(Y) "v_mfma_f32_32x32x16_bf16 v[" #Y ":" #Y "+15], v[68:71], v[64:67], v[16:31]\n\t"
#define RDV(D, off) "ds_read_b128 v[" #D ":" #D "+3], v90 offset:" #off "\n\t"
#define RDK(off) "ds_read_b128 v[68:71], v89 offset:" #off "\n\t"
#define STEP(X, Y, VA, VPB, VA2, VB2, KOFF, VOFF, VOFF2, PA, PB, EXTRA) \
  PA E1(X, 0) E1(X, 1) SMF(VPB, Y, 8) RDV(VA2, VOFF) \
  PB E1(X, 2) E1(X, 3) E1(X, 4) E1(X, 5) \
  "s_waitcnt lgkmcnt(1)\n\t" QK(Y) RDV(VB2, VOFF2) RDK(KOFF) \
  E1(X, 6) E1(X, 7) E1(X, 8) E1(X, 9) \
  SMF(VA, X, 0) EXTRA \
  E1(X, 10) E1(X, 11) E1(X, 12) E1(X, 13) E1(X, 14) E1(X, 15)
#define ATTN_LOOP(L1, L2, P0, P2, P3) \
        L1 ":\n\t" \
        "s_bitcmp1_b32 0x4424, %[cnt]\n\t" \
        "s_cbranch_scc0 " L2 "f\n\t" \
        "s_barrier\n\t" \
        L2 ":\n\t" \
        STEP(32, 48, 72, 84, 80, 84, 4096, 2048, 2560, P0, "", "") \
        STEP(48, 32, 80, 76, 72, 76, 6144, 4096, 4608, "", "", "") \
        STEP(32, 48, 72, 84, 80, 84, 8192, 6144, 6656, P2, "", "v_add_u32 v90, v90, v91\n\t") \
        STEP(48, 32, 80, 76, 72, 76, 10240, 0, 512, "", P3, "v_add_u32 v89, 0x2000, v89\n\ts_add_u32 %[cnt], %[cnt], 1\n\ts_cmp_lg_u32 %[cnt], 16\n\t") \
        "s_cbranch_scc1 " L1 "b\n\t"

__global__ __launch_bounds__(576, 3) void fused_block(const u16* __restrict__ x, const u16* __restrict__ wqkv, const u16* __restrict__ wo,
                                                      const u16* __restrict__ wr, const u16* __restrict__ wg, const u16* __restrict__ wu,
                                                      const u16* __restrict__ wd, char* __restrict__ ws, u16* __restrict__ out) {
  extern __shared__ __attribute__((aligned(16))) char smem[];
  LAS char* lds = (LAS char*)smem;
  const unsigned lds0 = (unsigned)(uintptr_t)lds;
  {
    unsigned long long pc; unsigned s0, s1, v0;
    asm volatile("v_readfirstlane_b32 %1, %4\n\ts_lshr_b32 %2, %5, 3\n\ts_getpc_b64 %0\n\ts_and_b64 %0, %0, -16\n\t"
                 "s_cmp_lt_u32 %1, 64\n\ts_cbranch_scc0 9f\n\t"
                 "s_cmp_lt_u32 %2, %6\n\ts_cbranch_scc0 9f\n\t"
                 "s_lshl_b32 %2, %2, 10\n\tv_lshlrev_b32 %3, 4, %4\n\tv_add_u32 %3, %2, %3\n\t"
                 "s_mov_b32 %1, m0\n\ts_mov_b32 m0, %7\n\ts_nop 0\n\t"
                 "global_load_lds_dwordx4 %3, %0\n\ts_mov_b32 m0, %1\n\t9:"
                 : "=&s"(pc), "=&s"(s0), "=&s"(s1), "=&v"(v0) : "v"(threadIdx.x), "s"(blockIdx.x), "i"(CODE_KB), "s"(lds0 + 131072) : "scc", "memory");
  }
  const int tid = threadIdx.x, lane = tid & 63, t = lane & 31, h = lane >> 5;
  const int wid = __builtin_amdgcn_readfirstlane(tid >> 6);
  const int b = blockIdx.x;
  const int rb1 = 4 * (b & 3) + ((b >> 3) & 3), cb1 = 8 * ((b >> 2) & 1) + (b >> 5);
  const int head = (b & 7) * 4 + ((b >> 3) & 3), qb = b >> 5;
  const int pm = head, cb = qb;
  const unsigned l16 = lane * 16;
  const int lw = wid - 4;
  const int rot = K1ROT == 3 ? ((((b >> 5) + ((b >> 3) & 3)) & 1)) * 4 : K1ROT == 4 ? ((((b >> 5) + ((b >> 3) & 3)) & 3)) * 2 : K1ROT == 5 ? (b & 7) : K1ROT == 6 ? ((b & 3) + ((((b >> 5) + ((b >> 3) & 3)) & 1)) * 4) : 0;
  unsigned xoff[2], woff[3];
  auto stage = [&](int c) {
    const int kc = (c + rot) & 7;
    const char* xs = (const char*)x + kc * 128;
    const char* wsrc = (const char*)wqkv + kc * (64 * 3072);
    const unsigned base = lds0 + (c & 3) * K1_SLOT;
#pragma unroll
    for (int p = 0; p < 4; ++p) glds16(xs + (p >> 1) * 16384, xoff[p & 1], base + lw * 4096 + p * 1024);
#pragma unroll
    for (int p = 0; p < 3; ++p) glds16(wsrc, woff[p], base + 16384 + (lw * 3 + p) * 1024);
  };
  if (lw >= 0 && lw < 4) {
#pragma unroll
    for (int p = 0; p < 2; ++p) {
      const int row = p * 8 + (lane >> 3), slot = lane & 7;
      xoff[p] = (unsigned)((rb1 * 128 + lw * 32 + row) * 1024 + ((slot ^ ((row >> 1) & 7)) * 16));
    }
#pragma unroll
    for (int p = 0; p < 3; ++p) {
      const int idx = (lw * 3 + p) * 64 + lane, k = idx / 12, c = idx - k * 12;
      woff[p] = (unsigned)(k * 3072 + cb1 * 192 + c * 16);
    }
    stage(0); stage(1);
  }
  FENCE();
  const bool isgu = b < NGU, isdn = b >= DN0 && b < DN0 + NDN;
  const __amdgpu_buffer_rsrc_t r_ws = mkrsrc(ws, WS_BYTES), r_out = mkrsrc(out, 2u << 20);
  STAMP_DECL STAMP(0);

  if (wid == 8) {
    __builtin_amdgcn_s_setprio(3);
    const u32x2 ep = LD64(0, WS_EPOCH);
    const u32x4 w4 = *(const u32x4*)(wr + lane * 8);
    if (lane < 16) { const unsigned v = (lane & 1) ? 0u : ONE2; *(LAS u32x4*)(lds + L_ONE + (lane >> 2) * 2048 + ((lane >> 1) & 1) * 512 + (lane & 1) * 16) = u32x4{v, v, v, v}; }
    if (lane == 16) *(LAS u32x4*)(lds + L_WF) = u32x4{0, 0, 0, 0};
#pragma unroll 1
    for (int c = 0; c < 5; ++c) BAR();
    const unsigned long long e64 = (((unsigned long long)ep[1] << 32) | ep[0]) + 1ull;
    const u32x2 tag = u32x2{(unsigned)e64, (unsigned)(e64 >> 32)};
    float sw;
    sw = dot2bf(w4[0], ONE2, dot2bf(w4[1], ONE2, 0.f)) + dot2bf(w4[2], ONE2, dot2bf(w4[3], ONE2, 0.f));
    sw = sum8(sw); sw += dppf<0x140>(sw);
    sw = (rdlf(sw, 0) + rdlf(sw, 16)) + (rdlf(sw, 32) + rdlf(sw, 48));
    const int hp = head >> 1;
    const int fidx = lane < 16 ? lane * 16 + (16 + hp) / 3 : lane < 32 ? (lane - 16) * 16 + (32 + hp) / 3 : (qb * 2 + (lane & 1)) * 16 + hp / 3;
    const char* kvsrc = ws + WS_KV + (size_t)head * 131072;
    LDS_BAR();
#pragma unroll 1
    for (int c = 6; c < 10; ++c) BAR();
    BAR();
    if (lane == 0) ST64(tag, 0, WS_FLAG + (rb1 * 16 + cb1) * 128);
    STAMP(1);
    unsigned long long seen = 0;
    auto wait_flags = [&](unsigned long long need) {
#pragma unroll 1
      for (unsigned spins = 0; spins < SPIN_CAP && (seen & need) != need; ++spins) {
        FENCE();
        const u32x2 f = LD64(fidx * 128, WS_FLAG);
        seen = __ballot((f[0] == tag[0]) & (f[1] == tag[1]));
      }
      FENCE();
    };
    if (FLDELAY) __builtin_amdgcn_s_sleep(FLDELAY);
    wait_flags(0x300070007ull);
    STAMP(5);
    glds8c(ws + WS_QF + head * 65536 + qb * 8192, l16, lds0 + L_Q);
#pragma unroll
    for (int p = 0; p < 3; ++p) glds8c(kvsrc + p * 8192, l16, lds0 + L_KV + p * 8192);
    VM_BAR(0);
    wait_flags(0xFFFFFFFFull);
#pragma unroll 1
    for (int p = 3; p < 10; ++p) glds8c(kvsrc + p * 8192, l16, lds0 + L_KV + p * 8192);
    VM_BAR(32);
    WAIT_VM(8);
#pragma unroll 1
    for (int p = 10; p < 16; ++p) glds8c(kvsrc + p * 8192, l16, lds0 + L_KV + p * 8192);
    WAIT_VM(0);
    {
      const u32x4 s4 = u32x4{SENT, SENT, SENT, SENT};
#pragma unroll
      for (int e = 0; e < 8; ++e) ST128(s4, l16, WS_OF + (qb * 4 + (e >> 1)) * 65536 + head * 2048 + (e & 1) * 1024);
#pragma unroll
      for (int e = 0; e < 8; ++e) {
        const int off = (pm * 64 + e * 8 + (lane >> 3)) * 1024 + cb * 128 + (lane & 7) * 16;
        ST128(s4, off, WS_HN);
        __builtin_amdgcn_raw_buffer_store_b128(s4, r_out, off, 0, SC1);
      }
      ST128(s4, l16, WS_XS + (pm * 8 + cb) * 1024);
      if (lane < 8 && b * 8 + lane < (22016 + 8192) / 16) ST128(s4, l16, WS_ACT + b * 128);
    }
    BAR();
    WAIT_VM(0);
    if (lane == 0) ST64(tag, 0, WS_FLAG2 + b * 128);
    unsigned foff[4];
    const unsigned fstr = isgu ? 32 * F_ * 2 : 32768;
#pragma unroll
    for (int j = 0; j < 4; ++j)
      foff[j] = (isgu ? (unsigned)((lane >> 1) * (F_ * 2) + b * 32) : (unsigned)((lane >> 1) * 1024 + (b - DN0) * 32)) + (lane & 1) * 16 + j * (fstr - 1024);
    auto ffn = [&](int q0, int q1) {
      if (isgu) {
#pragma unroll 1
        for (int p = q0; p < min(q1, 8); ++p) glds4((const char*)(p < 4 ? wg : wu) + (p & 3) * (128 * F_ * 2), foff, lds0 + L_F + p * 4096);
      } else if (isdn) {
#pragma unroll 1
        for (int p = q0; p < min(q1, 10); ++p) glds4((const char*)wd + p * 131072, foff, lds0 + L_F + p * 4096);
        if (q1 > 10) {
#pragma unroll 1
          for (int p = 40; p < 43; ++p) glds16((const char*)wd + p * 32768, foff[0], lds0 + L_F40 + (p - 40) * 2048);
        }
      }
    };
    ffn(0, 11);
    unsigned wooff[4];
#pragma unroll
    for (int j = 0; j < 4; ++j) wooff[j] = (unsigned)((lane >> 3) * 1024 + cb * 128 + (((lane & 7) ^ (((lane >> 4) & 1) << 2)) * 16) + j * 7168);
    BAR();
    WAIT_VM(24);
#pragma unroll 1
    for (int p = 0; p < 8; ++p) glds4((const char*)wo + p * 32768, wooff, lds0 + L_WO + p * 4096);
    WAIT_VM(24);
#pragma unroll 1
    for (int p = 8; p < 16; ++p) glds4((const char*)wo + p * 32768, wooff, lds0 + L_WO + p * 4096);
#pragma unroll 1
    for (unsigned spins = 0; spins < SPIN_CAP; ++spins) {
      FENCE();
      bool ok = true;
#pragma unroll
      for (int e = 0; e < 4; ++e) { const u32x2 f = LD64(lane * 128, WS_FLAG2 + e * 8192); ok &= (f[0] == tag[0]) & (f[1] == tag[1]); }
      if (__all(ok)) break;
      __builtin_amdgcn_s_sleep(16);
    }
    if (b == 0 && lane == 0) ST64(tag, 0, WS_EPOCH);
    WAIT_VM(0);
    STAMP(2);
    BAR();
    BAR(); BAR();
    u32x4 pv[8];
    {
      const u32x4 own = nosent4(ldsr128(lds + L_ST + l16));
      ST128(own, l16, WS_XS + (pm * 8 + cb) * 1024);
      STAMP(6);
      __builtin_amdgcn_s_sleep(XSDELAY);
#pragma unroll
      for (int e = 0; e < 8; ++e) pv[e] = LD128(l16, WS_XS + (pm * 8 + e) * 1024);
#pragma unroll 1
      for (unsigned spins = 0; spins < SPIN_CAP; ++spins) {
        bool miss[8], any = false;
#pragma unroll
        for (int e = 0; e < 8; ++e) { miss[e] = __any(umax4(pv[e]) == SENT); any |= miss[e]; }
        if (!any) break;
        FENCE();
#pragma unroll
        for (int e = 0; e < 8; ++e) if (miss[e]) pv[e] = LD128(l16, WS_XS + (pm * 8 + e) * 1024);
      }
    }
    STAMP(3);
#define SUM8(c) (((__uint_as_float(pv[0][c]) + __uint_as_float(pv[1][c])) + (__uint_as_float(pv[2][c]) + __uint_as_float(pv[3][c]))) + \
                 ((__uint_as_float(pv[4][c]) + __uint_as_float(pv[5][c])) + (__uint_as_float(pv[6][c]) + __uint_as_float(pv[7][c]))))
    const float mean = SUM8(0) * (1.f / D_);
    const float rstd = __builtin_amdgcn_rsqf(__builtin_fmaxf(SUM8(1) * (1.f / D_) - mean * mean, 0.f) + LN_EPS);
    if (cb == 0) {
      const unsigned u = __float_as_uint(rstd * (SUM8(2) - mean * sw));
      const unsigned ordv = (u & 0x80000000u) ? ~u : (u | 0x80000000u);
      __builtin_amdgcn_raw_buffer_store_b32(max((ordv >> 1) & 0x7FFFF800u, 0x800u) | (unsigned)(2047 - (pm * 64 + lane)), r_ws, lane * 4, WS_KEY + pm * 256, SC1);
    }
    *(LAS u32x2*)(lds + L_ST + 1024 + lane * 8) = u32x2{__float_as_uint(rstd), __float_as_uint(mean * rstd)};
    STAMP(4);
    LDS_BAR();
    STAMP_FLUSH();
    return;
  }

  if (wid >= 4) {
    const int wrd = 16384 + lw * 3072 + (8 * h + ((lane >> 2) & 3)) * 192 + 32 * ((lane >> 4) & 1) + 8 * (lane & 3);
    float cs[3] = {0.f, 0.f, 0.f};
#pragma unroll 1
    for (int c = 0; c < 8; ++c) {
      if (c < 7) WAIT_BAR(7); else WAIT_BAR(0);
      const LAS char* sb = lds + (c & 3) * K1_SLOT + wrd;
      u32x4 wf[3];
#pragma unroll
      for (int j = 0; j < 3; ++j) wf[j] = ldstr2(sb + j * 64, 768);
      if (c < 6) stage(c + 2);
#pragma unroll
      for (int e = 0; e < 4; ++e)
#pragma unroll
        for (int j = 0; j < 3; ++j) cs[j] = dot2bf(wf[j][e], ONE2, cs[j]);
    }
#pragma unroll
    for (int j = 0; j < 3; ++j) *(LAS float*)(lds + K1_CSP + (lw * 2 + h) * 384 + (32 * j + t) * 4) = cs[j];
    LDS_BAR();
    if (lw == 0 && lane < 24) {
      const LAS char* pp = lds + K1_CSP + l16;
      f32x4 sacc = *(const LAS f32x4*)pp;
#pragma unroll
      for (int e = 1; e < 8; ++e) sacc += *(const LAS f32x4*)(pp + e * 384);
      *(LAS f32x4*)(lds + K1_CS + l16) = sacc;
    }
    LDS_BAR();
    BAR();
  } else {
    if (cb1 < 10) qkv_body<0>(r_ws, lds, rb1, cb1, wid STAMP_PASS);
    else if (cb1 == 10) qkv_body<1>(r_ws, lds, rb1, cb1, wid STAMP_PASS);
    else qkv_body<3>(r_ws, lds, rb1, cb1, wid STAMP_PASS);
    WAIT_BAR(0);
    STAMP(4);
  }

  const bool onel = (lane & 16) != 0;
  unsigned kaddr = lds0 + L_KV + t * 32 + h * 16;
  unsigned vaddr = lds0 + (onel ? L_ONE + ((lane & 11) ? 16 : 0) : L_KV + 1024 + h * 256 + (lane & 15) * 16);
  const int vstep = onel ? 0 : 8192;
  const int IDX = 0xDDDDDDDD;
  const int row = pm * 64 + wid * 8 + (lane >> 3), col = cb * 64 + (lane & 7) * 8;
  f32x16 negm, sx, sy = f32x16{}, o = f32x16{};
  u32x4 vpb = u32x4{0, 0, 0, 0};
  asm volatile("" : "+{v[0:15]}"(o), "+{v[48:63]}"(sy), "+{v[84:87]}"(vpb));

  BAR();
  STAMP(5);
  const u32x4 xres = *(const u32x4*)(x + row * D_ + col), wrv = *(const u32x4*)(wr + col);
  const int mh = wid >> 2, kq = wid & 3;
  const u32x4 qf = ldsr128(lds + L_Q + kq * 2048 + mh * 1024 + t * 32 + h * 16);
  int ostoff = t * 32 + h * 16 + WS_OF + (qb * 4 + kq) * 65536 + head * 2048 + mh * 1024;
  asm volatile("" : "+v"(ostoff));
  u32x4 kn, va, vb;
  {
    const u32x4 k0 = ldsr128((const LAS char*)(uintptr_t)kaddr);
    kn = ldsr128((const LAS char*)(uintptr_t)kaddr + 2048);
    va = ldsr128((const LAS char*)(uintptr_t)vaddr); vb = ldsr128((const LAS char*)(uintptr_t)vaddr + 512);
    const float m = -rowmax16(mfma32(k0, qf, f32x16{}));
#pragma unroll
    for (int r = 0; r < 16; ++r) negm[r] = m;
    sx = mfma32(k0, qf, negm);
  }
  STAMP(6);
  {
    unsigned cnt;
    asm volatile(
        "s_mov_b32 %[cnt], 0\n\t"
        "s_nop 7\n\t"
        "s_cmp_gt_u32 %[wid], 3\n\t"
        "s_cbranch_scc1 3f\n\t"
        "s_setprio 1\n\t"
        ATTN_LOOP("1", "2", "", "", "")
        "s_setprio 0\n\t"
        "s_branch 5f\n\t"
        ATTN_LOOP("3", "4", "s_setprio 2\n\t", "", "s_setprio 0\n\t")
        "5:\n\t"
        "s_nop 1\n\t"
        SMF(84, 48, 8)
        "s_waitcnt lgkmcnt(0)\n\t"
        "s_nop 11\n\t"
        : "+{v[0:15]}"(o), "+{v[32:47]}"(sx), "+{v[48:63]}"(sy), "+{v[68:71]}"(kn), "+{v[72:75]}"(va), "+{v[76:79]}"(vb),
          "+{v[84:87]}"(vpb), "+{v89}"(kaddr), "+{v90}"(vaddr), [cnt] "=&s"(cnt)
        : "{v[16:31]}"(negm), "{v[64:67]}"(qf), "{v88}"(IDX), "{v91}"(vstep), [wid] "s"(wid)
        : "v80", "v81", "v82", "v83", "scc", "memory");
  }
  STAMP(7);
  auto normalise = [&]() {
    const float inv = __builtin_amdgcn_rcpf(o[8]);
    return nosent4(u32x4{pkbf(o[0] * inv, o[1] * inv), pkbf(o[2] * inv, o[3] * inv), pkbf(o[4] * inv, o[5] * inv), pkbf(o[6] * inv, o[7] * inv)});
  };
  u32x4 opk = normalise();
  asm volatile("" : "+v"(opk));
  {
    float c0, c1, c2, c3;
    asm("v_add_f32 %0, |%1|, |%2|" : "=v"(c0) : "v"(o[0]), "v"(o[1])); asm("v_add_f32 %0, |%1|, |%2|" : "=v"(c1) : "v"(o[2]), "v"(o[3]));
    asm("v_add_f32 %0, |%1|, |%2|" : "=v"(c2) : "v"(o[4]), "v"(o[5])); asm("v_add_f32 %0, |%1|, |%2|" : "=v"(c3) : "v"(o[6]), "v"(o[7]));
    const float chk = (c0 + c1) + (c2 + c3);
    const bool bad = !(o[8] < 1.8446744e19f) || !(chk < 3.0e38f);
    if (__builtin_expect(__any(bad), 0)) {
      float m = -INFINITY;
      o = f32x16{};
      const int kqo = head * 131072 + t * 32 + h * 16, vq = head * 131072 + 1024 + h * 256 + (lane & 15) * 16;
      const u32x4 ones = u32x4{ONE2, ONE2, ONE2, ONE2};
#pragma unroll 1
      for (int i = 0; i < 64; ++i) {
        f32x16 s = mfma32(LD128(kqo, WS_KV + i * 2048), qf, f32x16{});
        u32x4 v0 = LD128(vq, WS_KV + i * 2048), v1 = LD128(vq + 512, WS_KV + i * 2048);
        if (onel) { v0 = ones; v1 = ones; }
        const float mn = __builtin_fmaxf(m, rowmax16(s));
        const float al = __builtin_amdgcn_exp2f(m - mn);
        m = mn;
#pragma unroll
        for (int r = 0; r < 16; ++r) { o[r] *= al; s[r] = __builtin_amdgcn_exp2f(s[r] - mn); }
        o = smfmac32(v0, lo8(s), o, IDX);
        o = smfmac32(v1, hi8(s), o, IDX);
      }
      opk = normalise();
    }
  }
  __builtin_amdgcn_raw_buffer_store_b128(opk, r_ws, ostoff, 0, SC1);
  STAMP(8);

  const int g = lane >> 4, q = (lane >> 2) & 3, p4 = lane & 3;
  f32x4 a0 = f32x4{bflo(xres[0]), bfhi(xres[0]), bflo(xres[1]), bfhi(xres[1])}, a1 = f32x4{bflo(xres[2]), bfhi(xres[2]), bflo(xres[3]), bfhi(xres[3])};
  unsigned ra0, ra1;
  {
    const int lr = wid * 8 + (lane >> 3);
    const int sl = ((lane & 7) ^ ((lr & 7) | ((lr & 1) << 3))) * 16;
    const unsigned rbase = lds0 + (mh ? L_WO : L_PA) + (lr & 31) * 256;
    ra0 = rbase + sl; ra1 = rbase + (sl ^ 128);
  }
  LAS unsigned* cand = (LAS unsigned*)(lds + L_SEL);
  {
    u32x4 of[8], wf0[8], wf1[8];
    const int flip = ((q >> 1) & 1) << 6;
    const LAS char* wb = lds + L_WO + kq * 16384 + (8 * h + q) * 128 + 32 * (g & 1) + 8 * p4;
#pragma unroll
    for (int e = 0; e < 8; ++e) { wf0[e] = ldstr2(wb + flip + e * 2048, 512); wf1[e] = ldstr2(wb + (64 ^ flip) + e * 2048, 512); }
    const int ooff = t * 32 + h * 16, osoff = WS_OF + pm * 65536 + kq * 16384 + mh * 1024;
    unsigned pa[8];
    {
      const unsigned rb = lds0 + (mh ? L_WO + kq * 16384 : L_PA + kq * 8192) + t * 256;
      const int swz = (t & 7) | ((t & 1) << 3);
#pragma unroll
      for (int gq = 0; gq < 8; ++gq) { pa[gq] = rb + (((h * 8 + gq) ^ swz) * 16); asm volatile("" : "+v"(pa[gq])); }
    }
    asm volatile("" : "+v"(a0), "+v"(a1), "+v"(ra0), "+v"(ra1));
    __builtin_amdgcn_s_waitcnt(0xC07F);
    FENCE();
    if (!mh) *(volatile LAS unsigned*)(lds + L_WF + kq * 4) = 1u;
    else {
#pragma unroll 1
      for (unsigned spins = 0; spins < (1u << 24); ++spins) {
        if (__builtin_amdgcn_readfirstlane(*(volatile LAS unsigned*)(lds + L_WF + kq * 4))) break;
        __builtin_amdgcn_s_sleep(1);
      }
    }
    FENCE();
    if (LATEO > 1) __builtin_amdgcn_s_sleep(LATEO);
#pragma unroll
    for (int e = 0; e < 8; ++e) of[e] = LD128(ooff, osoff + e * 2048);
    f32x16 acc0 = f32x16{}, acc1 = f32x16{};
#pragma unroll 1
    for (unsigned spins = 0; spins < SPIN_CAP; ++spins) {
      bool miss[8], any = false;
#pragma unroll
      for (int e = 0; e < 8; ++e) { miss[e] = __any(umax4(of[e]) == SENT); any |= miss[e]; }
      if (!any) break;
      FENCE();
#pragma unroll
      for (int e = 0; e < 8; ++e) if (miss[e]) of[e] = LD128(ooff, osoff + e * 2048);
    }
#pragma unroll
    for (int e = 0; e < 8; ++e) { acc0 = mfma32(wf0[e], of[e], acc0); acc1 = mfma32(wf1[e], of[e], acc1); }
    STAMP(9);
#pragma unroll
    for (int gq = 0; gq < 4; ++gq) {
      *(LAS f32x4*)(uintptr_t)pa[gq] = f32x4{acc0[4 * gq], acc0[4 * gq + 1], acc0[4 * gq + 2], acc0[4 * gq + 3]};
      *(LAS f32x4*)(uintptr_t)pa[4 + gq] = f32x4{acc1[4 * gq], acc1[4 * gq + 1], acc1[4 * gq + 2], acc1[4 * gq + 3]};
    }
  }
  LDS_BAR();
  STAMP(10);
  float hv[8];
  {
    const LAS char* r0 = (const LAS char*)(uintptr_t)ra0;
    const LAS char* r1 = (const LAS char*)(uintptr_t)ra1;
    f32x4 t0[4], t1[4];
    if (mh) {
#pragma unroll
      for (int e = 0; e < 4; ++e) { t0[e] = *(const LAS f32x4*)(r0 + e * 16384); t1[e] = *(const LAS f32x4*)(r1 + e * 16384); }
    } else {
#pragma unroll
      for (int e = 0; e < 4; ++e) { t0[e] = *(const LAS f32x4*)(r0 + e * 8192); t1[e] = *(const LAS f32x4*)(r1 + e * 8192); }
    }
    __builtin_amdgcn_sched_barrier(0);
    a0 += (t0[0] + t0[1]) + (t0[2] + t0[3]);
    a1 += (t1[0] + t1[1]) + (t1[2] + t1[3]);
    const u32x4 hb = u32x4{pkbf(a0[0], a0[1]), pkbf(a0[2], a0[3]), pkbf(a1[0], a1[1]), pkbf(a1[2], a1[3])};
    float s1 = 0.f, s2 = 0.f, sd = 0.f;
#pragma unroll
    for (int e = 0; e < 4; ++e) { s1 = dot2bf(hb[e], ONE2, s1); s2 = dot2bf(hb[e], hb[e], s2); sd = dot2bf(hb[e], wrv[e], sd); }
    s1 = sum8(s1); s2 = sum8(s2); sd = sum8(sd);
    if ((lane & 7) == 0) *(LAS u32x4*)(lds + L_ST + wid * 128 + lane * 2) = u32x4{__float_as_uint(s1), __float_as_uint(s2), __float_as_uint(sd), 0u};
    LDS_BAR();
    if (tid < 80) cand[tid] = 0;
#pragma unroll
    for (int e = 0; e < 4; ++e) { hv[2 * e] = bflo(hb[e]); hv[2 * e + 1] = bfhi(hb[e]); }
  }
  STAMP(11);
  const int trd = (8 * g + q) * 32 + 8 * p4;
  u32x4 fw[8];
  if (isgu || isdn) {
#pragma unroll
    for (int p = 0; p < 8; ++p) {
      const int po = isgu ? L_F + ((p >> 2) * 16 + (wid & 3) * 4 + (p & 3)) * 1024 : p < 5 ? L_F + (p * 8 + wid) * 1024 : L_F40 + min(wid, 2) * 2048;
      fw[p] = ldstr2(lds + po + trd, 128);
    }
  }
  u32x4 k4;
  LDS_BAR();
  {
    STAMP(12);
    const u32x2 st = *(const LAS u32x2*)(lds + L_ST + 1024 + wid * 64 + (lane >> 3) * 8);
    const float rstd = __uint_as_float(st[0]), mr = __uint_as_float(st[1]);
    u32x4 hp, op;
#pragma unroll
    for (int e = 0; e < 4; ++e) {
      hp[e] = pkbf(hv[2 * e] * rstd - mr, hv[2 * e + 1] * rstd - mr);
      op[e] = pkbf(hv[2 * e] + bflo(hp[e]), hv[2 * e + 1] + bfhi(hp[e]));
    }
    ST128(nosent4(hp), row * 1024 + col * 2, WS_HN);
    __builtin_amdgcn_raw_buffer_store_b128(nosent4(op), r_out, row * 1024 + col * 2, 0, SC1);
  }
  STAMP(13);
  if (!isgu && !isdn) { STAMP_FLUSH(); return; }
  if (KEYDELAY) __builtin_amdgcn_s_sleep(KEYDELAY);
#pragma unroll 1
  for (unsigned spins = 0; spins < SPIN_CAP; ++spins) {
    FENCE();
    k4 = LD128(tid * 16, WS_KEY);
    if (__all(umax4(k4) != SENT)) break;
  }
  STAMP(14);
  {
    unsigned m = umax4(k4);
    m = max(m, dppu<0xB1>(m)); m = max(m, dppu<0x4E>(m)); m = max(m, dppu<0x141>(m)); m = max(m, dppu<0x140>(m));
    m = max(m, dppu<0x142>(m)); m = max(m, dppu<0x143>(m));
    if (lane == 63) cand[64 + wid] = m;
  }
  LDS_BAR();
  {
    const u32x4 g0 = *(LAS u32x4*)(cand + 64), g1 = *(LAS u32x4*)(cand + 68);
    const unsigned T = min(min(min(g0[0], g0[1]), min(g0[2], g0[3])), min(min(g1[0], g1[1]), min(g1[2], g1[3])));
    const unsigned c0 = k4[0] >= T, c1 = k4[1] >= T, c2 = k4[2] >= T, c3 = k4[3] >= T;
    const unsigned cnt = (c0 + c1) + (c2 + c3);
    if (cnt) {
      unsigned pos;
      asm volatile("ds_add_rtn_u32 %0, %1, %2\n\ts_waitcnt lgkmcnt(0)" : "=v"(pos) : "v"((unsigned)(lds0 + L_SEL + 72 * 4)), "v"(cnt) : "memory");
      cand[c0 && pos < 64 ? pos : 73] = k4[0]; pos += c0;
      cand[c1 && pos < 64 ? pos : 73] = k4[1]; pos += c1;
      cand[c2 && pos < 64 ? pos : 73] = k4[2]; pos += c2;
      cand[c3 && pos < 64 ? pos : 73] = k4[3];
    }
  }
  LDS_BAR();
  LAS unsigned* sel = cand + 80 + wid * 8;
  {
    unsigned nv = cand[72];
    unsigned c = cand[t];
    u32x4 o4[4];
#pragma unroll
    for (int e = 0; e < 4; ++e) o4[e] = *(LAS u32x4*)(cand + h * 16 + e * 4);
    asm volatile("" : "+v"(nv), "+v"(c), "+v"(o4[0]), "+v"(o4[1]), "+v"(o4[2]), "+v"(o4[3]));
    const unsigned n = __builtin_amdgcn_readfirstlane(nv);
    if (n <= 64 && wid >= (isgu ? 4 : 1)) {
      if (isgu) { STAMP_FLUSH(); return; }
    } else if (__builtin_expect(n <= 32, 1)) {
      unsigned ba = 0, bb = 0;
#pragma unroll
      for (int e = 0; e < 4; ++e) {
        ba = __builtin_amdgcn_alignbit(ba, c - o4[e][0], 31); bb = __builtin_amdgcn_alignbit(bb, c - o4[e][1], 31);
        ba = __builtin_amdgcn_alignbit(ba, c - o4[e][2], 31); bb = __builtin_amdgcn_alignbit(bb, c - o4[e][3], 31);
      }
      unsigned rk = __builtin_popcount(ba) + __builtin_popcount(bb);
      auto r2 = __builtin_amdgcn_permlane32_swap(rk, rk, false, false);
      rk = r2[0] + r2[1];
      if (rk < 8) sel[rk] = c;
    } else if (n <= 64) {
      const unsigned c2 = cand[32 + t];
      unsigned ba = 0, bb = 0;
#pragma unroll
      for (int e = 0; e < 8; ++e) {
        const u32x4 v = *(LAS u32x4*)(cand + h * 32 + e * 4);
#pragma unroll
        for (int j = 0; j < 4; ++j) { ba = __builtin_amdgcn_alignbit(ba, c - v[j], 31); bb = __builtin_amdgcn_alignbit(bb, c2 - v[j], 31); }
      }
      unsigned ra = __builtin_popcount(ba), rb2 = __builtin_popcount(bb);
      auto r1 = __builtin_amdgcn_permlane32_swap(ra, ra, false, false);
      auto r2 = __builtin_amdgcn_permlane32_swap(rb2, rb2, false, false);
      ra = r1[0] + r1[1]; rb2 = r2[0] + r2[1];
      if (ra < 8) sel[ra] = c;
      if (rb2 < 8) sel[rb2] = c2;
    } else {
      LAS unsigned* all = cand + 256;
      *(LAS u32x4*)(all + tid * 4) = k4;
      LDS_BAR();
      unsigned rk[4] = {0, 0, 0, 0};
#pragma unroll 1
      for (int j = 0; j < 2048; ++j) {
        const unsigned v = all[j];
#pragma unroll
        for (int e = 0; e < 4; ++e) rk[e] += v > k4[e];
      }
#pragma unroll
      for (int e = 0; e < 4; ++e) if (rk[e] < 8) cand[160 + rk[e]] = k4[e];
      LDS_BAR();
      sel = cand + 160;
      if (isgu && wid >= 4) { STAMP_FLUSH(); return; }
    }
  }
  const int tok = lane & 15;
  const unsigned key = sel[tok & 7];
  const int trow = 2047 - (int)(key & 2047u);
  STAMP(15);
  LAS f32x4* red = (LAS f32x4*)(lds + L_WO);
  if (isgu) {
    u32x4 xb[4];
#pragma unroll 1
    for (unsigned spins = 0; spins < SPIN_CAP; ++spins) {
      FENCE();
#pragma unroll
      for (int u = 0; u < 4; ++u) xb[u] = LD128(trow * 1024 + u * 64 + g * 16, WS_HN + wid * 256);
      if (__all(max(max(umax4(xb[0]), umax4(xb[1])), max(umax4(xb[2]), umax4(xb[3]))) != SENT)) break;
    }
    STAMP(16);
    f32x4 ag = f32x4{}, au = f32x4{};
#pragma unroll
    for (int u = 0; u < 4; ++u) { ag = mfma16(fw[u], xb[u], ag); au = mfma16(fw[4 + u], xb[u], au); }
    const f32x4 mine = tok < 8 ? ag : au;
    if (wid != 0) red[wid * 64 + lane] = mine;
    LDS_BAR();
    if (wid == 0) {
      const f32x4 s = (mine + red[64 + lane]) + (red[128 + lane] + red[192 + lane]);
      float av[4];
#pragma unroll
      for (int r = 0; r < 4; ++r) {
        const float gu = s[r] * dppf<0x128>(s[r]);
        av[r] = gu * __builtin_amdgcn_rcpf(1.f + __builtin_amdgcn_exp2f(s[r] * -1.4426950408889634f));
      }
      if (tok < 8) ST64((u32x2{nosent(pkbf(av[0], av[1])), nosent(pkbf(av[2], av[3]))}), tok * 32 + g * 8, WS_ACT + b * 256);
    }
    STAMP(17);
    STAMP_FLUSH();
    return;
  }
  const int nb = b - DN0;
  u32x2 o0 = u32x2{0, 0};
  float sg = 0.f;
  if (wid == 0) {
#pragma unroll 1
    for (unsigned spins = 0; spins < SPIN_CAP; ++spins) {
      FENCE();
      o0 = __builtin_amdgcn_raw_buffer_load_b64(r_out, trow * 1024 + g * 8, nb * 32, SC1);
      if (__all(max(o0[0], o0[1]) != SENT)) break;
    }
    const unsigned ov = ((key & 0x7FFFF800u) << 1) | 0x800u;
    const float r = rbf(__uint_as_float((ov & 0x80000000u) ? (ov & 0x7FFFFFFFu) : ~ov));
    sg = rbf(__builtin_amdgcn_rcpf(1.f + __expf(-r)));
  }
  const __amdgpu_buffer_rsrc_t r_act = mkrsrc(ws + WS_ACT, 22016);
  u32x4 ab[6];
  const int aoff = wid * 512 + (g >> 1) * 256 + (tok & 7) * 32 + (g & 1) * 16;
  if (DNDELAY) { if (wid == 0) __builtin_amdgcn_s_sleep(DNDELAY > 24 ? DNDELAY - 24 : 0); else __builtin_amdgcn_s_sleep(DNDELAY); }
  FENCE();
#pragma unroll
  for (int p = 0; p < 6; ++p) ab[p] = __builtin_amdgcn_raw_buffer_load_b128(r_act, aoff, p * 4096, SC1);
#pragma unroll 1
  for (unsigned spins = 0; spins < SPIN_CAP; ++spins) {
    bool miss[6], any = false;
#pragma unroll
    for (int p = 0; p < 6; ++p) { miss[p] = __any(umax4(ab[p]) == SENT); any |= miss[p]; }
    if (!any) break;
    FENCE();
#pragma unroll
    for (int p = 0; p < 6; ++p) if (miss[p]) ab[p] = __builtin_amdgcn_raw_buffer_load_b128(r_act, aoff, p * 4096, SC1);
  }
  STAMP(16);
  f32x4 ay = f32x4{};
#pragma unroll
  for (int p = 0; p < 6; ++p) ay = mfma16(fw[p], ab[p], ay);
  red[wid * 64 + lane] = ay;
  LDS_BAR();
  if (wid == 0 && tok < 8) {
    ay = ((ay + red[64 + lane]) + (red[128 + lane] + red[192 + lane])) + ((red[256 + lane] + red[320 + lane]) + (red[384 + lane] + red[448 + lane]));
    __builtin_amdgcn_raw_buffer_store_b64(u32x2{pkbf(fmaf(ay[0], sg, bflo(o0[0])), fmaf(ay[1], sg, bfhi(o0[0]))), pkbf(fmaf(ay[2], sg, bflo(o0[1])), fmaf(ay[3], sg, bfhi(o0[1])))}, r_out,
                                          trow * 1024 + g * 8, nb * 32, SC1);
  }
  STAMP(17);
}

extern "C" void kernel_launch(void* const* d_in, const int* in_sizes, int n_in, void* d_out, int out_size, void* d_ws, size_t ws_size,
                              hipStream_t stream) {
  (void)hipFuncSetAttribute((const void*)fused_block, hipFuncAttributeMaxDynamicSharedMemorySize, LDS_BYTES);
  fused_block<<<256, 576, LDS_BYTES, stream>>>((const u16*)d_in[0], (const u16*)d_in[1], (const u16*)d_in[2], (const u16*)d_in[4],
                                               (const u16*)d_in[5], (const u16*)d_in[6], (const u16*)d_in[7], (char*)d_ws, (u16*)d_out);
}
```

```cpp
#include <hip/hip_runtime.h>
#include <hip/hip_bf16.h>


typedef unsigned short u16;
typedef __bf16 bf16x2_t __attribute__((ext_vector_type(2)));
typedef __bf16 bf16x8_t __attribute__((ext_vector_type(8)));
typedef __bf16 bf16x16_t __attribute__((ext_vector_type(16)));
typedef short s16x4 __attribute__((ext_vector_type(4)));
typedef float f32x4 __attribute__((ext_vector_type(4)));
typedef float f32x8 __attribute__((ext_vector_type(8)));
typedef float f32x16 __attribute__((ext_vector_type(16)));
typedef unsigned u32x2 __attribute__((ext_vector_type(2)));
typedef unsigned u32x4 __attribute__((ext_vector_type(4)));
#define LAS __attribute__((address_space(3)))

constexpr int D_ = 512, F_ = 1376;
constexpr unsigned SENT = 0xFFFFFFFFu;
constexpr float LN_EPS = 1e-5f;
constexpr float QSCALE = 0.25f * 1.4426950408889634f;
constexpr unsigned SPIN_CAP = 1u << 16;
constexpr int SC1 = 16;
constexpr unsigned ONE2 = 0x3F803F80u;
constexpr int CODE_KB = 25;

constexpr unsigned WS_KV = 0;
constexpr unsigned WS_QF = 4u << 20;
constexpr unsigned WS_OF = 6u << 20;
constexpr unsigned WS_HN = 8u << 20;
constexpr unsigned WS_XS = 10u << 20;
constexpr unsigned WS_ACT = WS_XS + (256u << 10);
constexpr unsigned WS_KEY = WS_ACT + 22016;
constexpr unsigned WS_FLAG = WS_XS + (320u << 10);
constexpr unsigned WS_FLAG2 = WS_FLAG + 32768;
constexpr unsigned WS_EPOCH = WS_FLAG2 + 32768;
constexpr unsigned WS_BYTES = WS_EPOCH + 128;

__device__ __forceinline__ f32x16 mfma32(u32x4 a, u32x4 b, f32x16 c) {
  return __builtin_amdgcn_mfma_f32_32x32x16_bf16(__builtin_bit_cast(bf16x8_t, a), __builtin_bit_cast(bf16x8_t, b), c, 0, 0, 0);
}
__device__ __forceinline__ f32x4 mfma16(u32x4 a, u32x4 b, f32x4 c) {
  return __builtin_amdgcn_mfma_f32_16x16x32_bf16(__builtin_bit_cast(bf16x8_t, a), __builtin_bit_cast(bf16x8_t, b), c, 0, 0, 0);
}
__device__ __forceinline__ f32x16 smfmac32(u32x4 a, f32x8 b, f32x16 c, int idx) {
  return __builtin_amdgcn_smfmac_f32_32x32x32_bf16(__builtin_bit_cast(bf16x8_t, a), __builtin_bit_cast(bf16x16_t, b), c, idx, 0, 0);
}
__device__ __forceinline__ float dot2bf(unsigned a, unsigned b, float c) {
  return __builtin_amdgcn_fdot2_f32_bf16(__builtin_bit_cast(bf16x2_t, a), __builtin_bit_cast(bf16x2_t, b), c, false);
}
__device__ __forceinline__ float swap32_sum(float v) {
  auto r = __builtin_amdgcn_permlane32_swap(__float_as_uint(v), __float_as_uint(v), false, false);
  return __uint_as_float(r[0]) + __uint_as_float(r[1]);
}
__device__ __forceinline__ float swap32_max(float v) {
  auto r = __builtin_amdgcn_permlane32_swap(__float_as_uint(v), __float_as_uint(v), false, false);
  return __builtin_fmaxf(__uint_as_float(r[0]), __uint_as_float(r[1]));
}
__device__ __forceinline__ unsigned pkbf(float lo, float hi) {
  bf16x2_t v; v[0] = (__bf16)lo; v[1] = (__bf16)hi; return __builtin_bit_cast(unsigned, v);
}
__device__ __forceinline__ float bflo(unsigned u) { return __uint_as_float(u << 16); }
__device__ __forceinline__ float bfhi(unsigned u) { return __uint_as_float(u & 0xFFFF0000u); }
__device__ __forceinline__ float rbf(float f) { return (float)(__bf16)f; }
__device__ __forceinline__ void glds16(const void* sbase, unsigned voff, unsigned lds_base) {
  unsigned sv;
  asm volatile("s_mov_b32 %0, m0\n\ts_mov_b32 m0, %3\n\ts_nop 0\n\tglobal_load_lds_dwordx4 %1, %2\n\ts_mov_b32 m0, %0"
               : "=&s"(sv) : "v"(voff), "s"(sbase), "s"(lds_base) : "memory");
}
__device__ __forceinline__ void glds16c(const void* sbase, unsigned voff, unsigned lds_base) {
  unsigned sv;
  asm volatile("s_mov_b32 %0, m0\n\ts_mov_b32 m0, %3\n\ts_nop 0\n\tglobal_load_lds_dwordx4 %1, %2 sc1\n\ts_mov_b32 m0, %0"
               : "=&s"(sv) : "v"(voff), "s"(sbase), "s"(lds_base) : "memory");
}
__device__ __forceinline__ void glds8c(const void* sbase, unsigned voff, unsigned lds_base) {
  unsigned sv;
  asm volatile("s_mov_b32 %0, m0\n\ts_mov_b32 m0, %3\n\ts_nop 0\n\t"
               "global_load_lds_dwordx4 %1, %2 offset:-4096 sc1\n\tglobal_load_lds_dwordx4 %1, %2 offset:-3072 sc1\n\t"
               "global_load_lds_dwordx4 %1, %2 offset:-2048 sc1\n\tglobal_load_lds_dwordx4 %1, %2 offset:-1024 sc1\n\t"
               "global_load_lds_dwordx4 %1, %2 sc1\n\tglobal_load_lds_dwordx4 %1, %2 offset:1024 sc1\n\t"
               "global_load_lds_dwordx4 %1, %2 offset:2048 sc1\n\tglobal_load_lds_dwordx4 %1, %2 offset:3072 sc1\n\t"
               "s_mov_b32 m0, %0"
               : "=&s"(sv) : "v"(voff), "s"((const char*)sbase + 4096), "s"(lds_base + 4096) : "memory");
}
__device__ __forceinline__ void glds4(const void* sbase, const unsigned (&voff)[4], unsigned lds_base) {
  unsigned sv;
  asm volatile("s_mov_b32 %0, m0\n\ts_mov_b32 m0, %6\n\ts_nop 0\n\t"
               "global_load_lds_dwordx4 %1, %5\n\tglobal_load_lds_dwordx4 %2, %5 offset:1024\n\t"
               "global_load_lds_dwordx4 %3, %5 offset:2048\n\tglobal_load_lds_dwordx4 %4, %5 offset:3072\n\t"
               "s_mov_b32 m0, %0"
               : "=&s"(sv) : "v"(voff[0]), "v"(voff[1]), "v"(voff[2]), "v"(voff[3]), "s"(sbase), "s"(lds_base) : "memory");
}
#define FENCE() asm volatile("" ::: "memory")
#define WAIT_BAR(N) asm volatile("s_waitcnt vmcnt(" #N ") lgkmcnt(0)\n\ts_barrier" ::: "memory")
#define VM_BAR(N) asm volatile("s_waitcnt vmcnt(" #N ")\n\ts_barrier" ::: "memory")
#define LDS_BAR() do { FENCE(); __builtin_amdgcn_s_waitcnt(0xC07F); __builtin_amdgcn_s_barrier(); FENCE(); } while (0)
#define BAR() do { FENCE(); __builtin_amdgcn_s_barrier(); FENCE(); } while (0)
#define WAIT_VM(N) asm volatile("s_waitcnt vmcnt(" #N ")" ::: "memory")
#define LD128(off, soff) __builtin_amdgcn_raw_buffer_load_b128(r_ws, off, soff, SC1)
#define LD64(off, soff) __builtin_amdgcn_raw_buffer_load_b64(r_ws, off, soff, SC1)
#define ST128(v, off, soff) __builtin_amdgcn_raw_buffer_store_b128(v, r_ws, (off) + (soff), 0, SC1)
#define ST64(v, off, soff) __builtin_amdgcn_raw_buffer_store_b64(v, r_ws, off, soff, SC1)
__device__ __forceinline__ u32x4 ldsr128(const LAS char* p) { return *(const LAS u32x4*)p; }
__device__ __forceinline__ u32x2 ldstr(const LAS char* p) {
  return __builtin_bit_cast(u32x2, __builtin_amdgcn_ds_read_tr16_b64_v4i16((LAS s16x4*)p));
}
__device__ __forceinline__ u32x4 ldstr2(const LAS char* p, int off2) { const u32x2 a = ldstr(p), b = ldstr(p + off2); return u32x4{a[0], a[1], b[0], b[1]}; }
__device__ __forceinline__ int swapb23(int v) { return (v & ~12) | ((v & 4) << 1) | ((v & 8) >> 1); }
__device__ __forceinline__ unsigned nosent(unsigned v) { return min(v, 0xFFFFFFFEu); }
__device__ __forceinline__ u32x4 nosent4(u32x4 v) { return u32x4{nosent(v[0]), nosent(v[1]), nosent(v[2]), nosent(v[3])}; }
__device__ __forceinline__ unsigned umax4(u32x4 v) { return max(max(v[0], v[1]), max(v[2], v[3])); }
__device__ __forceinline__ __amdgpu_buffer_rsrc_t mkrsrc(const void* p, unsigned bytes) {
  return __builtin_amdgcn_make_buffer_rsrc((void*)p, 0, bytes, 0x00020000);
}
template <int CTRL> __device__ __forceinline__ float dppf(float v) {
  return __uint_as_float(__builtin_amdgcn_update_dpp(0, __float_as_uint(v), CTRL, 0xF, 0xF, true));
}
template <int CTRL> __device__ __forceinline__ unsigned dppu(unsigned v) { return __builtin_amdgcn_update_dpp(0, v, CTRL, 0xF, 0xF, true); }
__device__ __forceinline__ unsigned rdl(unsigned v, int l) { return (unsigned)__builtin_amdgcn_readlane((int)v, l); }
__device__ __forceinline__ float rdlf(float v, int l) { return __uint_as_float(rdl(__float_as_uint(v), l)); }
__device__ __forceinline__ float sum8(float v) { v += dppf<0xB1>(v); v += dppf<0x4E>(v); v += dppf<0x141>(v); return v; }
__device__ __forceinline__ u32x4 pack8(const float* v) { return u32x4{pkbf(v[0], v[1]), pkbf(v[2], v[3]), pkbf(v[4], v[5]), pkbf(v[6], v[7])}; }

constexpr int K1_SLOT = 16384 + 12288, K1_CS = 4 * K1_SLOT, K1_CSP = K1_CS + 512;
constexpr int L_Q = 0, L_KV = 24576, L_ONE = L_KV + 131072, LDS_BYTES = 163840;
constexpr int L_F = 0, L_F40 = L_ONE + 1024, L_WO = 40960, L_PA = L_WO + 65536, L_SEL = L_PA + 32768, L_ST = L_SEL + 12288, L_WF = L_ONE + 64;
constexpr int NGU = 86, DN0 = 128, NDN = 32;

template <int NV>
__device__ __forceinline__ void qkv_body(const __amdgpu_buffer_rsrc_t r_ws, LAS char* lds, int rb, int cb, int wid) {
  const int lane = threadIdx.x & 63, t = lane & 31, h = lane >> 5;
  int xr[4];
#pragma unroll
  for (int u = 0; u < 4; ++u) xr[u] = wid * 4096 + t * 128 + (((2 * u + h) ^ ((t >> 1) & 7)) * 16);
  const int wrd = 16384 + (8 * h + ((lane >> 2) & 3)) * 192 + 32 * ((lane >> 4) & 1) + 8 * (lane & 3);
  f32x16 acc[3];
#pragma unroll
  for (int j = 0; j < 3; ++j) acc[j] = f32x16{};
  float s1a = 0.f, s1b = 0.f, s2a = 0.f, s2b = 0.f;
  u32x4 xf = u32x4{0, 0, 0, 0}, wf[3];
#pragma unroll
  for (int j = 0; j < 3; ++j) wf[j] = u32x4{0, 0, 0, 0};
  auto mm = [&]() {
#pragma unroll
    for (int j = 0; j < 3; ++j) {
      if (j >= 3 - NV) acc[j] = mfma32(xf, wf[j], acc[j]);
      else acc[j] = mfma32(wf[j], xf, acc[j]);
    }
  };
  const int tile = rb * 4 + wid;
  const int voffV = tile * 2048 + 1024 + (t >> 4) * 131072 + h * 256 + swapb23(t & 15) * 16;
  const int voffK = tile * 2048 + swapb23(t) * 32 + h * 16;
  const int voffQ = tile * 1024 + t * 32 + h * 16;
#pragma unroll 1
  for (int c = 0; c < 8; ++c) {
    LDS_BAR();
    const LAS char* sb = lds + (c & 3) * K1_SLOT;
    u32x4 xn[4];
#pragma unroll
    for (int u = 0; u < 4; ++u) {
      xn[u] = ldsr128(sb + xr[u]);
      u32x4 wn[3];
#pragma unroll
      for (int j = 0; j < 3; ++j) wn[j] = ldstr2(sb + wrd + u * 3072 + j * 64, 768);
      mm();
#pragma unroll
      for (int z = 0; z < 3; ++z) { __builtin_amdgcn_sched_group_barrier(0x8, 1, 0); __builtin_amdgcn_sched_group_barrier(0x102, 3, 0); }
      __builtin_amdgcn_sched_barrier(0);
      xf = xn[u];
#pragma unroll
      for (int j = 0; j < 3; ++j) wf[j] = wn[j];
    }
#pragma unroll
    for (int u = 0; u < 4; ++u)
#pragma unroll
      for (int e = 0; e < 4; e += 2) {
        s1a = dot2bf(xn[u][e], ONE2, s1a); s2a = dot2bf(xn[u][e], xn[u][e], s2a);
        s1b = dot2bf(xn[u][e + 1], ONE2, s1b); s2b = dot2bf(xn[u][e + 1], xn[u][e + 1], s2b);
      }
    asm volatile("" : "+v"(s1a), "+v"(s1b), "+v"(s2a), "+v"(s2b));
    __builtin_amdgcn_sched_barrier(0);
  }
  __builtin_amdgcn_s_barrier();
  mm();
  LAS float* scr = (LAS float*)(lds + wid * 4096);
  const LAS float* csum = (const LAS float*)(lds + K1_CS);
  const float s2 = swap32_sum(s2a + s2b);
  const float mean = swap32_sum(s1a + s1b) * (1.f / D_);
  const float var = __builtin_fmaxf(s2 * (1.f / D_) - mean * mean, 0.f);
  const float rstd = __builtin_amdgcn_rsqf(var + LN_EPS);
  if (NV > 0 && h == 0) { scr[t] = rstd; scr[32 + t] = mean * rstd; }
  BAR();
#pragma unroll
  for (int j = 0; j < 3; ++j) {
    const int gt = cb * 3 + j;
    float v[16];
    if (j >= 3 - NV) {
      const float cs = csum[32 * j + t];
#pragma unroll
      for (int gq = 0; gq < 4; ++gq) {
        const f32x4 a = *(const LAS f32x4*)(scr + 8 * gq + 4 * h), b = *(const LAS f32x4*)(scr + 32 + 8 * gq + 4 * h);
#pragma unroll
        for (int e = 0; e < 4; ++e) v[4 * gq + e] = acc[j][4 * gq + e] * a[e] - b[e] * cs;
      }
      const int so = WS_KV + (gt - 32) * 262144;
      ST128(pack8(v), voffV, so); ST128(pack8(v + 8), voffV + 512, so);
    } else {
      const bool isq = gt < 16;
      const float sc = isq ? QSCALE : 1.f;
      const float a = rstd * sc, b = mean * rstd * sc;
#pragma unroll
      for (int gq = 0; gq < 4; ++gq) {
        const f32x4 c4 = *(const LAS f32x4*)(csum + 32 * j + 8 * gq + 4 * h);
#pragma unroll
        for (int e = 0; e < 4; ++e) v[4 * gq + e] = acc[j][4 * gq + e] * a - b * c4[e];
      }
      const int vo = isq ? voffQ : voffK;
      const int so = isq ? WS_QF + gt * 131072 : WS_KV + (gt - 16) * 262144;
      ST128(pack8(v), vo, so); ST128(pack8(v + 8), vo, so + (isq ? 65536 : 131072));
    }
  }
}

__device__ __forceinline__ float rowmax16(const f32x16& s) {
  float a = __builtin_fmaxf(__builtin_fmaxf(s[0], s[1]), s[2]), b = __builtin_fmaxf(__builtin_fmaxf(s[3], s[4]), s[5]);
  a = __builtin_fmaxf(__builtin_fmaxf(a, s[6]), s[7]); b = __builtin_fmaxf(__builtin_fmaxf(b, s[8]), s[9]);
  a = __builtin_fmaxf(__builtin_fmaxf(a, s[10]), s[11]); b = __builtin_fmaxf(__builtin_fmaxf(b, s[12]), s[13]);
  a = __builtin_fmaxf(__builtin_fmaxf(a, s[14]), s[15]);
  return swap32_max(__builtin_fmaxf(a, b));
}
__device__ __forceinline__ f32x8 lo8(const f32x16& s) { return __builtin_shufflevector(s, s, 0, 1, 2, 3, 4, 5, 6, 7); }
__device__ __forceinline__ f32x8 hi8(const f32x16& s) { return __builtin_shufflevector(s, s, 8, 9, 10, 11, 12, 13, 14, 15); }

#define E1(X, i) "v_exp_f32 v[" #X "+" #i "], v[" #X "+" #i "]\n\t"
#define SMF(A, B, off) "v_smfmac_f32_32x32x32_bf16 v[0:15], v[" #A ":" #A "+3], v[" #B "+" #off ":" #B "+" #off "+7], v88\n\t"
#define QK(Y) "v_mfma_f32_32x32x16_bf16 v[" #Y ":" #Y "+15], v[68:71], v[64:67], v[16:31]\n\t"
#define RDV(D, off) "ds_read_b128 v[" #D ":" #D "+3], v90 offset:" #off "\n\t"
#define RDK(off) "ds_read_b128 v[68:71], v89 offset:" #off "\n\t"
#define STEP(X, Y, VA, VPB, VA2, VB2, KOFF, VOFF, VOFF2, PA, PB, EXTRA) \
  PA E1(X, 0) E1(X, 1) SMF(VPB, Y, 8) RDV(VA2, VOFF) \
  PB E1(X, 2) E1(X, 3) E1(X, 4) E1(X, 5) \
  "s_waitcnt lgkmcnt(1)\n\t" QK(Y) RDV(VB2, VOFF2) RDK(KOFF) \
  E1(X, 6) E1(X, 7) E1(X, 8) E1(X, 9) \
  SMF(VA, X, 0) EXTRA \
  E1(X, 10) E1(X, 11) E1(X, 12) E1(X, 13) E1(X, 14) E1(X, 15)
#define ATTN_LOOP(L1, L2, P0, P2, P3) \
        L1 ":\n\t" \
        "s_bitcmp1_b32 0x408, %[cnt]\n\t" \
        "s_cbranch_scc0 " L2 "f\n\t" \
        "s_barrier\n\t" \
        L2 ":\n\t" \
        STEP(32, 48, 72, 84, 80, 84, 4096, 2048, 2560, P0, "", "") \
        STEP(48, 32, 80, 76, 72, 76, 6144, 4096, 4608, "", "", "") \
        STEP(32, 48, 72, 84, 80, 84, 8192, 6144, 6656, P2, "", "v_add_u32 v90, v90, v91\n\t") \
        STEP(48, 32, 80, 76, 72, 76, 10240, 0, 512, "", P3, "v_add_u32 v89, 0x2000, v89\n\ts_add_u32 %[cnt], %[cnt], 1\n\ts_cmp_lg_u32 %[cnt], 16\n\t") \
        "s_cbranch_scc1 " L1 "b\n\t"

__global__ __launch_bounds__(576, 3) void fused_block(const u16* __restrict__ x, const u16* __restrict__ wqkv, const u16* __restrict__ wo,
                                                      const u16* __restrict__ wr, const u16* __restrict__ wg, const u16* __restrict__ wu,
                                                      const u16* __restrict__ wd, char* __restrict__ ws, u16* __restrict__ out) {
  extern __shared__ __attribute__((aligned(16))) char smem[];
  LAS char* lds = (LAS char*)smem;
  const unsigned lds0 = (unsigned)(uintptr_t)lds;
  {
    unsigned long long pc; unsigned s0, s1, v0;
    asm volatile("v_readfirstlane_b32 %1, %4\n\ts_lshr_b32 %2, %5, 3\n\ts_getpc_b64 %0\n\ts_and_b64 %0, %0, -16\n\t"
                 "s_cmp_lt_u32 %1, 64\n\ts_cbranch_scc0 9f\n\t"
                 "s_cmp_lt_u32 %2, %6\n\ts_cbranch_scc0 9f\n\t"
                 "s_lshl_b32 %2, %2, 10\n\tv_lshlrev_b32 %3, 4, %4\n\tv_add_u32 %3, %2, %3\n\t"
                 "s_mov_b32 %1, m0\n\ts_mov_b32 m0, %7\n\ts_nop 0\n\t"
                 "global_load_lds_dwordx4 %3, %0\n\ts_mov_b32 m0, %1\n\t9:"
                 : "=&s"(pc), "=&s"(s0), "=&s"(s1), "=&v"(v0) : "v"(threadIdx.x), "s"(blockIdx.x), "i"(CODE_KB), "s"(lds0 + 131072) : "scc", "memory");
  }
  const int tid = threadIdx.x, lane = tid & 63, t = lane & 31, h = lane >> 5;
  const int wid = __builtin_amdgcn_readfirstlane(tid >> 6);
  const int b = blockIdx.x;
  const int rb1 = 4 * (b & 3) + ((b >> 3) & 3), cb1 = 8 * ((b >> 2) & 1) + (b >> 5);
  const int head = (b & 7) * 4 + ((b >> 3) & 3), qb = b >> 5;
  const int pm = head, cb = qb;
  const unsigned l16 = lane * 16;
  const int lw = wid - 4;
  const int rot = ((((b >> 5) + ((b >> 3) & 3)) & 1)) * 4;
  unsigned xoff[2], woff[3];
  auto stage = [&](int c) {
    const int kc = (c + rot) & 7;
    const char* xs = (const char*)x + kc * 128;
    const char* wsrc = (const char*)wqkv + kc * (64 * 3072);
    const unsigned base = lds0 + (c & 3) * K1_SLOT;
#pragma unroll
    for (int p = 0; p < 4; ++p) glds16(xs + (p >> 1) * 16384, xoff[p & 1], base + lw * 4096 + p * 1024);
#pragma unroll
    for (int p = 0; p < 3; ++p) glds16(wsrc, woff[p], base + 16384 + (lw * 3 + p) * 1024);
  };
  if (lw >= 0 && lw < 4) {
#pragma unroll
    for (int p = 0; p < 2; ++p) {
      const int row = p * 8 + (lane >> 3), slot = lane & 7;
      xoff[p] = (unsigned)((rb1 * 128 + lw * 32 + row) * 1024 + ((slot ^ ((row >> 1) & 7)) * 16));
    }
#pragma unroll
    for (int p = 0; p < 3; ++p) {
      const int idx = (lw * 3 + p) * 64 + lane, k = idx / 12, c = idx - k * 12;
      woff[p] = (unsigned)(k * 3072 + cb1 * 192 + c * 16);
    }
    stage(0); stage(1);
  }
  FENCE();
  const bool isgu = b < NGU, isdn = b >= DN0 && b < DN0 + NDN;
  const __amdgpu_buffer_rsrc_t r_ws = mkrsrc(ws, WS_BYTES), r_out = mkrsrc(out, 2u << 20);

  if (wid == 8) {
    __builtin_amdgcn_s_setprio(3);
    const u32x2 ep = LD64(0, WS_EPOCH);
    const u32x4 w4 = *(const u32x4*)(wr + lane * 8);
    if (lane < 16) { const unsigned v = (lane & 1) ? 0u : ONE2; *(LAS u32x4*)(lds + L_ONE + (lane >> 2) * 2048 + ((lane >> 1) & 1) * 512 + (lane & 1) * 16) = u32x4{v, v, v, v}; }
    if (lane == 16) *(LAS u32x4*)(lds + L_WF) = u32x4{0, 0, 0, 0};
#pragma unroll 1
    for (int c = 0; c < 5; ++c) BAR();
    const unsigned long long e64 = (((unsigned long long)ep[1] << 32) | ep[0]) + 1ull;
    const u32x2 tag = u32x2{(unsigned)e64, (unsigned)(e64 >> 32)};
    float sw;
    sw = dot2bf(w4[0], ONE2, dot2bf(w4[1], ONE2, 0.f)) + dot2bf(w4[2], ONE2, dot2bf(w4[3], ONE2, 0.f));
    sw = sum8(sw); sw += dppf<0x140>(sw);
    sw = (rdlf(sw, 0) + rdlf(sw, 16)) + (rdlf(sw, 32) + rdlf(sw, 48));
    const int hp = head >> 1;
    const int fidx = lane < 16 ? lane * 16 + (16 + hp) / 3 : lane < 32 ? (lane - 16) * 16 + (32 + hp) / 3 : (qb * 2 + (lane & 1)) * 16 + hp / 3;
    const char* kvsrc = ws + WS_KV + (size_t)head * 131072;
    unsigned foff[4];
    const unsigned fstr = isgu ? 32 * F_ * 2 : 32768;
#pragma unroll
    for (int j = 0; j < 4; ++j)
      foff[j] = (isgu ? (unsigned)((lane >> 1) * (F_ * 2) + b * 32) : (unsigned)((lane >> 1) * 1024 + (b - DN0) * 32)) + (lane & 1) * 16 + j * (fstr - 1024);
    unsigned wooff[4];
#pragma unroll
    for (int j = 0; j < 4; ++j) wooff[j] = (unsigned)((lane >> 3) * 1024 + cb * 128 + (((lane & 7) ^ (((lane >> 4) & 1) << 2)) * 16) + j * 7168);
    u32x4 s4 = u32x4{SENT, SENT, SENT, SENT};
    unsigned armoff = (lane >> 3) * 1024 + (lane & 7) * 16, l128 = lane * 128;
    asm volatile("" : "+v"(foff[0]), "+v"(foff[1]), "+v"(foff[2]), "+v"(foff[3]), "+v"(wooff[0]), "+v"(wooff[1]), "+v"(wooff[2]), "+v"(wooff[3]),
                      "+v"(s4), "+v"(armoff), "+v"(l128));
    LDS_BAR();
#pragma unroll 1
    for (int c = 6; c < 10; ++c) BAR();
    BAR();
    if (lane == 0) ST64(tag, 0, WS_FLAG + (rb1 * 16 + cb1) * 128);
    unsigned long long seen = 0;
    auto wait_flags = [&](unsigned long long need) {
#pragma unroll 1
      for (unsigned spins = 0; spins < SPIN_CAP && (seen & need) != need; ++spins) {
        FENCE();
        const u32x2 f = LD64(fidx * 128, WS_FLAG);
        seen = __ballot((f[0] == tag[0]) & (f[1] == tag[1]));
      }
      FENCE();
    };
    wait_flags(0x3000F000Full);
    glds8c(ws + WS_QF + head * 65536 + qb * 8192, l16, lds0 + L_Q);
#pragma unroll
    for (int p = 0; p < 4; ++p) glds8c(kvsrc + p * 8192, l16, lds0 + L_KV + p * 8192);
    VM_BAR(4);
    wait_flags(0xFFFFFFFFull);
#pragma unroll 1
    for (int p = 4; p < 10; ++p) glds8c(kvsrc + p * 8192, l16, lds0 + L_KV + p * 8192);
    WAIT_VM(8);
#pragma unroll 1
    for (int p = 10; p < 16; ++p) glds8c(kvsrc + p * 8192, l16, lds0 + L_KV + p * 8192);
    VM_BAR(0);
    {
#pragma unroll
      for (int e = 0; e < 8; ++e) __builtin_amdgcn_raw_buffer_store_b128(s4, r_ws, l16, WS_OF + (qb * 4 + (e >> 1)) * 65536 + head * 2048 + (e & 1) * 1024, SC1);
#pragma unroll
      for (int e = 0; e < 8; ++e) {
        const int so = (pm * 64 + e * 8) * 1024 + cb * 128;
        __builtin_amdgcn_raw_buffer_store_b128(s4, r_ws, armoff, WS_HN + so, SC1);
        __builtin_amdgcn_raw_buffer_store_b128(s4, r_out, armoff, so, SC1);
      }
      __builtin_amdgcn_raw_buffer_store_b128(s4, r_ws, l16, WS_XS + (pm * 8 + cb) * 1024, SC1);
      if (lane < 8 && b < (22016 + 8192) / 128) __builtin_amdgcn_raw_buffer_store_b128(s4, r_ws, l16, WS_ACT + b * 128, SC1);
    }
    WAIT_VM(0);
    asm volatile("" :: "v"(s4));
    if (lane == 0) ST64(tag, 0, WS_FLAG2 + b * 128);
    auto ffn = [&](int q0, int q1) {
      if (isgu) {
#pragma unroll 1
        for (int p = q0; p < min(q1, 8); ++p) glds4((const char*)(p < 4 ? wg : wu) + (p & 3) * (128 * F_ * 2), foff, lds0 + L_F + p * 4096);
      } else if (isdn) {
#pragma unroll 1
        for (int p = q0; p < min(q1, 10); ++p) glds4((const char*)wd + p * 131072, foff, lds0 + L_F + p * 4096);
        if (q1 > 10) {
#pragma unroll 1
          for (int p = 40; p < 43; ++p) glds16((const char*)wd + p * 32768, foff[0], lds0 + L_F40 + (p - 40) * 2048);
        }
      }
    };
    ffn(0, 11);
    BAR();
    WAIT_VM(24);
#pragma unroll 1
    for (int p = 0; p < 8; ++p) glds4((const char*)wo + p * 32768, wooff, lds0 + L_WO + p * 4096);
    WAIT_VM(24);
#pragma unroll 1
    for (int p = 8; p < 16; ++p) glds4((const char*)wo + p * 32768, wooff, lds0 + L_WO + p * 4096);
#pragma unroll 1
    for (unsigned spins = 0; spins < SPIN_CAP; ++spins) {
      FENCE();
      bool ok = true;
#pragma unroll
      for (int e = 0; e < 4; ++e) { const u32x2 f = LD64(l128, WS_FLAG2 + e * 8192); ok &= (f[0] == tag[0]) & (f[1] == tag[1]); }
      if (__all(ok)) break;
      __builtin_amdgcn_s_sleep(16);
    }
    if (b == 0 && lane == 0) ST64(tag, 0, WS_EPOCH);
    WAIT_VM(0);
    BAR();
    BAR(); BAR();
    u32x4 pv[8];
    {
      const u32x4 own = nosent4(ldsr128(lds + L_ST + l16));
      ST128(own, l16, WS_XS + (pm * 8 + cb) * 1024);
      __builtin_amdgcn_s_sleep(10);
#pragma unroll
      for (int e = 0; e < 8; ++e) pv[e] = LD128(l16, WS_XS + (pm * 8 + e) * 1024);
#pragma unroll 1
      for (unsigned spins = 0; spins < SPIN_CAP; ++spins) {
        bool miss[8], any = false;
#pragma unroll
        for (int e = 0; e < 8; ++e) { miss[e] = __any(umax4(pv[e]) == SENT); any |= miss[e]; }
        if (!any) break;
        FENCE();
#pragma unroll
        for (int e = 0; e < 8; ++e) if (miss[e]) pv[e] = LD128(l16, WS_XS + (pm * 8 + e) * 1024);
      }
    }
#define SUM8(c) (((__uint_as_float(pv[0][c]) + __uint_as_float(pv[1][c])) + (__uint_as_float(pv[2][c]) + __uint_as_float(pv[3][c]))) + \
                 ((__uint_as_float(pv[4][c]) + __uint_as_float(pv[5][c])) + (__uint_as_float(pv[6][c]) + __uint_as_float(pv[7][c]))))
    const float mean = SUM8(0) * (1.f / D_);
    const float rstd = __builtin_amdgcn_rsqf(__builtin_fmaxf(SUM8(1) * (1.f / D_) - mean * mean, 0.f) + LN_EPS);
    if (cb == 0) {
      const unsigned u = __float_as_uint(rstd * (SUM8(2) - mean * sw));
      const unsigned ordv = (u & 0x80000000u) ? ~u : (u | 0x80000000u);
      __builtin_amdgcn_raw_buffer_store_b32(max((ordv >> 1) & 0x7FFFF800u, 0x800u) | (unsigned)(2047 - (pm * 64 + lane)), r_ws, lane * 4, WS_KEY + pm * 256, SC1);
    }
    *(LAS u32x2*)(lds + L_ST + 1024 + lane * 8) = u32x2{__float_as_uint(rstd), __float_as_uint(mean * rstd)};
    LDS_BAR();
    return;
  }

  if (wid >= 4) {
    const int wrd = 16384 + lw * 3072 + (8 * h + ((lane >> 2) & 3)) * 192 + 32 * ((lane >> 4) & 1) + 8 * (lane & 3);
    float cs[3] = {0.f, 0.f, 0.f};
#pragma unroll 1
    for (int c = 0; c < 8; ++c) {
      if (c < 7) WAIT_BAR(7); else WAIT_BAR(0);
      const LAS char* sb = lds + (c & 3) * K1_SLOT + wrd;
      u32x4 wf[3];
#pragma unroll
      for (int j = 0; j < 3; ++j) wf[j] = ldstr2(sb + j * 64, 768);
      if (c < 6) stage(c + 2);
#pragma unroll
      for (int e = 0; e < 4; ++e)
#pragma unroll
        for (int j = 0; j < 3; ++j) cs[j] = dot2bf(wf[j][e], ONE2, cs[j]);
    }
#pragma unroll
    for (int j = 0; j < 3; ++j) *(LAS float*)(lds + K1_CSP + (lw * 2 + h) * 384 + (32 * j + t) * 4) = cs[j];
    LDS_BAR();
    if (lw == 0 && lane < 24) {
      const LAS char* pp = lds + K1_CSP + l16;
      f32x4 sacc = *(const LAS f32x4*)pp;
#pragma unroll
      for (int e = 1; e < 8; ++e) sacc += *(const LAS f32x4*)(pp + e * 384);
      *(LAS f32x4*)(lds + K1_CS + l16) = sacc;
    }
    LDS_BAR();
    BAR();
  } else {
    if (cb1 < 10) qkv_body<0>(r_ws, lds, rb1, cb1, wid);
    else if (cb1 == 10) qkv_body<1>(r_ws, lds, rb1, cb1, wid);
    else qkv_body<3>(r_ws, lds, rb1, cb1, wid);
    WAIT_BAR(0);
  }

  const bool onel = (lane & 16) != 0;
  unsigned kaddr = lds0 + L_KV + t * 32 + h * 16;
  unsigned vaddr = lds0 + (onel ? L_ONE + ((lane & 11) ? 16 : 0) : L_KV + 1024 + h * 256 + (lane & 15) * 16);
  const int vstep = onel ? 0 : 8192;
  const int IDX = 0xDDDDDDDD;
  const int row = pm * 64 + wid * 8 + (lane >> 3), col = cb * 64 + (lane & 7) * 8;
  f32x16 negm, sx, sy = f32x16{}, o = f32x16{};
  u32x4 vpb = u32x4{0, 0, 0, 0};
  asm volatile("" : "+{v[0:15]}"(o), "+{v[48:63]}"(sy), "+{v[84:87]}"(vpb));

  BAR();
  const u32x4 xres = *(const u32x4*)(x + row * D_ + col), wrv = *(const u32x4*)(wr + col);
  const int mh = wid >> 2, kq = wid & 3;
  const u32x4 qf = ldsr128(lds + L_Q + kq * 2048 + mh * 1024 + t * 32 + h * 16);
  int ostoff = t * 32 + h * 16 + WS_OF + (qb * 4 + kq) * 65536 + head * 2048 + mh * 1024;
  asm volatile("" : "+v"(ostoff));
  u32x4 kn, va, vb;
  {
    const u32x4 k0 = ldsr128((const LAS char*)(uintptr_t)kaddr);
    kn = ldsr128((const LAS char*)(uintptr_t)kaddr + 2048);
    va = ldsr128((const LAS char*)(uintptr_t)vaddr); vb = ldsr128((const LAS char*)(uintptr_t)vaddr + 512);
    const float m = -rowmax16(mfma32(k0, qf, f32x16{}));
#pragma unroll
    for (int r = 0; r < 16; ++r) negm[r] = m;
    sx = mfma32(k0, qf, negm);
  }
  {
    unsigned cnt;
    asm volatile(
        "s_mov_b32 %[cnt], 0\n\t"
        "s_nop 7\n\t"
        "s_cmp_gt_u32 %[wid], 3\n\t"
        "s_cbranch_scc1 3f\n\t"
        "s_setprio 1\n\t"
        ATTN_LOOP("1", "2", "", "", "")
        "s_setprio 0\n\t"
        "s_branch 5f\n\t"
        ATTN_LOOP("3", "4", "s_setprio 2\n\t", "", "s_setprio 0\n\t")
        "5:\n\t"
        "s_nop 1\n\t"
        SMF(84, 48, 8)
        "s_waitcnt lgkmcnt(0)\n\t"
        "s_nop 11\n\t"
        : "+{v[0:15]}"(o), "+{v[32:47]}"(sx), "+{v[48:63]}"(sy), "+{v[68:71]}"(kn), "+{v[72:75]}"(va), "+{v[76:79]}"(vb),
          "+{v[84:87]}"(vpb), "+{v89}"(kaddr), "+{v90}"(vaddr), [cnt] "=&s"(cnt)
        : "{v[16:31]}"(negm), "{v[64:67]}"(qf), "{v88}"(IDX), "{v91}"(vstep), [wid] "s"(wid)
        : "v80", "v81", "v82", "v83", "scc", "memory");
  }
  auto normalise = [&]() {
    const float inv = __builtin_amdgcn_rcpf(o[8]);
    return nosent4(u32x4{pkbf(o[0] * inv, o[1] * inv), pkbf(o[2] * inv, o[3] * inv), pkbf(o[4] * inv, o[5] * inv), pkbf(o[6] * inv, o[7] * inv)});
  };
  u32x4 opk = normalise();
  asm volatile("" : "+v"(opk));
  {
    float c0, c1, c2, c3;
    asm("v_add_f32 %0, |%1|, |%2|" : "=v"(c0) : "v"(o[0]), "v"(o[1])); asm("v_add_f32 %0, |%1|, |%2|" : "=v"(c1) : "v"(o[2]), "v"(o[3]));
    asm("v_add_f32 %0, |%1|, |%2|" : "=v"(c2) : "v"(o[4]), "v"(o[5])); asm("v_add_f32 %0, |%1|, |%2|" : "=v"(c3) : "v"(o[6]), "v"(o[7]));
    const float chk = (c0 + c1) + (c2 + c3);
    const bool bad = !(o[8] < 1.8446744e19f) || !(chk < 3.0e38f);
    if (__builtin_expect(__any(bad), 0)) {
      float m = -INFINITY;
      o = f32x16{};
      const int kqo = head * 131072 + t * 32 + h * 16, vq = head * 131072 + 1024 + h * 256 + (lane & 15) * 16;
      const u32x4 ones = u32x4{ONE2, ONE2, ONE2, ONE2};
#pragma unroll 1
      for (int i = 0; i < 64; ++i) {
        f32x16 s = mfma32(LD128(kqo, WS_KV + i * 2048), qf, f32x16{});
        u32x4 v0 = LD128(vq, WS_KV + i * 2048), v1 = LD128(vq + 512, WS_KV + i * 2048);
        if (onel) { v0 = ones; v1 = ones; }
        const float mn = __builtin_fmaxf(m, rowmax16(s));
        const float al = __builtin_amdgcn_exp2f(m - mn);
        m = mn;
#pragma unroll
        for (int r = 0; r < 16; ++r) { o[r] *= al; s[r] = __builtin_amdgcn_exp2f(s[r] - mn); }
        o = smfmac32(v0, lo8(s), o, IDX);
        o = smfmac32(v1, hi8(s), o, IDX);
      }
      opk = normalise();
    }
  }
  __builtin_amdgcn_raw_buffer_store_b128(opk, r_ws, ostoff, 0, SC1);
  BAR();

  const int g = lane >> 4, q = (lane >> 2) & 3, p4 = lane & 3;
  f32x4 a0 = f32x4{bflo(xres[0]), bfhi(xres[0]), bflo(xres[1]), bfhi(xres[1])}, a1 = f32x4{bflo(xres[2]), bfhi(xres[2]), bflo(xres[3]), bfhi(xres[3])};
  unsigned ra0, ra1;
  {
    const int lr = wid * 8 + (lane >> 3);
    const int sl = ((lane & 7) ^ ((lr & 7) | ((lr & 1) << 3))) * 16;
    const unsigned rbase = lds0 + (mh ? L_WO : L_PA) + (lr & 31) * 256;
    ra0 = rbase + sl; ra1 = rbase + (sl ^ 128);
  }
  LAS unsigned* cand = (LAS unsigned*)(lds + L_SEL);
  {
    u32x4 of[8], wf0[8], wf1[8];
    const int flip = ((q >> 1) & 1) << 6;
    const LAS char* wb = lds + L_WO + kq * 16384 + (8 * h + q) * 128 + 32 * (g & 1) + 8 * p4;
#pragma unroll
    for (int e = 0; e < 8; ++e) { wf0[e] = ldstr2(wb + flip + e * 2048, 512); wf1[e] = ldstr2(wb + (64 ^ flip) + e * 2048, 512); }
    const int ooff = t * 32 + h * 16, osoff = WS_OF + pm * 65536 + kq * 16384 + mh * 1024;
    unsigned pa[8];
    {
      const unsigned rb = lds0 + (mh ? L_WO + kq * 16384 : L_PA + kq * 8192) + t * 256;
      const int swz = (t & 7) | ((t & 1) << 3);
#pragma unroll
      for (int gq = 0; gq < 8; ++gq) { pa[gq] = rb + (((h * 8 + gq) ^ swz) * 16); asm volatile("" : "+v"(pa[gq])); }
    }
    asm volatile("" : "+v"(a0), "+v"(a1), "+v"(ra0), "+v"(ra1));
    __builtin_amdgcn_s_waitcnt(0xC07F);
    FENCE();
    if (!mh) *(volatile LAS unsigned*)(lds + L_WF + kq * 4) = 1u;
    else {
#pragma unroll 1
      for (unsigned spins = 0; spins < (1u << 24); ++spins) {
        if (__builtin_amdgcn_readfirstlane(*(volatile LAS unsigned*)(lds + L_WF + kq * 4))) break;
        __builtin_amdgcn_s_sleep(1);
      }
    }
    FENCE();
#pragma unroll
    for (int e = 0; e < 8; ++e) of[e] = LD128(ooff, osoff + e * 2048);
    f32x16 acc0 = f32x16{}, acc1 = f32x16{};
#pragma unroll 1
    for (unsigned spins = 0; spins < SPIN_CAP; ++spins) {
      bool miss[8], any = false;
#pragma unroll
      for (int e = 0; e < 8; ++e) { miss[e] = __any(umax4(of[e]) == SENT); any |= miss[e]; }
      if (!any) break;
      FENCE();
#pragma unroll
      for (int e = 0; e < 8; ++e) if (miss[e]) of[e] = LD128(ooff, osoff + e * 2048);
    }
#pragma unroll
    for (int e = 0; e < 8; ++e) acc0 = mfma32(wf0[e], of[e], acc0);
    __builtin_amdgcn_sched_barrier(0);
#pragma unroll
    for (int e = 0; e < 8; ++e) {
      acc1 = mfma32(wf1[e], of[e], acc1);
      if (e >= 3 && e < 7) *(LAS f32x4*)(uintptr_t)pa[e - 3] = f32x4{acc0[4 * e - 12], acc0[4 * e - 11], acc0[4 * e - 10], acc0[4 * e - 9]};
      asm volatile("" : "+v"(acc1));
      __builtin_amdgcn_sched_barrier(0);
    }
#pragma unroll
    for (int gq = 0; gq < 4; ++gq)
      *(LAS f32x4*)(uintptr_t)pa[4 + gq] = f32x4{acc1[4 * gq], acc1[4 * gq + 1], acc1[4 * gq + 2], acc1[4 * gq + 3]};
  }
  LDS_BAR();
  float hv[8];
  {
    const LAS char* r0 = (const LAS char*)(uintptr_t)ra0;
    const LAS char* r1 = (const LAS char*)(uintptr_t)ra1;
    f32x4 t0[4], t1[4];
    if (mh) {
#pragma unroll
      for (int e = 0; e < 4; ++e) { t0[e] = *(const LAS f32x4*)(r0 + e * 16384); t1[e] = *(const LAS f32x4*)(r1 + e * 16384); }
    } else {
#pragma unroll
      for (int e = 0; e < 4; ++e) { t0[e] = *(const LAS f32x4*)(r0 + e * 8192); t1[e] = *(const LAS f32x4*)(r1 + e * 8192); }
    }
    __builtin_amdgcn_sched_barrier(0);
    a0 += (t0[0] + t0[1]) + (t0[2] + t0[3]);
    a1 += (t1[0] + t1[1]) + (t1[2] + t1[3]);
    const u32x4 hb = u32x4{pkbf(a0[0], a0[1]), pkbf(a0[2], a0[3]), pkbf(a1[0], a1[1]), pkbf(a1[2], a1[3])};
    float s1 = 0.f, s2 = 0.f, sd = 0.f;
#pragma unroll
    for (int e = 0; e < 4; ++e) { s1 = dot2bf(hb[e], ONE2, s1); s2 = dot2bf(hb[e], hb[e], s2); sd = dot2bf(hb[e], wrv[e], sd); }
    s1 = sum8(s1); s2 = sum8(s2); sd = sum8(sd);
    if ((lane & 7) == 0) *(LAS u32x4*)(lds + L_ST + wid * 128 + lane * 2) = u32x4{__float_as_uint(s1), __float_as_uint(s2), __float_as_uint(sd), 0u};
    LDS_BAR();
    if (tid < 80) cand[tid] = 0;
#pragma unroll
    for (int e = 0; e < 4; ++e) { hv[2 * e] = bflo(hb[e]); hv[2 * e + 1] = bfhi(hb[e]); }
  }
  const int trd = (8 * g + q) * 32 + 8 * p4;
  u32x4 fw[8];
  if (isgu || isdn) {
#pragma unroll
    for (int p = 0; p < 8; ++p) {
      const int po = isgu ? L_F + ((p >> 2) * 16 + (wid & 3) * 4 + (p & 3)) * 1024 : p < 5 ? L_F + (p * 8 + wid) * 1024 : L_F40 + min(wid, 2) * 2048;
      fw[p] = ldstr2(lds + po + trd, 128);
    }
  }
  u32x4 k4;
  LDS_BAR();
  {
    const u32x2 st = *(const LAS u32x2*)(lds + L_ST + 1024 + wid * 64 + (lane >> 3) * 8);
    const float rstd = __uint_as_float(st[0]), mr = __uint_as_float(st[1]);
    u32x4 hp, op;
#pragma unroll
    for (int e = 0; e < 4; ++e) {
      hp[e] = pkbf(hv[2 * e] * rstd - mr, hv[2 * e + 1] * rstd - mr);
      op[e] = pkbf(hv[2 * e] + bflo(hp[e]), hv[2 * e + 1] + bfhi(hp[e]));
    }
    ST128(nosent4(hp), row * 1024 + col * 2, WS_HN);
    __builtin_amdgcn_raw_buffer_store_b128(nosent4(op), r_out, row * 1024 + col * 2, 0, SC1);
  }
  if (!isgu && !isdn) return;
#pragma unroll 1
  for (unsigned spins = 0; spins < SPIN_CAP; ++spins) {
    FENCE();
    k4 = LD128(tid * 16, WS_KEY);
    if (__all(umax4(k4) != SENT)) break;
  }
  {
    unsigned m = umax4(k4);
    m = max(m, dppu<0xB1>(m)); m = max(m, dppu<0x4E>(m)); m = max(m, dppu<0x141>(m)); m = max(m, dppu<0x140>(m));
    m = max(m, dppu<0x142>(m)); m = max(m, dppu<0x143>(m));
    if (lane == 63) cand[64 + wid] = m;
  }
  LDS_BAR();
  {
    const u32x4 g0 = *(LAS u32x4*)(cand + 64), g1 = *(LAS u32x4*)(cand + 68);
    const unsigned T = min(min(min(g0[0], g0[1]), min(g0[2], g0[3])), min(min(g1[0], g1[1]), min(g1[2], g1[3])));
    const unsigned c0 = k4[0] >= T, c1 = k4[1] >= T, c2 = k4[2] >= T, c3 = k4[3] >= T;
    const unsigned cnt = (c0 + c1) + (c2 + c3);
    if (cnt) {
      unsigned pos;
      asm volatile("ds_add_rtn_u32 %0, %1, %2\n\ts_waitcnt lgkmcnt(0)" : "=v"(pos) : "v"((unsigned)(lds0 + L_SEL + 72 * 4)), "v"(cnt) : "memory");
      cand[c0 && pos < 64 ? pos : 73] = k4[0]; pos += c0;
      cand[c1 && pos < 64 ? pos : 73] = k4[1]; pos += c1;
      cand[c2 && pos < 64 ? pos : 73] = k4[2]; pos += c2;
      cand[c3 && pos < 64 ? pos : 73] = k4[3];
    }
  }
  LDS_BAR();
  LAS unsigned* sel = cand + 80 + wid * 8;
  {
    unsigned nv = cand[72];
    unsigned c = cand[t];
    u32x4 o4[4];
#pragma unroll
    for (int e = 0; e < 4; ++e) o4[e] = *(LAS u32x4*)(cand + h * 16 + e * 4);
    asm volatile("" : "+v"(nv), "+v"(c), "+v"(o4[0]), "+v"(o4[1]), "+v"(o4[2]), "+v"(o4[3]));
    const unsigned n = __builtin_amdgcn_readfirstlane(nv);
    if (n <= 64 && wid >= (isgu ? 4 : 1)) {
      if (isgu) return;
    } else if (__builtin_expect(n <= 32, 1)) {
      unsigned ba = 0, bb = 0;
#pragma unroll
      for (int e = 0; e < 4; ++e) {
        ba = __builtin_amdgcn_alignbit(ba, c - o4[e][0], 31); bb = __builtin_amdgcn_alignbit(bb, c - o4[e][1], 31);
        ba = __builtin_amdgcn_alignbit(ba, c - o4[e][2], 31); bb = __builtin_amdgcn_alignbit(bb, c - o4[e][3], 31);
      }
      unsigned rk = __builtin_popcount(ba) + __builtin_popcount(bb);
      auto r2 = __builtin_amdgcn_permlane32_swap(rk, rk, false, false);
      rk = r2[0] + r2[1];
      if (rk < 8) sel[rk] = c;
    } else if (n <= 64) {
      const unsigned c2 = cand[32 + t];
      unsigned ba = 0, bb = 0;
#pragma unroll
      for (int e = 0; e < 8; ++e) {
        const u32x4 v = *(LAS u32x4*)(cand + h * 32 + e * 4);
#pragma unroll
        for (int j = 0; j < 4; ++j) { ba = __builtin_amdgcn_alignbit(ba, c - v[j], 31); bb = __builtin_amdgcn_alignbit(bb, c2 - v[j], 31); }
      }
      unsigned ra = __builtin_popcount(ba), rb2 = __builtin_popcount(bb);
      auto r1 = __builtin_amdgcn_permlane32_swap(ra, ra, false, false);
      auto r2 = __builtin_amdgcn_permlane32_swap(rb2, rb2, false, false);
      ra = r1[0] + r1[1]; rb2 = r2[0] + r2[1];
      if (ra < 8) sel[ra] = c;
      if (rb2 < 8) sel[rb2] = c2;
    } else {
      LAS unsigned* all = cand + 256;
      *(LAS u32x4*)(all + tid * 4) = k4;
      LDS_BAR();
      unsigned rk[4] = {0, 0, 0, 0};
#pragma unroll 1
      for (int j = 0; j < 2048; ++j) {
        const unsigned v = all[j];
#pragma unroll
        for (int e = 0; e < 4; ++e) rk[e] += v > k4[e];
      }
#pragma unroll
      for (int e = 0; e < 4; ++e) if (rk[e] < 8) cand[160 + rk[e]] = k4[e];
      LDS_BAR();
      sel = cand + 160;
      if (isgu && wid >= 4) return;
    }
  }
  const int tok = lane & 15;
  const unsigned key = sel[tok & 7];
  const int trow = 2047 - (int)(key & 2047u);
  LAS f32x4* red = (LAS f32x4*)(lds + L_WO);
  if (isgu) {
    u32x4 xb[4];
#pragma unroll 1
    for (unsigned spins = 0; spins < SPIN_CAP; ++spins) {
      FENCE();
#pragma unroll
      for (int u = 0; u < 4; ++u) xb[u] = LD128(trow * 1024 + u * 64 + g * 16, WS_HN + wid * 256);
      if (__all(max(max(umax4(xb[0]), umax4(xb[1])), max(umax4(xb[2]), umax4(xb[3]))) != SENT)) break;
    }
    f32x4 ag = f32x4{}, au = f32x4{};
#pragma unroll
    for (int u = 0; u < 4; ++u) { ag = mfma16(fw[u], xb[u], ag); au = mfma16(fw[4 + u], xb[u], au); }
    const f32x4 mine = tok < 8 ? ag : au;
    if (wid != 0) red[wid * 64 + lane] = mine;
    LDS_BAR();
    if (wid == 0) {
      const f32x4 s = (mine + red[64 + lane]) + (red[128 + lane] + red[192 + lane]);
      float av[4];
#pragma unroll
      for (int r = 0; r < 4; ++r) {
        const float gu = s[r] * dppf<0x128>(s[r]);
        av[r] = gu * __builtin_amdgcn_rcpf(1.f + __builtin_amdgcn_exp2f(s[r] * -1.4426950408889634f));
      }
      if (tok < 8) ST64((u32x2{nosent(pkbf(av[0], av[1])), nosent(pkbf(av[2], av[3]))}), tok * 32 + g * 8, WS_ACT + b * 256);
    }
    return;
  }
  const int nb = b - DN0;
  u32x2 o0 = u32x2{0, 0};
  float sg = 0.f;
  if (wid == 0) {
#pragma unroll 1
    for (unsigned spins = 0; spins < SPIN_CAP; ++spins) {
      FENCE();
      o0 = __builtin_amdgcn_raw_buffer_load_b64(r_out, trow * 1024 + g * 8, nb * 32, SC1);
      if (__all(max(o0[0], o0[1]) != SENT)) break;
    }
    const unsigned ov = ((key & 0x7FFFF800u) << 1) | 0x800u;
    const float r = rbf(__uint_as_float((ov & 0x80000000u) ? (ov & 0x7FFFFFFFu) : ~ov));
    sg = rbf(__builtin_amdgcn_rcpf(1.f + __expf(-r)));
  }
  const __amdgpu_buffer_rsrc_t r_act = mkrsrc(ws + WS_ACT, 22016);
  u32x4 ab[6];
  const int aoff = wid * 512 + (g >> 1) * 256 + (tok & 7) * 32 + (g & 1) * 16;
#pragma unroll
  for (int p = 0; p < 6; ++p) ab[p] = __builtin_amdgcn_raw_buffer_load_b128(r_act, aoff, p * 4096, SC1);
#pragma unroll 1
  for (unsigned spins = 0; spins < SPIN_CAP; ++spins) {
    bool miss[6], any = false;
#pragma unroll
    for (int p = 0; p < 6; ++p) { miss[p] = __any(umax4(ab[p]) == SENT); any |= miss[p]; }
    if (!any) break;
    FENCE();
#pragma unroll
    for (int p = 0; p < 6; ++p) if (miss[p]) ab[p] = __builtin_amdgcn_raw_buffer_load_b128(r_act, aoff, p * 4096, SC1);
  }
  f32x4 ay = f32x4{};
#pragma unroll
  for (int p = 0; p < 6; ++p) ay = mfma16(fw[p], ab[p], ay);
  red[wid * 64 + lane] = ay;
  LDS_BAR();
  if (wid == 0 && tok < 8) {
    ay = ((ay + red[64 + lane]) + (red[128 + lane] + red[192 + lane])) + ((red[256 + lane] + red[320 + lane]) + (red[384 + lane] + red[448 + lane]));
    __builtin_amdgcn_raw_buffer_store_b64(u32x2{pkbf(fmaf(ay[0], sg, bflo(o0[0])), fmaf(ay[1], sg, bfhi(o0[0]))), pkbf(fmaf(ay[2], sg, bflo(o0[1])), fmaf(ay[3], sg, bfhi(o0[1])))}, r_out,
                                          trow * 1024 + g * 8, nb * 32, SC1);
  }
}

extern "C" void kernel_launch(void* const* d_in, const int* in_sizes, int n_in, void* d_out, int out_size, void* d_ws, size_t ws_size,
                              hipStream_t stream) {
  (void)hipFuncSetAttribute((const void*)fused_block, hipFuncAttributeMaxDynamicSharedMemorySize, LDS_BYTES);
  fused_block<<<256, 576, LDS_BYTES, stream>>>((const u16*)d_in[0], (const u16*)d_in[1], (const u16*)d_in[2], (const u16*)d_in[4],
                                               (const u16*)d_in[5], (const u16*)d_in[6], (const u16*)d_in[7], (char*)d_ws, (u16*)d_out);
}
```

```cpp
#define DIAG 0
#include <hip/hip_runtime.h>
#include <hip/hip_bf16.h>

typedef unsigned short u16;
typedef __bf16 bf16x2_t __attribute__((ext_vector_type(2)));
typedef __bf16 bf16x8_t __attribute__((ext_vector_type(8)));
typedef __bf16 bf16x16_t __attribute__((ext_vector_type(16)));
typedef short s16x4 __attribute__((ext_vector_type(4)));
typedef float f32x4 __attribute__((ext_vector_type(4)));
typedef float f32x8 __attribute__((ext_vector_type(8)));
typedef float f32x16 __attribute__((ext_vector_type(16)));
typedef unsigned u32x2 __attribute__((ext_vector_type(2)));
typedef unsigned u32x4 __attribute__((ext_vector_type(4)));
#define LAS __attribute__((address_space(3)))

constexpr int D_ = 512, F_ = 1376;
constexpr unsigned SENT = 0xFFFFFFFFu;
constexpr float LN_EPS = 1e-5f;
constexpr float QSCALE = 0.25f * 1.4426950408889634f;
constexpr unsigned SPIN_CAP = 1u << 16;
constexpr int SC1 = 16;
constexpr unsigned ONE2 = 0x3F803F80u;
constexpr int CODE_KB = 25;

constexpr unsigned WS_KV = 0;
constexpr unsigned WS_QF = 4u << 20;
constexpr unsigned WS_OF = 6u << 20;
constexpr unsigned WS_HN = 8u << 20;
constexpr unsigned WS_XS = 10u << 20;
constexpr unsigned WS_ACT = WS_XS + (256u << 10);
constexpr unsigned WS_KEY = WS_ACT + 22016;
constexpr unsigned WS_FLAG = WS_XS + (320u << 10);
constexpr unsigned WS_FLAG2 = WS_FLAG + 32768;
constexpr unsigned WS_EPOCH = WS_FLAG2 + 32768;
constexpr unsigned WS_BYTES = WS_EPOCH + 128;

__device__ __forceinline__ f32x16 mfma32(u32x4 a, u32x4 b, f32x16 c) {
  return __builtin_amdgcn_mfma_f32_32x32x16_bf16(__builtin_bit_cast(bf16x8_t, a), __builtin_bit_cast(bf16x8_t, b), c, 0, 0, 0);
}
__device__ __forceinline__ f32x4 mfma16(u32x4 a, u32x4 b, f32x4 c) {
  return __builtin_amdgcn_mfma_f32_16x16x32_bf16(__builtin_bit_cast(bf16x8_t, a), __builtin_bit_cast(bf16x8_t, b), c, 0, 0, 0);
}
__device__ __forceinline__ f32x16 smfmac32(u32x4 a, f32x8 b, f32x16 c, int idx) {
  return __builtin_amdgcn_smfmac_f32_32x32x32_bf16(__builtin_bit_cast(bf16x8_t, a), __builtin_bit_cast(bf16x16_t, b), c, idx, 0, 0);
}
__device__ __forceinline__ float dot2bf(unsigned a, unsigned b, float c) {
  return __builtin_amdgcn_fdot2_f32_bf16(__builtin_bit_cast(bf16x2_t, a), __builtin_bit_cast(bf16x2_t, b), c, false);
}
__device__ __forceinline__ float swap32_sum(float v) {
  auto r = __builtin_amdgcn_permlane32_swap(__float_as_uint(v), __float_as_uint(v), false, false);
  return __uint_as_float(r[0]) + __uint_as_float(r[1]);
}
__device__ __forceinline__ float swap32_max(float v) {
  auto r = __builtin_amdgcn_permlane32_swap(__float_as_uint(v), __float_as_uint(v), false, false);
  return __builtin_fmaxf(__uint_as_float(r[0]), __uint_as_float(r[1]));
}
__device__ __forceinline__ unsigned pkbf(float lo, float hi) {
  bf16x2_t v; v[0] = (__bf16)lo; v[1] = (__bf16)hi; return __builtin_bit_cast(unsigned, v);
}
__device__ __forceinline__ float bflo(unsigned u) { return __uint_as_float(u << 16); }
__device__ __forceinline__ float bfhi(unsigned u) { return __uint_as_float(u & 0xFFFF0000u); }
__device__ __forceinline__ float rbf(float f) { return (float)(__bf16)f; }
__device__ __forceinline__ void glds16(const void* sbase, unsigned voff, unsigned lds_base) {
  unsigned sv;
  asm volatile("s_mov_b32 %0, m0\n\ts_mov_b32 m0, %3\n\ts_nop 0\n\tglobal_load_lds_dwordx4 %1, %2\n\ts_mov_b32 m0, %0"
               : "=&s"(sv) : "v"(voff), "s"(sbase), "s"(lds_base) : "memory");
}
__device__ __forceinline__ void glds8c(const void* sbase, unsigned voff, unsigned lds_base) {
  unsigned sv;
  asm volatile("s_mov_b32 %0, m0\n\ts_mov_b32 m0, %3\n\ts_nop 0\n\t"
               "global_load_lds_dwordx4 %1, %2 offset:-4096 sc1\n\tglobal_load_lds_dwordx4 %1, %2 offset:-3072 sc1\n\t"
               "global_load_lds_dwordx4 %1, %2 offset:-2048 sc1\n\tglobal_load_lds_dwordx4 %1, %2 offset:-1024 sc1\n\t"
               "global_load_lds_dwordx4 %1, %2 sc1\n\tglobal_load_lds_dwordx4 %1, %2 offset:1024 sc1\n\t"
               "global_load_lds_dwordx4 %1, %2 offset:2048 sc1\n\tglobal_load_lds_dwordx4 %1, %2 offset:3072 sc1\n\t"
               "s_mov_b32 m0, %0"
               : "=&s"(sv) : "v"(voff), "s"((const char*)sbase + 4096), "s"(lds_base + 4096) : "memory");
}
__device__ __forceinline__ void glds4(const void* sbase, const unsigned (&voff)[4], unsigned lds_base) {
  unsigned sv;
  asm volatile("s_mov_b32 %0, m0\n\ts_mov_b32 m0, %6\n\ts_nop 0\n\t"
               "global_load_lds_dwordx4 %1, %5\n\tglobal_load_lds_dwordx4 %2, %5 offset:1024\n\t"
               "global_load_lds_dwordx4 %3, %5 offset:2048\n\tglobal_load_lds_dwordx4 %4, %5 offset:3072\n\t"
               "s_mov_b32 m0, %0"
               : "=&s"(sv) : "v"(voff[0]), "v"(voff[1]), "v"(voff[2]), "v"(voff[3]), "s"(sbase), "s"(lds_base) : "memory");
}
#define FENCE() asm volatile("" ::: "memory")
#define WAIT_BAR(N) asm volatile("s_waitcnt vmcnt(" #N ") lgkmcnt(0)\n\ts_barrier" ::: "memory")
#define VM_BAR(N) asm volatile("s_waitcnt vmcnt(" #N ")\n\ts_barrier" ::: "memory")
#define LDS_BAR() do { FENCE(); __builtin_amdgcn_s_waitcnt(0xC07F); __builtin_amdgcn_s_barrier(); FENCE(); } while (0)
#define BAR() do { FENCE(); __builtin_amdgcn_s_barrier(); FENCE(); } while (0)
#define WAIT_VM(N) asm volatile("s_waitcnt vmcnt(" #N ")" ::: "memory")
#define LD128(off, soff) __builtin_amdgcn_raw_buffer_load_b128(r_ws, off, soff, SC1)
#define LD64(off, soff) __builtin_amdgcn_raw_buffer_load_b64(r_ws, off, soff, SC1)
#define ST128(v, off, soff) __builtin_amdgcn_raw_buffer_store_b128(v, r_ws, (off) + (soff), 0, SC1)
#define ST64(v, off, soff) __builtin_amdgcn_raw_buffer_store_b64(v, r_ws, off, soff, SC1)
__device__ __forceinline__ u32x4 ldsr128(const LAS char* p) { return *(const LAS u32x4*)p; }
__device__ __forceinline__ u32x2 ldstr(const LAS char* p) {
  return __builtin_bit_cast(u32x2, __builtin_amdgcn_ds_read_tr16_b64_v4i16((LAS s16x4*)p));
}
__device__ __forceinline__ u32x4 ldstr2(const LAS char* p, int off2) { const u32x2 a = ldstr(p), b = ldstr(p + off2); return u32x4{a[0], a[1], b[0], b[1]}; }
__device__ __forceinline__ int swapb23(int v) { return (v & ~12) | ((v & 4) << 1) | ((v & 8) >> 1); }
__device__ __forceinline__ unsigned nosent(unsigned v) { return min(v, 0xFFFFFFFEu); }
__device__ __forceinline__ u32x4 nosent4(u32x4 v) { return u32x4{nosent(v[0]), nosent(v[1]), nosent(v[2]), nosent(v[3])}; }
__device__ __forceinline__ unsigned umax4(u32x4 v) { return max(max(v[0], v[1]), max(v[2], v[3])); }
__device__ __forceinline__ __amdgpu_buffer_rsrc_t mkrsrc(const void* p, unsigned bytes) {
  return __builtin_amdgcn_make_buffer_rsrc((void*)p, 0, bytes, 0x00020000);
}
template <int CTRL> __device__ __forceinline__ float dppf(float v) {
  return __uint_as_float(__builtin_amdgcn_update_dpp(0, __float_as_uint(v), CTRL, 0xF, 0xF, true));
}
template <int CTRL> __device__ __forceinline__ unsigned dppu(unsigned v) { return __builtin_amdgcn_update_dpp(0, v, CTRL, 0xF, 0xF, true); }
__device__ __forceinline__ unsigned rdl(unsigned v, int l) { return (unsigned)__builtin_amdgcn_readlane((int)v, l); }
__device__ __forceinline__ float rdlf(float v, int l) { return __uint_as_float(rdl(__float_as_uint(v), l)); }
__device__ __forceinline__ float sum8(float v) { v += dppf<0xB1>(v); v += dppf<0x4E>(v); v += dppf<0x141>(v); return v; }
__device__ __forceinline__ u32x4 pack8(const float* v) { return u32x4{pkbf(v[0], v[1]), pkbf(v[2], v[3]), pkbf(v[4], v[5]), pkbf(v[6], v[7])}; }

constexpr int K1_SLOT = 16384 + 12288, K1_CS = 4 * K1_SLOT, K1_CSP = K1_CS + 512;
constexpr int L_Q = 0, L_KV = 24576, L_ONE = L_KV + 131072, LDS_BYTES = 163840;
constexpr int L_F = 0, L_F40 = L_ONE + 1024, L_WO = 40960, L_PA = L_WO + 65536, L_SEL = L_PA + 32768, L_ST = L_SEL + 12288, L_WF = L_ONE + 64;
constexpr int NGU = 86, DN0 = 128, NDN = 32;

template <int NV>
__device__ __forceinline__ void qkv_body(const __amdgpu_buffer_rsrc_t r_ws, LAS char* lds, int rb, int cb, int wid) {
  const int lane = threadIdx.x & 63, t = lane & 31, h = lane >> 5;
  int xr[4];
#pragma unroll
  for (int u = 0; u < 4; ++u) xr[u] = wid * 4096 + t * 128 + (((2 * u + h) ^ ((t >> 1) & 7)) * 16);
  const int wrd = 16384 + (8 * h + ((lane >> 2) & 3)) * 192 + 32 * ((lane >> 4) & 1) + 8 * (lane & 3);
  f32x16 acc[3];
#pragma unroll
  for (int j = 0; j < 3; ++j) acc[j] = f32x16{};
  float s1a = 0.f, s1b = 0.f, s2a = 0.f, s2b = 0.f;
  u32x4 xf = u32x4{0, 0, 0, 0}, wf[3];
#pragma unroll
  for (int j = 0; j < 3; ++j) wf[j] = u32x4{0, 0, 0, 0};
  auto mm = [&]() {
#pragma unroll
    for (int j = 0; j < 3; ++j) {
      if (j >= 3 - NV) acc[j] = mfma32(xf, wf[j], acc[j]);
      else acc[j] = mfma32(wf[j], xf, acc[j]);
    }
  };
  const int tile = rb * 4 + wid;
  const int voffV = tile * 2048 + 1024 + (t >> 4) * 131072 + h * 256 + swapb23(t & 15) * 16;
  const int voffK = tile * 2048 + swapb23(t) * 32 + h * 16;
  const int voffQ = tile * 1024 + t * 32 + h * 16;
#pragma unroll 1
  for (int c = 0; c < 8; ++c) {
    LDS_BAR();
    const LAS char* sb = lds + (c & 3) * K1_SLOT;
    u32x4 xn[4];
#pragma unroll
    for (int u = 0; u < 4; ++u) {
      xn[u] = ldsr128(sb + xr[u]);
      u32x4 wn[3];
#pragma unroll
      for (int j = 0; j < 3; ++j) wn[j] = ldstr2(sb + wrd + u * 3072 + j * 64, 768);
      mm();
#pragma unroll
      for (int z = 0; z < 3; ++z) { __builtin_amdgcn_sched_group_barrier(0x8, 1, 0); __builtin_amdgcn_sched_group_barrier(0x102, 3, 0); }
      __builtin_amdgcn_sched_barrier(0);
      xf = xn[u];
#pragma unroll
      for (int j = 0; j < 3; ++j) wf[j] = wn[j];
    }
#pragma unroll
    for (int u = 0; u < 4; ++u)
#pragma unroll
      for (int e = 0; e < 4; e += 2) {
        s1a = dot2bf(xn[u][e], ONE2, s1a); s2a = dot2bf(xn[u][e], xn[u][e], s2a);
        s1b = dot2bf(xn[u][e + 1], ONE2, s1b); s2b = dot2bf(xn[u][e + 1], xn[u][e + 1], s2b);
      }
    asm volatile("" : "+v"(s1a), "+v"(s1b), "+v"(s2a), "+v"(s2b));
    __builtin_amdgcn_sched_barrier(0);
  }
  __builtin_amdgcn_s_barrier();
  mm();
  LAS float* scr = (LAS float*)(lds + wid * 4096);
  const LAS float* csum = (const LAS float*)(lds + K1_CS);
  const float s2 = swap32_sum(s2a + s2b);
  const float mean = swap32_sum(s1a + s1b) * (1.f / D_);
  const float var = __builtin_fmaxf(s2 * (1.f / D_) - mean * mean, 0.f);
  const float rstd = __builtin_amdgcn_rsqf(var + LN_EPS);
  if (NV > 0 && h == 0) { scr[t] = rstd; scr[32 + t] = mean * rstd; }
  BAR();
#pragma unroll
  for (int j = 0; j < 3; ++j) {
    const int gt = cb * 3 + j;
    float v[16];
    if (j >= 3 - NV) {
      const float cs = csum[32 * j + t];
#pragma unroll
      for (int gq = 0; gq < 4; ++gq) {
        const f32x4 a = *(const LAS f32x4*)(scr + 8 * gq + 4 * h), b = *(const LAS f32x4*)(scr + 32 + 8 * gq + 4 * h);
#pragma unroll
        for (int e = 0; e < 4; ++e) v[4 * gq + e] = acc[j][4 * gq + e] * a[e] - b[e] * cs;
      }
      const int so = WS_KV + (gt - 32) * 262144;
      ST128(pack8(v), voffV, so); ST128(pack8(v + 8), voffV + 512, so);
    } else {
      const bool isq = gt < 16;
      const float sc = isq ? QSCALE : 1.f;
      const float a = rstd * sc, b = mean * rstd * sc;
#pragma unroll
      for (int gq = 0; gq < 4; ++gq) {
        const f32x4 c4 = *(const LAS f32x4*)(csum + 32 * j + 8 * gq + 4 * h);
#pragma unroll
        for (int e = 0; e < 4; ++e) v[4 * gq + e] = acc[j][4 * gq + e] * a - b * c4[e];
      }
      const int vo = isq ? voffQ : voffK;
      const int so = isq ? WS_QF + gt * 131072 : WS_KV + (gt - 16) * 262144;
      ST128(pack8(v), vo, so); ST128(pack8(v + 8), vo, so + (isq ? 65536 : 131072));
    }
  }
}

__device__ __forceinline__ float rowmax16(const f32x16& s) {
  float a = __builtin_fmaxf(__builtin_fmaxf(s[0], s[1]), s[2]), b = __builtin_fmaxf(__builtin_fmaxf(s[3], s[4]), s[5]);
  a = __builtin_fmaxf(__builtin_fmaxf(a, s[6]), s[7]); b = __builtin_fmaxf(__builtin_fmaxf(b, s[8]), s[9]);
  a = __builtin_fmaxf(__builtin_fmaxf(a, s[10]), s[11]); b = __builtin_fmaxf(__builtin_fmaxf(b, s[12]), s[13]);
  a = __builtin_fmaxf(__builtin_fmaxf(a, s[14]), s[15]);
  return swap32_max(__builtin_fmaxf(a, b));
}
__device__ __forceinline__ f32x8 lo8(const f32x16& s) { return __builtin_shufflevector(s, s, 0, 1, 2, 3, 4, 5, 6, 7); }
__device__ __forceinline__ f32x8 hi8(const f32x16& s) { return __builtin_shufflevector(s, s, 8, 9, 10, 11, 12, 13, 14, 15); }

#define E1(X, i) "v_exp_f32 v[" #X "+" #i "], v[" #X "+" #i "]\n\t"
#define SMF(A, B, off) "v_smfmac_f32_32x32x32_bf16 v[0:15], v[" #A ":" #A "+3], v[" #B "+" #off ":" #B "+" #off "+7], v88\n\t"
#define QK(Y) "v_mfma_f32_32x32x16_bf16 v[" #Y ":" #Y "+15], v[68:71], v[64:67], v[16:31]\n\t"
#define RDV(D, off) "ds_read_b128 v[" #D ":" #D "+3], v90 offset:" #off "\n\t"
#define RDK(off) "ds_read_b128 v[68:71], v89 offset:" #off "\n\t"
#define STEP(X, Y, VA, VPB, VA2, VB2, KOFF, VOFF, VOFF2, PA, PB, EXTRA) \
  PA E1(X, 0) E1(X, 1) SMF(VPB, Y, 8) RDV(VA2, VOFF) \
  PB E1(X, 2) E1(X, 3) E1(X, 4) E1(X, 5) \
  "s_waitcnt lgkmcnt(1)\n\t" QK(Y) RDV(VB2, VOFF2) RDK(KOFF) \
  E1(X, 6) E1(X, 7) E1(X, 8) E1(X, 9) \
  SMF(VA, X, 0) EXTRA \
  E1(X, 10) E1(X, 11) E1(X, 12) E1(X, 13) E1(X, 14) E1(X, 15)
#define ATTN_LOOP(L1, L2, P0, P2, P3) \
        L1 ":\n\t" \
        "s_bitcmp1_b32 0x408, %[cnt]\n\t" \
        "s_cbranch_scc0 " L2 "f\n\t" \
        "s_barrier\n\t" \
        L2 ":\n\t" \
        STEP(32, 48, 72, 84, 80, 84, 4096, 2048, 2560, P0, "", "") \
        STEP(48, 32, 80, 76, 72, 76, 6144, 4096, 4608, "", "", "") \
        STEP(32, 48, 72, 84, 80, 84, 8192, 6144, 6656, P2, "", "v_add_u32 v90, v90, v91\n\t") \
        STEP(48, 32, 80, 76, 72, 76, 10240, 0, 512, "", P3, "v_add_u32 v89, 0x2000, v89\n\ts_add_u32 %[cnt], %[cnt], 1\n\ts_cmp_lg_u32 %[cnt], 16\n\t") \
        "s_cbranch_scc1 " L1 "b\n\t"

__global__ __launch_bounds__(576, 3) void fused_block(const u16* __restrict__ x, const u16* __restrict__ wqkv, const u16* __restrict__ wo,
                                                      const u16* __restrict__ wr, const u16* __restrict__ wg, const u16* __restrict__ wu,
                                                      const u16* __restrict__ wd, char* __restrict__ ws, u16* __restrict__ out) {
  extern __shared__ __attribute__((aligned(16))) char smem[];
  LAS char* lds = (LAS char*)smem;
  const unsigned lds0 = (unsigned)(uintptr_t)lds;
  const unsigned t00 = (unsigned)__builtin_amdgcn_s_memrealtime();
  {
    unsigned long long pc; unsigned s0, s1, v0;
    asm volatile("v_readfirstlane_b32 %1, %4\n\ts_lshr_b32 %2, %5, 3\n\ts_getpc_b64 %0\n\ts_and_b64 %0, %0, -16\n\t"
                 "s_cmp_lt_u32 %1, 64\n\ts_cbranch_scc0 9f\n\t"
                 "s_cmp_lt_u32 %2, %6\n\ts_cbranch_scc0 9f\n\t"
                 "s_lshl_b32 %2, %2, 10\n\tv_lshlrev_b32 %3, 4, %4\n\tv_add_u32 %3, %2, %3\n\t"
                 "s_mov_b32 %1, m0\n\ts_mov_b32 m0, %7\n\ts_nop 0\n\t"
                 "global_load_lds_dwordx4 %3, %0\n\ts_mov_b32 m0, %1\n\t9:"
                 : "=&s"(pc), "=&s"(s0), "=&s"(s1), "=&v"(v0) : "v"(threadIdx.x), "s"(blockIdx.x), "i"(CODE_KB), "s"(lds0 + 131072) : "scc", "memory");
  }
  const int tid = threadIdx.x, lane = tid & 63, t = lane & 31, h = lane >> 5;
  const int wid = __builtin_amdgcn_readfirstlane(tid >> 6);
  const int b = blockIdx.x;
  const int rb1 = 4 * (b & 3) + ((b >> 3) & 3), cb1 = 8 * ((b >> 2) & 1) + (b >> 5);
  const int head = (b & 7) * 4 + ((b >> 3) & 3), qb = b >> 5;
  const int pm = head, cb = qb;
  const unsigned l16 = lane * 16;
  const int lw = wid - 4;
  const int rot = ((((b >> 5) + ((b >> 3) & 3)) & 1)) * 4;
  unsigned xoff[2], woff[3];
  auto stage = [&](int c) {
    const int kc = (c + rot) & 7;
    const char* xs = (const char*)x + kc * 128;
    const char* wsrc = (const char*)wqkv + kc * (64 * 3072);
    const unsigned base = lds0 + (c & 3) * K1_SLOT;
#pragma unroll
    for (int p = 0; p < 4; ++p) glds16(xs + (p >> 1) * 16384, xoff[p & 1], base + lw * 4096 + p * 1024);
#pragma unroll
    for (int p = 0; p < 3; ++p) glds16(wsrc, woff[p], base + 16384 + (lw * 3 + p) * 1024);
  };
  if (lw >= 0 && lw < 4) {
#pragma unroll
    for (int p = 0; p < 2; ++p) {
      const int row = p * 8 + (lane >> 3), slot = lane & 7;
      xoff[p] = (unsigned)((rb1 * 128 + lw * 32 + row) * 1024 + ((slot ^ ((row >> 1) & 7)) * 16));
    }
#pragma unroll
    for (int p = 0; p < 3; ++p) {
      const int idx = (lw * 3 + p) * 64 + lane, k = idx / 12, c = idx - k * 12;
      woff[p] = (unsigned)(k * 3072 + cb1 * 192 + c * 16);
    }
    stage(0); stage(1);
  }
  FENCE();
  const bool isgu = b < NGU, isdn = b >= DN0 && b < DN0 + NDN;
  const __amdgpu_buffer_rsrc_t r_ws = mkrsrc(ws, WS_BYTES), r_out = mkrsrc(out, 2u << 20);

  if (wid == 8) {
    __builtin_amdgcn_s_setprio(3);
    const u32x2 ep = LD64(0, WS_EPOCH);
    const u32x4 w4 = *(const u32x4*)(wr + lane * 8);
    if (lane < 16) { const unsigned v = (lane & 1) ? 0u : ONE2; *(LAS u32x4*)(lds + L_ONE + (lane >> 2) * 2048 + ((lane >> 1) & 1) * 512 + (lane & 1) * 16) = u32x4{v, v, v, v}; }
    if (lane == 16) *(LAS u32x4*)(lds + L_WF) = u32x4{0, 0, 0, 0};
#pragma unroll 1
    for (int c = 0; c < 5; ++c) BAR();
    const unsigned long long e64 = (((unsigned long long)ep[1] << 32) | ep[0]) + 1ull;
    const u32x2 tag = u32x2{(unsigned)e64, (unsigned)(e64 >> 32)};
    float sw;
    sw = dot2bf(w4[0], ONE2, dot2bf(w4[1], ONE2, 0.f)) + dot2bf(w4[2], ONE2, dot2bf(w4[3], ONE2, 0.f));
    sw = sum8(sw); sw += dppf<0x140>(sw);
    sw = (rdlf(sw, 0) + rdlf(sw, 16)) + (rdlf(sw, 32) + rdlf(sw, 48));
    const int hp = head >> 1;
    const int fidx = lane < 16 ? lane * 16 + (16 + hp) / 3 : lane < 32 ? (lane - 16) * 16 + (32 + hp) / 3 : (qb * 2 + (lane & 1)) * 16 + hp / 3;
    const char* kvsrc = ws + WS_KV + (size_t)head * 131072;
    unsigned foff[4];
    const unsigned fstr = isgu ? 32 * F_ * 2 : 32768;
#pragma unroll
    for (int j = 0; j < 4; ++j)
      foff[j] = (isgu ? (unsigned)((lane >> 1) * (F_ * 2) + b * 32) : (unsigned)((lane >> 1) * 1024 + (b - DN0) * 32)) + (lane & 1) * 16 + j * (fstr - 1024);
    unsigned wooff[4];
#pragma unroll
    for (int j = 0; j < 4; ++j) wooff[j] = (unsigned)((lane >> 3) * 1024 + cb * 128 + (((lane & 7) ^ (((lane >> 4) & 1) << 2)) * 16) + j * 7168);
    u32x4 s4 = u32x4{SENT, SENT, SENT, SENT};
    unsigned armoff = (lane >> 3) * 1024 + (lane & 7) * 16, l128 = lane * 128;
    asm volatile("" : "+v"(foff[0]), "+v"(foff[1]), "+v"(foff[2]), "+v"(foff[3]), "+v"(wooff[0]), "+v"(wooff[1]), "+v"(wooff[2]), "+v"(wooff[3]),
                      "+v"(s4), "+v"(armoff), "+v"(l128));
    LDS_BAR();
#pragma unroll 1
    for (int c = 6; c < 10; ++c) BAR();
    BAR();
    if (lane == 0) ST64(tag, 0, WS_FLAG + (rb1 * 16 + cb1) * 128);
    unsigned long long seen = 0;
    auto wait_flags = [&](unsigned long long need) {
#pragma unroll 1
      for (unsigned spins = 0; spins < SPIN_CAP && (seen & need) != need; ++spins) {
        FENCE();
        const u32x2 f = LD64(fidx * 128, WS_FLAG);
        seen = __ballot((f[0] == tag[0]) & (f[1] == tag[1]));
      }
      FENCE();
    };
    wait_flags(0x3000F000Full);
    glds8c(ws + WS_QF + head * 65536 + qb * 8192, l16, lds0 + L_Q);
#pragma unroll
    for (int p = 0; p < 4; ++p) glds8c(kvsrc + p * 8192, l16, lds0 + L_KV + p * 8192);
    VM_BAR(4);
    wait_flags(0xFFFFFFFFull);
#pragma unroll 1
    for (int p = 4; p < 10; ++p) glds8c(kvsrc + p * 8192, l16, lds0 + L_KV + p * 8192);
    WAIT_VM(8);
#pragma unroll 1
    for (int p = 10; p < 16; ++p) glds8c(kvsrc + p * 8192, l16, lds0 + L_KV + p * 8192);
    VM_BAR(0);
    {
#pragma unroll
      for (int e = 0; e < 8; ++e) __builtin_amdgcn_raw_buffer_store_b128(s4, r_ws, l16, WS_OF + (qb * 4 + (e >> 1)) * 65536 + head * 2048 + (e & 1) * 1024, SC1);
#pragma unroll
      for (int e = 0; e < 8; ++e) {
        const int so = (pm * 64 + e * 8) * 1024 + cb * 128;
        __builtin_amdgcn_raw_buffer_store_b128(s4, r_ws, armoff, WS_HN + so, SC1);
        __builtin_amdgcn_raw_buffer_store_b128(s4, r_out, armoff, so, SC1);
      }
      __builtin_amdgcn_raw_buffer_store_b128(s4, r_ws, l16, WS_XS + (pm * 8 + cb) * 1024, SC1);
      if (lane < 8 && b < (22016 + 8192) / 128) __builtin_amdgcn_raw_buffer_store_b128(s4, r_ws, l16, WS_ACT + b * 128, SC1);
    }
    if (lane == 0) __builtin_amdgcn_raw_buffer_store_b32(t00, r_ws, 16, WS_FLAG2 + b * 128, SC1);
    WAIT_VM(0);
    asm volatile("" :: "v"(s4));
    if (lane == 0) ST64(tag, 0, WS_FLAG2 + b * 128);
    auto ffn = [&](int q0, int q1) {
      if (isgu) {
#pragma unroll 1
        for (int p = q0; p < min(q1, 8); ++p) glds4((const char*)(p < 4 ? wg : wu) + (p & 3) * (128 * F_ * 2), foff, lds0 + L_F + p * 4096);
      } else if (isdn) {
#pragma unroll 1
        for (int p = q0; p < min(q1, 10); ++p) glds4((const char*)wd + p * 131072, foff, lds0 + L_F + p * 4096);
        if (q1 > 10) {
#pragma unroll 1
          for (int p = 40; p < 43; ++p) glds16((const char*)wd + p * 32768, foff[0], lds0 + L_F40 + (p - 40) * 2048);
        }
      }
    };
    ffn(0, 11);
    BAR();
    WAIT_VM(24);
#pragma unroll 1
    for (int p = 0; p < 8; ++p) glds4((const char*)wo + p * 32768, wooff, lds0 + L_WO + p * 4096);
    WAIT_VM(24);
#pragma unroll 1
    for (int p = 8; p < 16; ++p) glds4((const char*)wo + p * 32768, wooff, lds0 + L_WO + p * 4096);
#pragma unroll 1
    for (unsigned spins = 0; spins < SPIN_CAP; ++spins) {
      FENCE();
      bool ok = true;
#pragma unroll
      for (int e = 0; e < 4; ++e) { const u32x2 f = LD64(l128, WS_FLAG2 + e * 8192); ok &= (f[0] == tag[0]) & (f[1] == tag[1]); }
      if (__all(ok)) break;
      __builtin_amdgcn_s_sleep(16);
    }
    if (b == 0 && lane == 0) ST64(tag, 0, WS_EPOCH);
    if (b == DN0) {
      int mn = 0, mx = 0;
#pragma unroll
      for (int e = 0; e < 4; ++e) { const int d = (int)(__builtin_amdgcn_raw_buffer_load_b32(r_ws, l128 + 16, WS_FLAG2 + e * 8192, SC1) - t00); mn = min(mn, d); mx = max(mx, d); }
      for (int o = 1; o < 64; o <<= 1) { mn = min(mn, __shfl_xor(mn, o)); mx = max(mx, __shfl_xor(mx, o)); }
      if (lane == 0) { *(LAS unsigned*)(lds + L_ONE + 128) = t00 + mn; *(LAS unsigned*)(lds + L_ONE + 132) = mx - mn; }
      __builtin_amdgcn_s_waitcnt(0xC07F);
    }
    WAIT_VM(0);
    BAR();
    BAR(); BAR();
    u32x4 pv[8];
    {
      const u32x4 own = nosent4(ldsr128(lds + L_ST + l16));
      ST128(own, l16, WS_XS + (pm * 8 + cb) * 1024);
      __builtin_amdgcn_s_sleep(10);
#pragma unroll
      for (int e = 0; e < 8; ++e) pv[e] = LD128(l16, WS_XS + (pm * 8 + e) * 1024);
#pragma unroll 1
      for (unsigned spins = 0; spins < SPIN_CAP; ++spins) {
        bool miss[8], any = false;
#pragma unroll
        for (int e = 0; e < 8; ++e) { miss[e] = __any(umax4(pv[e]) == SENT); any |= miss[e]; }
        if (!any) break;
        FENCE();
#pragma unroll
        for (int e = 0; e < 8; ++e) if (miss[e]) pv[e] = LD128(l16, WS_XS + (pm * 8 + e) * 1024);
      }
    }
#define SUM8(c) (((__uint_as_float(pv[0][c]) + __uint_as_float(pv[1][c])) + (__uint_as_float(pv[2][c]) + __uint_as_float(pv[3][c]))) + \
                 ((__uint_as_float(pv[4][c]) + __uint_as_float(pv[5][c])) + (__uint_as_float(pv[6][c]) + __uint_as_float(pv[7][c]))))
    const float mean = SUM8(0) * (1.f / D_);
    const float rstd = __builtin_amdgcn_rsqf(__builtin_fmaxf(SUM8(1) * (1.f / D_) - mean * mean, 0.f) + LN_EPS);
    if (cb == 0) {
      const unsigned u = __float_as_uint(rstd * (SUM8(2) - mean * sw));
      const unsigned ordv = (u & 0x80000000u) ? ~u : (u | 0x80000000u);
      __builtin_amdgcn_raw_buffer_store_b32(max((ordv >> 1) & 0x7FFFF800u, 0x800u) | (unsigned)(2047 - (pm * 64 + lane)), r_ws, lane * 4, WS_KEY + pm * 256, SC1);
    }
    *(LAS u32x2*)(lds + L_ST + 1024 + lane * 8) = u32x2{__float_as_uint(rstd), __float_as_uint(mean * rstd)};
    LDS_BAR();
    return;
  }

  if (wid >= 4) {
    const int wrd = 16384 + lw * 3072 + (8 * h + ((lane >> 2) & 3)) * 192 + 32 * ((lane >> 4) & 1) + 8 * (lane & 3);
    float cs[3] = {0.f, 0.f, 0.f};
#pragma unroll 1
    for (int c = 0; c < 8; ++c) {
      if (c < 7) WAIT_BAR(7); else WAIT_BAR(0);
      const LAS char* sb = lds + (c & 3) * K1_SLOT + wrd;
      u32x4 wf[3];
#pragma unroll
      for (int j = 0; j < 3; ++j) wf[j] = ldstr2(sb + j * 64, 768);
      if (c < 6) stage(c + 2);
#pragma unroll
      for (int e = 0; e < 4; ++e)
#pragma unroll
        for (int j = 0; j < 3; ++j) cs[j] = dot2bf(wf[j][e], ONE2, cs[j]);
    }
#pragma unroll
    for (int j = 0; j < 3; ++j) *(LAS float*)(lds + K1_CSP + (lw * 2 + h) * 384 + (32 * j + t) * 4) = cs[j];
    LDS_BAR();
    if (lw == 0 && lane < 24) {
      const LAS char* pp = lds + K1_CSP + l16;
      f32x4 sacc = *(const LAS f32x4*)pp;
#pragma unroll
      for (int e = 1; e < 8; ++e) sacc += *(const LAS f32x4*)(pp + e * 384);
      *(LAS f32x4*)(lds + K1_CS + l16) = sacc;
    }
    LDS_BAR();
    BAR();
  } else {
    if (cb1 < 10) qkv_body<0>(r_ws, lds, rb1, cb1, wid);
    else if (cb1 == 10) qkv_body<1>(r_ws, lds, rb1, cb1, wid);
    else qkv_body<3>(r_ws, lds, rb1, cb1, wid);
    WAIT_BAR(0);
  }

  const bool onel = (lane & 16) != 0;
  unsigned kaddr = lds0 + L_KV + t * 32 + h * 16;
  unsigned vaddr = lds0 + (onel ? L_ONE + ((lane & 11) ? 16 : 0) : L_KV + 1024 + h * 256 + (lane & 15) * 16);
  const int vstep = onel ? 0 : 8192;
  const int IDX = 0xDDDDDDDD;
  const int row = pm * 64 + wid * 8 + (lane >> 3), col = cb * 64 + (lane & 7) * 8;
  f32x16 negm, sx, sy = f32x16{}, o = f32x16{};
  u32x4 vpb = u32x4{0, 0, 0, 0};
  asm volatile("" : "+{v[0:15]}"(o), "+{v[48:63]}"(sy), "+{v[84:87]}"(vpb));

  BAR();
  const u32x4 xres = *(const u32x4*)(x + row * D_ + col), wrv = *(const u32x4*)(wr + col);
  const int mh = wid >> 2, kq = wid & 3;
  const u32x4 qf = ldsr128(lds + L_Q + kq * 2048 + mh * 1024 + t * 32 + h * 16);
  int ostoff = t * 32 + h * 16 + WS_OF + (qb * 4 + kq) * 65536 + head * 2048 + mh * 1024;
  asm volatile("" : "+v"(ostoff));
  u32x4 kn, va, vb;
  {
    const u32x4 k0 = ldsr128((const LAS char*)(uintptr_t)kaddr);
    kn = ldsr128((const LAS char*)(uintptr_t)kaddr + 2048);
    va = ldsr128((const LAS char*)(uintptr_t)vaddr); vb = ldsr128((const LAS char*)(uintptr_t)vaddr + 512);
    const float m = -rowmax16(mfma32(k0, qf, f32x16{}));
#pragma unroll
    for (int r = 0; r < 16; ++r) negm[r] = m;
    sx = mfma32(k0, qf, negm);
  }
  {
    unsigned cnt;
    asm volatile(
        "s_mov_b32 %[cnt], 0\n\t"
        "s_nop 7\n\t"
        "s_cmp_gt_u32 %[wid], 3\n\t"
        "s_cbranch_scc1 3f\n\t"
        "s_setprio 1\n\t"
        ATTN_LOOP("1", "2", "", "", "")
        "s_setprio 0\n\t"
        "s_branch 5f\n\t"
        ATTN_LOOP("3", "4", "s_setprio 2\n\t", "", "s_setprio 0\n\t")
        "5:\n\t"
        "s_nop 1\n\t"
        SMF(84, 48, 8)
        "s_waitcnt lgkmcnt(0)\n\t"
        "s_nop 11\n\t"
        : "+{v[0:15]}"(o), "+{v[32:47]}"(sx), "+{v[48:63]}"(sy), "+{v[68:71]}"(kn), "+{v[72:75]}"(va), "+{v[76:79]}"(vb),
          "+{v[84:87]}"(vpb), "+{v89}"(kaddr), "+{v90}"(vaddr), [cnt] "=&s"(cnt)
        : "{v[16:31]}"(negm), "{v[64:67]}"(qf), "{v88}"(IDX), "{v91}"(vstep), [wid] "s"(wid)
        : "v80", "v81", "v82", "v83", "scc", "memory");
  }
  auto normalise = [&]() {
    const float inv = __builtin_amdgcn_rcpf(o[8]);
    return nosent4(u32x4{pkbf(o[0] * inv, o[1] * inv), pkbf(o[2] * inv, o[3] * inv), pkbf(o[4] * inv, o[5] * inv), pkbf(o[6] * inv, o[7] * inv)});
  };
  u32x4 opk = normalise();
  asm volatile("" : "+v"(opk));
  {
    float c0, c1, c2, c3;
    asm("v_add_f32 %0, |%1|, |%2|" : "=v"(c0) : "v"(o[0]), "v"(o[1])); asm("v_add_f32 %0, |%1|, |%2|" : "=v"(c1) : "v"(o[2]), "v"(o[3]));
    asm("v_add_f32 %0, |%1|, |%2|" : "=v"(c2) : "v"(o[4]), "v"(o[5])); asm("v_add_f32 %0, |%1|, |%2|" : "=v"(c3) : "v"(o[6]), "v"(o[7]));
    const float chk = (c0 + c1) + (c2 + c3);
    const bool bad = !(o[8] < 1.8446744e19f) || !(chk < 3.0e38f);
    if (__builtin_expect(__any(bad), 0)) {
      float m = -INFINITY;
      o = f32x16{};
      const int kqo = head * 131072 + t * 32 + h * 16, vq = head * 131072 + 1024 + h * 256 + (lane & 15) * 16;
      const u32x4 ones = u32x4{ONE2, ONE2, ONE2, ONE2};
#pragma unroll 1
      for (int i = 0; i < 64; ++i) {
        f32x16 s = mfma32(LD128(kqo, WS_KV + i * 2048), qf, f32x16{});
        u32x4 v0 = LD128(vq, WS_KV + i * 2048), v1 = LD128(vq + 512, WS_KV + i * 2048);
        if (onel) { v0 = ones; v1 = ones; }
        const float mn = __builtin_fmaxf(m, rowmax16(s));
        const float al = __builtin_amdgcn_exp2f(m - mn);
        m = mn;
#pragma unroll
        for (int r = 0; r < 16; ++r) { o[r] *= al; s[r] = __builtin_amdgcn_exp2f(s[r] - mn); }
        o = smfmac32(v0, lo8(s), o, IDX);
        o = smfmac32(v1, hi8(s), o, IDX);
      }
      opk = normalise();
    }
  }
  __builtin_amdgcn_raw_buffer_store_b128(opk, r_ws, ostoff, 0, SC1);
  BAR();

  const int g = lane >> 4, q = (lane >> 2) & 3, p4 = lane & 3;
  f32x4 a0 = f32x4{bflo(xres[0]), bfhi(xres[0]), bflo(xres[1]), bfhi(xres[1])}, a1 = f32x4{bflo(xres[2]), bfhi(xres[2]), bflo(xres[3]), bfhi(xres[3])};
  unsigned ra0, ra1;
  {
    const int lr = wid * 8 + (lane >> 3);
    const int sl = ((lane & 7) ^ ((lr & 7) | ((lr & 1) << 3))) * 16;
    const unsigned rbase = lds0 + (mh ? L_WO : L_PA) + (lr & 31) * 256;
    ra0 = rbase + sl; ra1 = rbase + (sl ^ 128);
  }
  LAS unsigned* cand = (LAS unsigned*)(lds + L_SEL);
  {
    u32x4 of[8], wf0[8], wf1[8];
    const int flip = ((q >> 1) & 1) << 6;
    const LAS char* wb = lds + L_WO + kq * 16384 + (8 * h + q) * 128 + 32 * (g & 1) + 8 * p4;
#pragma unroll
    for (int e = 0; e < 8; ++e) { wf0[e] = ldstr2(wb + flip + e * 2048, 512); wf1[e] = ldstr2(wb + (64 ^ flip) + e * 2048, 512); }
    const int ooff = t * 32 + h * 16, osoff = WS_OF + pm * 65536 + kq * 16384 + mh * 1024;
    unsigned pa[8];
    {
      const unsigned rb = lds0 + (mh ? L_WO + kq * 16384 : L_PA + kq * 8192) + t * 256;
      const int swz = (t & 7) | ((t & 1) << 3);
#pragma unroll
      for (int gq = 0; gq < 8; ++gq) { pa[gq] = rb + (((h * 8 + gq) ^ swz) * 16); asm volatile("" : "+v"(pa[gq])); }
    }
    asm volatile("" : "+v"(a0), "+v"(a1), "+v"(ra0), "+v"(ra1));
    __builtin_amdgcn_s_waitcnt(0xC07F);
    FENCE();
    if (!mh) *(volatile LAS unsigned*)(lds + L_WF + kq * 4) = 1u;
    else {
#pragma unroll 1
      for (unsigned spins = 0; spins < (1u << 24); ++spins) {
        if (__builtin_amdgcn_readfirstlane(*(volatile LAS unsigned*)(lds + L_WF + kq * 4))) break;
        __builtin_amdgcn_s_sleep(1);
      }
    }
    FENCE();
#pragma unroll
    for (int e = 0; e < 8; ++e) of[e] = LD128(ooff, osoff + e * 2048);
    f32x16 acc0 = f32x16{}, acc1 = f32x16{};
#pragma unroll 1
    for (unsigned spins = 0; spins < SPIN_CAP; ++spins) {
      bool miss[8], any = false;
#pragma unroll
      for (int e = 0; e < 8; ++e) { miss[e] = __any(umax4(of[e]) == SENT); any |= miss[e]; }
      if (!any) break;
      FENCE();
#pragma unroll
      for (int e = 0; e < 8; ++e) if (miss[e]) of[e] = LD128(ooff, osoff + e * 2048);
    }
#pragma unroll
    for (int e = 0; e < 8; ++e) acc0 = mfma32(wf0[e], of[e], acc0);
    __builtin_amdgcn_sched_barrier(0);
#pragma unroll
    for (int e = 0; e < 8; ++e) {
      acc1 = mfma32(wf1[e], of[e], acc1);
      if (e >= 3 && e < 7) *(LAS f32x4*)(uintptr_t)pa[e - 3] = f32x4{acc0[4 * e - 12], acc0[4 * e - 11], acc0[4 * e - 10], acc0[4 * e - 9]};
      asm volatile("" : "+v"(acc1));
      __builtin_amdgcn_sched_barrier(0);
    }
#pragma unroll
    for (int gq = 0; gq < 4; ++gq)
      *(LAS f32x4*)(uintptr_t)pa[4 + gq] = f32x4{acc1[4 * gq], acc1[4 * gq + 1], acc1[4 * gq + 2], acc1[4 * gq + 3]};
  }
  LDS_BAR();
  float hv[8];
  {
    const LAS char* r0 = (const LAS char*)(uintptr_t)ra0;
    const LAS char* r1 = (const LAS char*)(uintptr_t)ra1;
    f32x4 t0[4], t1[4];
    if (mh) {
#pragma unroll
      for (int e = 0; e < 4; ++e) { t0[e] = *(const LAS f32x4*)(r0 + e * 16384); t1[e] = *(const LAS f32x4*)(r1 + e * 16384); }
    } else {
#pragma unroll
      for (int e = 0; e < 4; ++e) { t0[e] = *(const LAS f32x4*)(r0 + e * 8192); t1[e] = *(const LAS f32x4*)(r1 + e * 8192); }
    }
    __builtin_amdgcn_sched_barrier(0);
    a0 += (t0[0] + t0[1]) + (t0[2] + t0[3]);
    a1 += (t1[0] + t1[1]) + (t1[2] + t1[3]);
    const u32x4 hb = u32x4{pkbf(a0[0], a0[1]), pkbf(a0[2], a0[3]), pkbf(a1[0], a1[1]), pkbf(a1[2], a1[3])};
    float s1 = 0.f, s2 = 0.f, sd = 0.f;
#pragma unroll
    for (int e = 0; e < 4; ++e) { s1 = dot2bf(hb[e], ONE2, s1); s2 = dot2bf(hb[e], hb[e], s2); sd = dot2bf(hb[e], wrv[e], sd); }
    s1 = sum8(s1); s2 = sum8(s2); sd = sum8(sd);
    if ((lane & 7) == 0) *(LAS u32x4*)(lds + L_ST + wid * 128 + lane * 2) = u32x4{__float_as_uint(s1), __float_as_uint(s2), __float_as_uint(sd), 0u};
    LDS_BAR();
    if (tid < 80) cand[tid] = 0;
#pragma unroll
    for (int e = 0; e < 4; ++e) { hv[2 * e] = bflo(hb[e]); hv[2 * e + 1] = bfhi(hb[e]); }
  }
  const int trd = (8 * g + q) * 32 + 8 * p4;
  u32x4 fw[8];
  if (isgu || isdn) {
#pragma unroll
    for (int p = 0; p < 8; ++p) {
      const int po = isgu ? L_F + ((p >> 2) * 16 + (wid & 3) * 4 + (p & 3)) * 1024 : p < 5 ? L_F + (p * 8 + wid) * 1024 : L_F40 + min(wid, 2) * 2048;
      fw[p] = ldstr2(lds + po + trd, 128);
    }
  }
  u32x4 k4;
  LDS_BAR();
  {
    const u32x2 st = *(const LAS u32x2*)(lds + L_ST + 1024 + wid * 64 + (lane >> 3) * 8);
    const float rstd = __uint_as_float(st[0]), mr = __uint_as_float(st[1]);
    u32x4 hp, op;
#pragma unroll
    for (int e = 0; e < 4; ++e) {
      hp[e] = pkbf(hv[2 * e] * rstd - mr, hv[2 * e + 1] * rstd - mr);
      op[e] = pkbf(hv[2 * e] + bflo(hp[e]), hv[2 * e + 1] + bfhi(hp[e]));
    }
    ST128(nosent4(hp), row * 1024 + col * 2, WS_HN);
    __builtin_amdgcn_raw_buffer_store_b128(nosent4(op), r_out, row * 1024 + col * 2, 0, SC1);
  }
  if (!isgu && !isdn) return;
#pragma unroll 1
  for (unsigned spins = 0; spins < SPIN_CAP; ++spins) {
    FENCE();
    k4 = LD128(tid * 16, WS_KEY);
    if (__all(umax4(k4) != SENT)) break;
  }
  {
    unsigned m = umax4(k4);
    m = max(m, dppu<0xB1>(m)); m = max(m, dppu<0x4E>(m)); m = max(m, dppu<0x141>(m)); m = max(m, dppu<0x140>(m));
    m = max(m, dppu<0x142>(m)); m = max(m, dppu<0x143>(m));
    if (lane == 63) cand[64 + wid] = m;
  }
  LDS_BAR();
  {
    const u32x4 g0 = *(LAS u32x4*)(cand + 64), g1 = *(LAS u32x4*)(cand + 68);
    const unsigned T = min(min(min(g0[0], g0[1]), min(g0[2], g0[3])), min(min(g1[0], g1[1]), min(g1[2], g1[3])));
    const unsigned c0 = k4[0] >= T, c1 = k4[1] >= T, c2 = k4[2] >= T, c3 = k4[3] >= T;
    const unsigned cnt = (c0 + c1) + (c2 + c3);
    if (cnt) {
      unsigned pos;
      asm volatile("ds_add_rtn_u32 %0, %1, %2\n\ts_waitcnt lgkmcnt(0)" : "=v"(pos) : "v"((unsigned)(lds0 + L_SEL + 72 * 4)), "v"(cnt) : "memory");
      cand[c0 && pos < 64 ? pos : 73] = k4[0]; pos += c0;
      cand[c1 && pos < 64 ? pos : 73] = k4[1]; pos += c1;
      cand[c2 && pos < 64 ? pos : 73] = k4[2]; pos += c2;
      cand[c3 && pos < 64 ? pos : 73] = k4[3];
    }
  }
  LDS_BAR();
  LAS unsigned* sel = cand + 80 + wid * 8;
  {
    unsigned nv = cand[72];
    unsigned c = cand[t];
    u32x4 o4[4];
#pragma unroll
    for (int e = 0; e < 4; ++e) o4[e] = *(LAS u32x4*)(cand + h * 16 + e * 4);
    asm volatile("" : "+v"(nv), "+v"(c), "+v"(o4[0]), "+v"(o4[1]), "+v"(o4[2]), "+v"(o4[3]));
    const unsigned n = __builtin_amdgcn_readfirstlane(nv);
    if (n <= 64 && wid >= (isgu ? 4 : 1)) {
      if (isgu) return;
    } else if (__builtin_expect(n <= 32, 1)) {
      unsigned ba = 0, bb = 0;
#pragma unroll
      for (int e = 0; e < 4; ++e) {
        ba = __builtin_amdgcn_alignbit(ba, c - o4[e][0], 31); bb = __builtin_amdgcn_alignbit(bb, c - o4[e][1], 31);
        ba = __builtin_amdgcn_alignbit(ba, c - o4[e][2], 31); bb = __builtin_amdgcn_alignbit(bb, c - o4[e][3], 31);
      }
      unsigned rk = __builtin_popcount(ba) + __builtin_popcount(bb);
      auto r2 = __builtin_amdgcn_permlane32_swap(rk, rk, false, false);
      rk = r2[0] + r2[1];
      if (rk < 8) sel[rk] = c;
    } else if (n <= 64) {
      const unsigned c2 = cand[32 + t];
      unsigned ba = 0, bb = 0;
#pragma unroll
      for (int e = 0; e < 8; ++e) {
        const u32x4 v = *(LAS u32x4*)(cand + h * 32 + e * 4);
#pragma unroll
        for (int j = 0; j < 4; ++j) { ba = __builtin_amdgcn_alignbit(ba, c - v[j], 31); bb = __builtin_amdgcn_alignbit(bb, c2 - v[j], 31); }
      }
      unsigned ra = __builtin_popcount(ba), rb2 = __builtin_popcount(bb);
      auto r1 = __builtin_amdgcn_permlane32_swap(ra, ra, false, false);
      auto r2 = __builtin_amdgcn_permlane32_swap(rb2, rb2, false, false);
      ra = r1[0] + r1[1]; rb2 = r2[0] + r2[1];
      if (ra < 8) sel[ra] = c;
      if (rb2 < 8) sel[rb2] = c2;
    } else {
      LAS unsigned* all = cand + 256;
      *(LAS u32x4*)(all + tid * 4) = k4;
      LDS_BAR();
      unsigned rk[4] = {0, 0, 0, 0};
#pragma unroll 1
      for (int j = 0; j < 2048; ++j) {
        const unsigned v = all[j];
#pragma unroll
        for (int e = 0; e < 4; ++e) rk[e] += v > k4[e];
      }
#pragma unroll
      for (int e = 0; e < 4; ++e) if (rk[e] < 8) cand[160 + rk[e]] = k4[e];
      LDS_BAR();
      sel = cand + 160;
      if (isgu && wid >= 4) return;
    }
  }
  const int tok = lane & 15;
  const unsigned key = sel[tok & 7];
  const int trow = 2047 - (int)(key & 2047u);
  LAS f32x4* red = (LAS f32x4*)(lds + L_WO);
  if (isgu) {
    u32x4 xb[4];
#pragma unroll 1
    for (unsigned spins = 0; spins < SPIN_CAP; ++spins) {
      FENCE();
#pragma unroll
      for (int u = 0; u < 4; ++u) xb[u] = LD128(trow * 1024 + u * 64 + g * 16, WS_HN + wid * 256);
      if (__all(max(max(umax4(xb[0]), umax4(xb[1])), max(umax4(xb[2]), umax4(xb[3]))) != SENT)) break;
    }
    f32x4 ag = f32x4{}, au = f32x4{};
#pragma unroll
    for (int u = 0; u < 4; ++u) { ag = mfma16(fw[u], xb[u], ag); au = mfma16(fw[4 + u], xb[u], au); }
    const f32x4 mine = tok < 8 ? ag : au;
    if (wid != 0) red[wid * 64 + lane] = mine;
    LDS_BAR();
    if (wid == 0) {
      const f32x4 s = (mine + red[64 + lane]) + (red[128 + lane] + red[192 + lane]);
      float av[4];
#pragma unroll
      for (int r = 0; r < 4; ++r) {
        const float gu = s[r] * dppf<0x128>(s[r]);
        av[r] = gu * __builtin_amdgcn_rcpf(1.f + __builtin_amdgcn_exp2f(s[r] * -1.4426950408889634f));
      }
      if (tok < 8) ST64((u32x2{nosent(pkbf(av[0], av[1])), nosent(pkbf(av[2], av[3]))}), tok * 32 + g * 8, WS_ACT + b * 256);
    }
    return;
  }
  const int nb = b - DN0;
  u32x2 o0 = u32x2{0, 0};
  float sg = 0.f;
  if (wid == 0) {
#pragma unroll 1
    for (unsigned spins = 0; spins < SPIN_CAP; ++spins) {
      FENCE();
      o0 = __builtin_amdgcn_raw_buffer_load_b64(r_out, trow * 1024 + g * 8, nb * 32, SC1);
      if (__all(max(o0[0], o0[1]) != SENT)) break;
    }
    const unsigned ov = ((key & 0x7FFFF800u) << 1) | 0x800u;
    const float r = rbf(__uint_as_float((ov & 0x80000000u) ? (ov & 0x7FFFFFFFu) : ~ov));
    sg = rbf(__builtin_amdgcn_rcpf(1.f + __expf(-r)));
  }
  const __amdgpu_buffer_rsrc_t r_act = mkrsrc(ws + WS_ACT, 22016);
  u32x4 ab[6];
  const int aoff = wid * 512 + (g >> 1) * 256 + (tok & 7) * 32 + (g & 1) * 16;
#pragma unroll
  for (int p = 0; p < 6; ++p) ab[p] = __builtin_amdgcn_raw_buffer_load_b128(r_act, aoff, p * 4096, SC1);
#pragma unroll 1
  for (unsigned spins = 0; spins < SPIN_CAP; ++spins) {
    bool miss[6], any = false;
#pragma unroll
    for (int p = 0; p < 6; ++p) { miss[p] = __any(umax4(ab[p]) == SENT); any |= miss[p]; }
    if (!any) break;
    FENCE();
#pragma unroll
    for (int p = 0; p < 6; ++p) if (miss[p]) ab[p] = __builtin_amdgcn_raw_buffer_load_b128(r_act, aoff, p * 4096, SC1);
  }
  f32x4 ay = f32x4{};
#pragma unroll
  for (int p = 0; p < 6; ++p) ay = mfma16(fw[p], ab[p], ay);
  red[wid * 64 + lane] = ay;
  LDS_BAR();
  if (wid == 0 && tok < 8) {
    ay = ((ay + red[64 + lane]) + (red[128 + lane] + red[192 + lane])) + ((red[256 + lane] + red[320 + lane]) + (red[384 + lane] + red[448 + lane]));
    __builtin_amdgcn_raw_buffer_store_b64(u32x2{pkbf(fmaf(ay[0], sg, bflo(o0[0])), fmaf(ay[1], sg, bfhi(o0[0]))), pkbf(fmaf(ay[2], sg, bflo(o0[1])), fmaf(ay[3], sg, bfhi(o0[1])))}, r_out,
                                          trow * 1024 + g * 8, nb * 32, SC1);
  }
  if (DIAG > 0 && wid == 0 && b == DN0) {
    const unsigned now = (unsigned)__builtin_amdgcn_s_memrealtime();
    const unsigned tmin = *(volatile LAS unsigned*)(lds + L_ONE + 128), skew = *(volatile LAS unsigned*)(lds + L_ONE + 132);
    const unsigned d = DIAG == 1 ? 4u * min(now - tmin, 20000u) : 20u * min(skew, 2000u);
    const unsigned dd = __builtin_amdgcn_readfirstlane(d);
    while ((unsigned)__builtin_amdgcn_s_memrealtime() - now < dd) __builtin_amdgcn_s_sleep(1);
  }
}

extern "C" void kernel_launch(void* const* d_in, const int* in_sizes, int n_in, void* d_out, int out_size, void* d_ws, size_t ws_size,
                              hipStream_t stream) {
  (void)hipFuncSetAttribute((const void*)fused_block, hipFuncAttributeMaxDynamicSharedMemorySize, LDS_BYTES);
  fused_block<<<256, 576, LDS_BYTES, stream>>>((const u16*)d_in[0], (const u16*)d_in[1], (const u16*)d_in[2], (const u16*)d_in[4],
                                               (const u16*)d_in[5], (const u16*)d_in[6], (const u16*)d_in[7], (char*)d_ws, (u16*)d_out);
}
```

```cpp
#include <hip/hip_runtime.h>
#include <hip/hip_bf16.h>

typedef unsigned short u16;
typedef __bf16 bf16x2_t __attribute__((ext_vector_type(2)));
typedef __bf16 bf16x8_t __attribute__((ext_vector_type(8)));
typedef __bf16 bf16x16_t __attribute__((ext_vector_type(16)));
typedef short s16x4 __attribute__((ext_vector_type(4)));
typedef float f32x4 __attribute__((ext_vector_type(4)));
typedef float f32x8 __attribute__((ext_vector_type(8)));
typedef float f32x16 __attribute__((ext_vector_type(16)));
typedef unsigned u32x2 __attribute__((ext_vector_type(2)));
typedef unsigned u32x4 __attribute__((ext_vector_type(4)));
#define LAS __attribute__((address_space(3)))

constexpr int D_ = 512, F_ = 1376;
constexpr unsigned SENT = 0xFFFFFFFFu;
constexpr float LN_EPS = 1e-5f;
constexpr float QSCALE = 0.25f * 1.4426950408889634f;
constexpr unsigned SPIN_CAP = 1u << 16;
constexpr int SC1 = 16;
constexpr unsigned ONE2 = 0x3F803F80u;
constexpr int CODE_KB = 25;

constexpr unsigned WS_KV = 0;
constexpr unsigned WS_QF = 4u << 20;
constexpr unsigned WS_OF = 6u << 20;
constexpr unsigned WS_HN = 8u << 20;
constexpr unsigned WS_XS = 10u << 20;
constexpr unsigned WS_ACT = WS_XS + (256u << 10);
constexpr unsigned WS_KEY = WS_ACT + 22016;
constexpr unsigned WS_FLAG = WS_XS + (320u << 10);
constexpr unsigned WS_FLAG2 = WS_FLAG + 32768;
constexpr unsigned WS_EPOCH = WS_FLAG2 + 32768;
constexpr unsigned WS_BYTES = WS_EPOCH + 128;

__device__ __forceinline__ f32x16 mfma32(u32x4 a, u32x4 b, f32x16 c) {
  return __builtin_amdgcn_mfma_f32_32x32x16_bf16(__builtin_bit_cast(bf16x8_t, a), __builtin_bit_cast(bf16x8_t, b), c, 0, 0, 0);
}
__device__ __forceinline__ f32x4 mfma16(u32x4 a, u32x4 b, f32x4 c) {
  return __builtin_amdgcn_mfma_f32_16x16x32_bf16(__builtin_bit_cast(bf16x8_t, a), __builtin_bit_cast(bf16x8_t, b), c, 0, 0, 0);
}
__device__ __forceinline__ f32x16 smfmac32(u32x4 a, f32x8 b, f32x16 c, int idx) {
  return __builtin_amdgcn_smfmac_f32_32x32x32_bf16(__builtin_bit_cast(bf16x8_t, a), __builtin_bit_cast(bf16x16_t, b), c, idx, 0, 0);
}
__device__ __forceinline__ float dot2bf(unsigned a, unsigned b, float c) {
  return __builtin_amdgcn_fdot2_f32_bf16(__builtin_bit_cast(bf16x2_t, a), __builtin_bit_cast(bf16x2_t, b), c, false);
}
__device__ __forceinline__ float swap32_sum(float v) {
  auto r = __builtin_amdgcn_permlane32_swap(__float_as_uint(v), __float_as_uint(v), false, false);
  return __uint_as_float(r[0]) + __uint_as_float(r[1]);
}
__device__ __forceinline__ float swap32_max(float v) {
  auto r = __builtin_amdgcn_permlane32_swap(__float_as_uint(v), __float_as_uint(v), false, false);
  return __builtin_fmaxf(__uint_as_float(r[0]), __uint_as_float(r[1]));
}
__device__ __forceinline__ unsigned pkbf(float lo, float hi) {
  bf16x2_t v; v[0] = (__bf16)lo; v[1] = (__bf16)hi; return __builtin_bit_cast(unsigned, v);
}
__device__ __forceinline__ float bflo(unsigned u) { return __uint_as_float(u << 16); }
__device__ __forceinline__ float bfhi(unsigned u) { return __uint_as_float(u & 0xFFFF0000u); }
__device__ __forceinline__ float rbf(float f) { return (float)(__bf16)f; }
__device__ __forceinline__ void glds16(const void* sbase, unsigned voff, unsigned lds_base) {
  unsigned sv;
  asm volatile("s_mov_b32 %0, m0\n\ts_mov_b32 m0, %3\n\ts_nop 0\n\tglobal_load_lds_dwordx4 %1, %2\n\ts_mov_b32 m0, %0"
               : "=&s"(sv) : "v"(voff), "s"(sbase), "s"(lds_base) : "memory");
}
__device__ __forceinline__ void glds8c(const void* sbase, unsigned voff, unsigned lds_base) {
  unsigned sv;
  asm volatile("s_mov_b32 %0, m0\n\ts_mov_b32 m0, %3\n\ts_nop 0\n\t"
               "global_load_lds_dwordx4 %1, %2 offset:-4096 sc1\n\tglobal_load_lds_dwordx4 %1, %2 offset:-3072 sc1\n\t"
               "global_load_lds_dwordx4 %1, %2 offset:-2048 sc1\n\tglobal_load_lds_dwordx4 %1, %2 offset:-1024 sc1\n\t"
               "global_load_lds_dwordx4 %1, %2 sc1\n\tglobal_load_lds_dwordx4 %1, %2 offset:1024 sc1\n\t"
               "global_load_lds_dwordx4 %1, %2 offset:2048 sc1\n\tglobal_load_lds_dwordx4 %1, %2 offset:3072 sc1\n\t"
               "s_mov_b32 m0, %0"
               : "=&s"(sv) : "v"(voff), "s"((const char*)sbase + 4096), "s"(lds_base + 4096) : "memory");
}
__device__ __forceinline__ void glds4(const void* sbase, const unsigned (&voff)[4], unsigned lds_base) {
  unsigned sv;
  asm volatile("s_mov_b32 %0, m0\n\ts_mov_b32 m0, %6\n\ts_nop 0\n\t"
               "global_load_lds_dwordx4 %1, %5\n\tglobal_load_lds_dwordx4 %2, %5 offset:1024\n\t"
               "global_load_lds_dwordx4 %3, %5 offset:2048\n\tglobal_load_lds_dwordx4 %4, %5 offset:3072\n\t"
               "s_mov_b32 m0, %0"
               : "=&s"(sv) : "v"(voff[0]), "v"(voff[1]), "v"(voff[2]), "v"(voff[3]), "s"(sbase), "s"(lds_base) : "memory");
}
#define FENCE() asm volatile("" ::: "memory")
#define WAIT_BAR(N) asm volatile("s_waitcnt vmcnt(" #N ") lgkmcnt(0)\n\ts_barrier" ::: "memory")
#define VM_BAR(N) asm volatile("s_waitcnt vmcnt(" #N ")\n\ts_barrier" ::: "memory")
#define LDS_BAR() do { FENCE(); __builtin_amdgcn_s_waitcnt(0xC07F); __builtin_amdgcn_s_barrier(); FENCE(); } while (0)
#define BAR() do { FENCE(); __builtin_amdgcn_s_barrier(); FENCE(); } while (0)
#define WAIT_VM(N) asm volatile("s_waitcnt vmcnt(" #N ")" ::: "memory")
#define LD128(off, soff) __builtin_amdgcn_raw_buffer_load_b128(r_ws, off, soff, SC1)
#define LD64(off, soff) __builtin_amdgcn_raw_buffer_load_b64(r_ws, off, soff, SC1)
#define ST128(v, off, soff) __builtin_amdgcn_raw_buffer_store_b128(v, r_ws, (off) + (soff), 0, SC1)
#define ST64(v, off, soff) __builtin_amdgcn_raw_buffer_store_b64(v, r_ws, off, soff, SC1)
__device__ __forceinline__ u32x4 ldsr128(const LAS char* p) { return *(const LAS u32x4*)p; }
__device__ __forceinline__ u32x2 ldstr(const LAS char* p) {
  return __builtin_bit_cast(u32x2, __builtin_amdgcn_ds_read_tr16_b64_v4i16((LAS s16x4*)p));
}
__device__ __forceinline__ u32x4 ldstr2(const LAS char* p, int off2) { const u32x2 a = ldstr(p), b = ldstr(p + off2); return u32x4{a[0], a[1], b[0], b[1]}; }
__device__ __forceinline__ int swapb23(int v) { return (v & ~12) | ((v & 4) << 1) | ((v & 8) >> 1); }
__device__ __forceinline__ unsigned nosent(unsigned v) { return min(v, 0xFFFFFFFEu); }
__device__ __forceinline__ u32x4 nosent4(u32x4 v) { return u32x4{nosent(v[0]), nosent(v[1]), nosent(v[2]), nosent(v[3])}; }
__device__ __forceinline__ unsigned umax4(u32x4 v) { return max(max(v[0], v[1]), max(v[2], v[3])); }
__device__ __forceinline__ __amdgpu_buffer_rsrc_t mkrsrc(const void* p, unsigned bytes) {
  return __builtin_amdgcn_make_buffer_rsrc((void*)p, 0, bytes, 0x00020000);
}
template <int CTRL> __device__ __forceinline__ float dppf(float v) {
  return __uint_as_float(__builtin_amdgcn_update_dpp(0, __float_as_uint(v), CTRL, 0xF, 0xF, true));
}
template <int CTRL> __device__ __forceinline__ unsigned dppu(unsigned v) { return __builtin_amdgcn_update_dpp(0, v, CTRL, 0xF, 0xF, true); }
__device__ __forceinline__ unsigned rdl(unsigned v, int l) { return (unsigned)__builtin_amdgcn_readlane((int)v, l); }
__device__ __forceinline__ float rdlf(float v, int l) { return __uint_as_float(rdl(__float_as_uint(v), l)); }
__device__ __forceinline__ float sum8(float v) { v += dppf<0xB1>(v); v += dppf<0x4E>(v); v += dppf<0x141>(v); return v; }
__device__ __forceinline__ u32x4 pack8(const float* v) { return u32x4{pkbf(v[0], v[1]), pkbf(v[2], v[3]), pkbf(v[4], v[5]), pkbf(v[6], v[7])}; }

constexpr int K1_SLOT = 16384 + 12288, K1_CS = 4 * K1_SLOT, K1_CSP = K1_CS + 512;
constexpr int L_Q = 0, L_KV = 24576, L_ONE = L_KV + 131072, LDS_BYTES = 163840;
constexpr int L_F = 0, L_F40 = L_ONE + 1024, L_WO = 40960, L_PA = L_WO + 65536, L_SEL = L_PA + 32768, L_ST = L_SEL + 12288, L_WF = L_ONE + 64;
constexpr int NGU = 86, DN0 = 128, NDN = 32;

template <int NV>
__device__ __forceinline__ void qkv_body(const __amdgpu_buffer_rsrc_t r_ws, LAS char* lds, int rb, int cb, int wid) {
  const int lane = threadIdx.x & 63, t = lane & 31, h = lane >> 5;
  int xr[4];
#pragma unroll
  for (int u = 0; u < 4; ++u) xr[u] = wid * 4096 + t * 128 + (((2 * u + h) ^ ((t >> 1) & 7)) * 16);
  const int wrd = 16384 + (8 * h + ((lane >> 2) & 3)) * 192 + 32 * ((lane >> 4) & 1) + 8 * (lane & 3);
  f32x16 acc[3];
#pragma unroll
  for (int j = 0; j < 3; ++j) acc[j] = f32x16{};
  float s1a = 0.f, s1b = 0.f, s2a = 0.f, s2b = 0.f;
  u32x4 xf = u32x4{0, 0, 0, 0}, wf[3];
#pragma unroll
  for (int j = 0; j < 3; ++j) wf[j] = u32x4{0, 0, 0, 0};
  auto mm = [&]() {
#pragma unroll
    for (int j = 0; j < 3; ++j) {
      if (j >= 3 - NV) acc[j] = mfma32(xf, wf[j], acc[j]);
      else acc[j] = mfma32(wf[j], xf, acc[j]);
    }
  };
  const int tile = rb * 4 + wid;
  const int voffV = tile * 2048 + 1024 + (t >> 4) * 131072 + h * 256 + swapb23(t & 15) * 16;
  const int voffK = tile * 2048 + swapb23(t) * 32 + h * 16;
  const int voffQ = tile * 1024 + t * 32 + h * 16;
#pragma unroll 1
  for (int c = 0; c < 8; ++c) {
    LDS_BAR();
    const LAS char* sb = lds + (c & 3) * K1_SLOT;
    u32x4 xn[4];
#pragma unroll
    for (int u = 0; u < 4; ++u) {
      xn[u] = ldsr128(sb + xr[u]);
      u32x4 wn[3];
#pragma unroll
      for (int j = 0; j < 3; ++j) wn[j] = ldstr2(sb + wrd + u * 3072 + j * 64, 768);
      mm();
#pragma unroll
      for (int z = 0; z < 3; ++z) { __builtin_amdgcn_sched_group_barrier(0x8, 1, 0); __builtin_amdgcn_sched_group_barrier(0x102, 3, 0); }
      __builtin_amdgcn_sched_barrier(0);
      xf = xn[u];
#pragma unroll
      for (int j = 0; j < 3; ++j) wf[j] = wn[j];
    }
#pragma unroll
    for (int u = 0; u < 4; ++u)
#pragma unroll
      for (int e = 0; e < 4; e += 2) {
        s1a = dot2bf(xn[u][e], ONE2, s1a); s2a = dot2bf(xn[u][e], xn[u][e], s2a);
        s1b = dot2bf(xn[u][e + 1], ONE2, s1b); s2b = dot2bf(xn[u][e + 1], xn[u][e + 1], s2b);
      }
    asm volatile("" : "+v"(s1a), "+v"(s1b), "+v"(s2a), "+v"(s2b));
    __builtin_amdgcn_sched_barrier(0);
  }
  __builtin_amdgcn_s_barrier();
  mm();
  LAS float* scr = (LAS float*)(lds + wid * 4096);
  const LAS float* csum = (const LAS float*)(lds + K1_CS);
  const float s2 = swap32_sum(s2a + s2b);
  const float mean = swap32_sum(s1a + s1b) * (1.f / D_);
  const float var = __builtin_fmaxf(s2 * (1.f / D_) - mean * mean, 0.f);
  const float rstd = __builtin_amdgcn_rsqf(var + LN_EPS);
  if (NV > 0 && h == 0) { scr[t] = rstd; scr[32 + t] = mean * rstd; }
  BAR();
#pragma unroll
  for (int j = 0; j < 3; ++j) {
    const int gt = cb * 3 + j;
    float v[16];
    if (j >= 3 - NV) {
      const float cs = csum[32 * j + t];
#pragma unroll
      for (int gq = 0; gq < 4; ++gq) {
        const f32x4 a = *(const LAS f32x4*)(scr + 8 * gq + 4 * h), b = *(const LAS f32x4*)(scr + 32 + 8 * gq + 4 * h);
#pragma unroll
        for (int e = 0; e < 4; ++e) v[4 * gq + e] = acc[j][4 * gq + e] * a[e] - b[e] * cs;
      }
      const int so = WS_KV + (gt - 32) * 262144;
      ST128(pack8(v), voffV, so); ST128(pack8(v + 8), voffV + 512, so);
    } else {
      const bool isq = gt < 16;
      const float sc = isq ? QSCALE : 1.f;
      const float a = rstd * sc, b = mean * rstd * sc;
#pragma unroll
      for (int gq = 0; gq < 4; ++gq) {
        const f32x4 c4 = *(const LAS f32x4*)(csum + 32 * j + 8 * gq + 4 * h);
#pragma unroll
        for (int e = 0; e < 4; ++e) v[4 * gq + e] = acc[j][4 * gq + e] * a - b * c4[e];
      }
      const int vo = isq ? voffQ : voffK;
      const int so = isq ? WS_QF + gt * 131072 : WS_KV + (gt - 16) * 262144;
      ST128(pack8(v), vo, so); ST128(pack8(v + 8), vo, so + (isq ? 65536 : 131072));
    }
  }
}

__device__ __forceinline__ float rowmax16(const f32x16& s) {
  float a = __builtin_fmaxf(__builtin_fmaxf(s[0], s[1]), s[2]), b = __builtin_fmaxf(__builtin_fmaxf(s[3], s[4]), s[5]);
  a = __builtin_fmaxf(__builtin_fmaxf(a, s[6]), s[7]); b = __builtin_fmaxf(__builtin_fmaxf(b, s[8]), s[9]);
  a = __builtin_fmaxf(__builtin_fmaxf(a, s[10]), s[11]); b = __builtin_fmaxf(__builtin_fmaxf(b, s[12]), s[13]);
  a = __builtin_fmaxf(__builtin_fmaxf(a, s[14]), s[15]);
  return swap32_max(__builtin_fmaxf(a, b));
}
__device__ __forceinline__ f32x8 lo8(const f32x16& s) { return __builtin_shufflevector(s, s, 0, 1, 2, 3, 4, 5, 6, 7); }
__device__ __forceinline__ f32x8 hi8(const f32x16& s) { return __builtin_shufflevector(s, s, 8, 9, 10, 11, 12, 13, 14, 15); }

#define E1(X, i) "v_exp_f32 v[" #X "+" #i "], v[" #X "+" #i "]\n\t"
#define SMF(A, B, off) "v_smfmac_f32_32x32x32_bf16 v[0:15], v[" #A ":" #A "+3], v[" #B "+" #off ":" #B "+" #off "+7], v88\n\t"
#define QK(Y) "v_mfma_f32_32x32x16_bf16 v[" #Y ":" #Y "+15], v[68:71], v[64:67], v[16:31]\n\t"
#define RDV(D, off) "ds_read_b128 v[" #D ":" #D "+3], v90 offset:" #off "\n\t"
#define RDK(off) "ds_read_b128 v[68:71], v89 offset:" #off "\n\t"
#define STEP(X, Y, VA, VPB, VA2, VB2, KOFF, VOFF, VOFF2, PA, PB, EXTRA) \
  PA E1(X, 0) E1(X, 1) SMF(VPB, Y, 8) RDV(VA2, VOFF) \
  PB E1(X, 2) E1(X, 3) E1(X, 4) E1(X, 5) \
  "s_waitcnt lgkmcnt(1)\n\t" QK(Y) RDV(VB2, VOFF2) RDK(KOFF) \
  E1(X, 6) E1(X, 7) E1(X, 8) E1(X, 9) \
  SMF(VA, X, 0) EXTRA \
  E1(X, 10) E1(X, 11) E1(X, 12) E1(X, 13) E1(X, 14) E1(X, 15)
#define ATTN_LOOP(L1, L2, P0, P2, P3) \
        L1 ":\n\t" \
        "s_bitcmp1_b32 0x408, %[cnt]\n\t" \
        "s_cbranch_scc0 " L2 "f\n\t" \
        "s_barrier\n\t" \
        L2 ":\n\t" \
        STEP(32, 48, 72, 84, 80, 84, 4096, 2048, 2560, P0, "", "") \
        STEP(48, 32, 80, 76, 72, 76, 6144, 4096, 4608, "", "", "") \
        STEP(32, 48, 72, 84, 80, 84, 8192, 6144, 6656, P2, "", "v_add_u32 v90, v90, v91\n\t") \
        STEP(48, 32, 80, 76, 72, 76, 10240, 0, 512, "", P3, "v_add_u32 v89, 0x2000, v89\n\ts_add_u32 %[cnt], %[cnt], 1\n\ts_cmp_lg_u32 %[cnt], 16\n\t") \
        "s_cbranch_scc1 " L1 "b\n\t"

__global__ __launch_bounds__(576, 3) void fused_block(const u16* __restrict__ x, const u16* __restrict__ wqkv, const u16* __restrict__ wo,
                                                      const u16* __restrict__ wr, const u16* __restrict__ wg, const u16* __restrict__ wu,
                                                      const u16* __restrict__ wd, char* __restrict__ ws, u16* __restrict__ out) {
  extern __shared__ __attribute__((aligned(16))) char smem[];
  LAS char* lds = (LAS char*)smem;
  const unsigned lds0 = (unsigned)(uintptr_t)lds;
  {
    unsigned long long pc; unsigned s0, s1, v0;
    asm volatile("v_readfirstlane_b32 %1, %4\n\ts_lshr_b32 %2, %5, 3\n\ts_getpc_b64 %0\n\ts_and_b64 %0, %0, -16\n\t"
                 "s_cmp_lt_u32 %1, 64\n\ts_cbranch_scc0 9f\n\t"
                 "s_cmp_lt_u32 %2, %6\n\ts_cbranch_scc0 9f\n\t"
                 "s_lshl_b32 %2, %2, 10\n\tv_lshlrev_b32 %3, 4, %4\n\tv_add_u32 %3, %2, %3\n\t"
                 "s_mov_b32 %1, m0\n\ts_mov_b32 m0, %7\n\ts_nop 0\n\t"
                 "global_load_lds_dwordx4 %3, %0\n\ts_mov_b32 m0, %1\n\t9:"
                 : "=&s"(pc), "=&s"(s0), "=&s"(s1), "=&v"(v0) : "v"(threadIdx.x), "s"(blockIdx.x), "i"(CODE_KB), "s"(lds0 + 131072) : "scc", "memory");
  }
  const int tid = threadIdx.x, lane = tid & 63, t = lane & 31, h = lane >> 5;
  const int wid = __builtin_amdgcn_readfirstlane(tid >> 6);
  const int b = blockIdx.x;
  const int rb1 = 4 * (b & 3) + ((b >> 3) & 3), cb1 = 8 * ((b >> 2) & 1) + (b >> 5);
  const int head = (b & 7) * 4 + ((b >> 3) & 3), qb = b >> 5;
  const int pm = head, cb = qb;
  const unsigned l16 = lane * 16;
  const int lw = wid - 4;
  const int rot = ((((b >> 5) + ((b >> 3) & 3)) & 1)) * 4;
  unsigned xoff[2], woff[3];
  auto stage = [&](int c) {
    const int kc = (c + rot) & 7;
    const char* xs = (const char*)x + kc * 128;
    const char* wsrc = (const char*)wqkv + kc * (64 * 3072);
    const unsigned base = lds0 + (c & 3) * K1_SLOT;
#pragma unroll
    for (int p = 0; p < 4; ++p) glds16(xs + (p >> 1) * 16384, xoff[p & 1], base + lw * 4096 + p * 1024);
#pragma unroll
    for (int p = 0; p < 3; ++p) glds16(wsrc, woff[p], base + 16384 + (lw * 3 + p) * 1024);
  };
  if (lw >= 0 && lw < 4) {
#pragma unroll
    for (int p = 0; p < 2; ++p) {
      const int row = p * 8 + (lane >> 3), slot = lane & 7;
      xoff[p] = (unsigned)((rb1 * 128 + lw * 32 + row) * 1024 + ((slot ^ ((row >> 1) & 7)) * 16));
    }
#pragma unroll
    for (int p = 0; p < 3; ++p) {
      const int idx = (lw * 3 + p) * 64 + lane, k = idx / 12, c = idx - k * 12;
      woff[p] = (unsigned)(k * 3072 + cb1 * 192 + c * 16);
    }
    stage(0); stage(1);
  }
  FENCE();
  const bool isgu = b < NGU, isdn = b >= DN0 && b < DN0 + NDN;
  const __amdgpu_buffer_rsrc_t r_ws = mkrsrc(ws, WS_BYTES), r_out = mkrsrc(out, 2u << 20);

  if (wid == 8) {
    __builtin_amdgcn_s_setprio(3);
    const u32x2 ep = LD64(0, WS_EPOCH);
    const u32x4 w4 = *(const u32x4*)(wr + lane * 8);
    if (lane < 16) { const unsigned v = (lane & 1) ? 0u : ONE2; *(LAS u32x4*)(lds + L_ONE + (lane >> 2) * 2048 + ((lane >> 1) & 1) * 512 + (lane & 1) * 16) = u32x4{v, v, v, v}; }
    if (lane == 16) *(LAS u32x4*)(lds + L_WF) = u32x4{0, 0, 0, 0};
#pragma unroll 1
    for (int c = 0; c < 5; ++c) BAR();
    const unsigned long long e64 = (((unsigned long long)ep[1] << 32) | ep[0]) + 1ull;
    const u32x2 tag = u32x2{(unsigned)e64, (unsigned)(e64 >> 32)};
    float sw;
    sw = dot2bf(w4[0], ONE2, dot2bf(w4[1], ONE2, 0.f)) + dot2bf(w4[2], ONE2, dot2bf(w4[3], ONE2, 0.f));
    sw = sum8(sw); sw += dppf<0x140>(sw);
    sw = (rdlf(sw, 0) + rdlf(sw, 16)) + (rdlf(sw, 32) + rdlf(sw, 48));
    const int hp = head >> 1;
    const int fidx = lane < 16 ? lane * 16 + (16 + hp) / 3 : lane < 32 ? (lane - 16) * 16 + (32 + hp) / 3 : (qb * 2 + (lane & 1)) * 16 + hp / 3;
    const char* kvsrc = ws + WS_KV + (size_t)head * 131072;
    unsigned foff[4];
    const unsigned fstr = isgu ? 32 * F_ * 2 : 32768;
#pragma unroll
    for (int j = 0; j < 4; ++j)
      foff[j] = (isgu ? (unsigned)((lane >> 1) * (F_ * 2) + b * 32) : (unsigned)((lane >> 1) * 1024 + (b - DN0) * 32)) + (lane & 1) * 16 + j * (fstr - 1024);
    unsigned wooff[4];
#pragma unroll
    for (int j = 0; j < 4; ++j) wooff[j] = (unsigned)((lane >> 3) * 1024 + cb * 128 + (((lane & 7) ^ (((lane >> 4) & 1) << 2)) * 16) + j * 7168);
    u32x4 s4 = u32x4{SENT, SENT, SENT, SENT};
    unsigned armoff = (lane >> 3) * 1024 + (lane & 7) * 16, l128 = lane * 128;
    asm volatile("" : "+v"(foff[0]), "+v"(foff[1]), "+v"(foff[2]), "+v"(foff[3]), "+v"(wooff[0]), "+v"(wooff[1]), "+v"(wooff[2]), "+v"(wooff[3]),
                      "+v"(s4), "+v"(armoff), "+v"(l128));
    LDS_BAR();
#pragma unroll 1
    for (int c = 6; c < 10; ++c) BAR();
    BAR();
    if (lane == 0) ST64(tag, 0, WS_FLAG + (rb1 * 16 + cb1) * 128);
    unsigned long long seen = 0;
    auto wait_flags = [&](unsigned long long need) {
#pragma unroll 1
      for (unsigned spins = 0; spins < SPIN_CAP && (seen & need) != need; ++spins) {
        FENCE();
        const u32x2 f = LD64(fidx * 128, WS_FLAG);
        seen = __ballot((f[0] == tag[0]) & (f[1] == tag[1]));
      }
      FENCE();
    };
    wait_flags(0x3000F000Full);
    glds8c(ws + WS_QF + head * 65536 + qb * 8192, l16, lds0 + L_Q);
#pragma unroll
    for (int p = 0; p < 4; ++p) glds8c(kvsrc + p * 8192, l16, lds0 + L_KV + p * 8192);
    VM_BAR(4);
    wait_flags(0xFFFFFFFFull);
#pragma unroll 1
    for (int p = 4; p < 10; ++p) glds8c(kvsrc + p * 8192, l16, lds0 + L_KV + p * 8192);
    WAIT_VM(8);
#pragma unroll 1
    for (int p = 10; p < 16; ++p) glds8c(kvsrc + p * 8192, l16, lds0 + L_KV + p * 8192);
    VM_BAR(0);
    {
#pragma unroll
      for (int e = 0; e < 8; ++e) __builtin_amdgcn_raw_buffer_store_b128(s4, r_ws, l16, WS_OF + (qb * 4 + (e >> 1)) * 65536 + head * 2048 + (e & 1) * 1024, SC1);
#pragma unroll
      for (int e = 0; e < 8; ++e) {
        const int so = (pm * 64 + e * 8) * 1024 + cb * 128;
        __builtin_amdgcn_raw_buffer_store_b128(s4, r_ws, armoff, WS_HN + so, SC1);
        __builtin_amdgcn_raw_buffer_store_b128(s4, r_out, armoff, so, SC1);
      }
      __builtin_amdgcn_raw_buffer_store_b128(s4, r_ws, l16, WS_XS + (pm * 8 + cb) * 1024, SC1);
      if (lane < 8 && b < (22016 + 8192) / 128) __builtin_amdgcn_raw_buffer_store_b128(s4, r_ws, l16, WS_ACT + b * 128, SC1);
    }
    WAIT_VM(0);
    asm volatile("" :: "v"(s4));
    if (lane == 0) ST64(tag, 0, WS_FLAG2 + b * 128);
    auto ffn = [&](int q0, int q1) {
      if (isgu) {
#pragma unroll 1
        for (int p = q0; p < min(q1, 8); ++p) glds4((const char*)(p < 4 ? wg : wu) + (p & 3) * (128 * F_ * 2), foff, lds0 + L_F + p * 4096);
      } else if (isdn) {
#pragma unroll 1
        for (int p = q0; p < min(q1, 10); ++p) glds4((const char*)wd + p * 131072, foff, lds0 + L_F + p * 4096);
        if (q1 > 10) {
#pragma unroll 1
          for (int p = 40; p < 43; ++p) glds16((const char*)wd + p * 32768, foff[0], lds0 + L_F40 + (p - 40) * 2048);
        }
      }
    };
    ffn(0, 11);
    BAR();
    WAIT_VM(24);
#pragma unroll 1
    for (int p = 0; p < 8; ++p) glds4((const char*)wo + p * 32768, wooff, lds0 + L_WO + p * 4096);
    WAIT_VM(24);
#pragma unroll 1
    for (int p = 8; p < 16; ++p) glds4((const char*)wo + p * 32768, wooff, lds0 + L_WO + p * 4096);
#pragma unroll 1
    for (unsigned spins = 0; spins < SPIN_CAP; ++spins) {
      FENCE();
      bool ok = true;
#pragma unroll
      for (int e = 0; e < 4; ++e) { const u32x2 f = LD64(l128, WS_FLAG2 + e * 8192); ok &= (f[0] == tag[0]) & (f[1] == tag[1]); }
      if (__all(ok)) break;
      __builtin_amdgcn_s_sleep(16);
    }
    if (b == 0 && lane == 0) ST64(tag, 0, WS_EPOCH);
    WAIT_VM(0);
    BAR();
    BAR(); BAR();
    u32x4 pv[8];
    {
      const u32x4 own = nosent4(ldsr128(lds + L_ST + l16));
      ST128(own, l16, WS_XS + (pm * 8 + cb) * 1024);
      __builtin_amdgcn_s_sleep(10);
#pragma unroll
      for (int e = 0; e < 8; ++e) pv[e] = LD128(l16, WS_XS + (pm * 8 + e) * 1024);
#pragma unroll 1
      for (unsigned spins = 0; spins < SPIN_CAP; ++spins) {
        bool miss[8], any = false;
#pragma unroll
        for (int e = 0; e < 8; ++e) { miss[e] = __any(umax4(pv[e]) == SENT); any |= miss[e]; }
        if (!any) break;
        FENCE();
#pragma unroll
        for (int e = 0; e < 8; ++e) if (miss[e]) pv[e] = LD128(l16, WS_XS + (pm * 8 + e) * 1024);
      }
    }
#define SUM8(c) (((__uint_as_float(pv[0][c]) + __uint_as_float(pv[1][c])) + (__uint_as_float(pv[2][c]) + __uint_as_float(pv[3][c]))) + \
                 ((__uint_as_float(pv[4][c]) + __uint_as_float(pv[5][c])) + (__uint_as_float(pv[6][c]) + __uint_as_float(pv[7][c]))))
    const float mean = SUM8(0) * (1.f / D_);
    const float rstd = __builtin_amdgcn_rsqf(__builtin_fmaxf(SUM8(1) * (1.f / D_) - mean * mean, 0.f) + LN_EPS);
    if (cb == 0) {
      const unsigned u = __float_as_uint(rstd * (SUM8(2) - mean * sw));
      const unsigned ordv = (u & 0x80000000u) ? ~u : (u | 0x80000000u);
      __builtin_amdgcn_raw_buffer_store_b32(max((ordv >> 1) & 0x7FFFF800u, 0x800u) | (unsigned)(2047 - (pm * 64 + lane)), r_ws, lane * 4, WS_KEY + pm * 256, SC1);
    }
    *(LAS u32x2*)(lds + L_ST + 1024 + lane * 8) = u32x2{__float_as_uint(rstd), __float_as_uint(mean * rstd)};
    LDS_BAR();
    return;
  }

  if (wid >= 4) {
    const int wrd = 16384 + lw * 3072 + (8 * h + ((lane >> 2) & 3)) * 192 + 32 * ((lane >> 4) & 1) + 8 * (lane & 3);
    float cs[3] = {0.f, 0.f, 0.f};
#pragma unroll 1
    for (int c = 0; c < 8; ++c) {
      if (c < 7) WAIT_BAR(7); else WAIT_BAR(0);
      const LAS char* sb = lds + (c & 3) * K1_SLOT + wrd;
      u32x4 wf[3];
#pragma unroll
      for (int j = 0; j < 3; ++j) wf[j] = ldstr2(sb + j * 64, 768);
      if (c < 6) stage(c + 2);
#pragma unroll
      for (int e = 0; e < 4; ++e)
#pragma unroll
        for (int j = 0; j < 3; ++j) cs[j] = dot2bf(wf[j][e], ONE2, cs[j]);
    }
#pragma unroll
    for (int j = 0; j < 3; ++j) *(LAS float*)(lds + K1_CSP + (lw * 2 + h) * 384 + (32 * j + t) * 4) = cs[j];
    LDS_BAR();
    if (lw == 0 && lane < 24) {
      const LAS char* pp = lds + K1_CSP + l16;
      f32x4 sacc = *(const LAS f32x4*)pp;
#pragma unroll
      for (int e = 1; e < 8; ++e) sacc += *(const LAS f32x4*)(pp + e * 384);
      *(LAS f32x4*)(lds + K1_CS + l16) = sacc;
    }
    LDS_BAR();
    BAR();
  } else {
    if (cb1 < 10) qkv_body<0>(r_ws, lds, rb1, cb1, wid);
    else if (cb1 == 10) qkv_body<1>(r_ws, lds, rb1, cb1, wid);
    else qkv_body<3>(r_ws, lds, rb1, cb1, wid);
    WAIT_BAR(0);
  }

  const bool onel = (lane & 16) != 0;
  unsigned kaddr = lds0 + L_KV + t * 32 + h * 16;
  unsigned vaddr = lds0 + (onel ? L_ONE + ((lane & 11) ? 16 : 0) : L_KV + 1024 + h * 256 + (lane & 15) * 16);
  const int vstep = onel ? 0 : 8192;
  const int IDX = 0xDDDDDDDD;
  const int row = pm * 64 + wid * 8 + (lane >> 3), col = cb * 64 + (lane & 7) * 8;
  f32x16 negm, sx, sy = f32x16{}, o = f32x16{};
  u32x4 vpb = u32x4{0, 0, 0, 0};
  asm volatile("" : "+{v[0:15]}"(o), "+{v[48:63]}"(sy), "+{v[84:87]}"(vpb));

  BAR();
  const u32x4 xres = *(const u32x4*)(x + row * D_ + col), wrv = *(const u32x4*)(wr + col);
  const int mh = wid >> 2, kq = wid & 3;
  const u32x4 qf = ldsr128(lds + L_Q + kq * 2048 + mh * 1024 + t * 32 + h * 16);
  int ostoff = t * 32 + h * 16 + WS_OF + (qb * 4 + kq) * 65536 + head * 2048 + mh * 1024;
  asm volatile("" : "+v"(ostoff));
  u32x4 kn, va, vb;
  {
    const u32x4 k0 = ldsr128((const LAS char*)(uintptr_t)kaddr);
    kn = ldsr128((const LAS char*)(uintptr_t)kaddr + 2048);
    va = ldsr128((const LAS char*)(uintptr_t)vaddr); vb = ldsr128((const LAS char*)(uintptr_t)vaddr + 512);
    const float m = -rowmax16(mfma32(k0, qf, f32x16{}));
#pragma unroll
    for (int r = 0; r < 16; ++r) negm[r] = m;
    sx = mfma32(k0, qf, negm);
  }
  {
    unsigned cnt;
    asm volatile(
        "s_mov_b32 %[cnt], 0\n\t"
        "s_nop 7\n\t"
        "s_cmp_gt_u32 %[wid], 3\n\t"
        "s_cbranch_scc1 3f\n\t"
        "s_setprio 1\n\t"
        ATTN_LOOP("1", "2", "", "", "")
        "s_setprio 0\n\t"
        "s_branch 5f\n\t"
        ATTN_LOOP("3", "4", "s_setprio 2\n\t", "", "s_setprio 0\n\t")
        "5:\n\t"
        "s_nop 1\n\t"
        SMF(84, 48, 8)
        "s_waitcnt lgkmcnt(0)\n\t"
        "s_nop 11\n\t"
        : "+{v[0:15]}"(o), "+{v[32:47]}"(sx), "+{v[48:63]}"(sy), "+{v[68:71]}"(kn), "+{v[72:75]}"(va), "+{v[76:79]}"(vb),
          "+{v[84:87]}"(vpb), "+{v89}"(kaddr), "+{v90}"(vaddr), [cnt] "=&s"(cnt)
        : "{v[16:31]}"(negm), "{v[64:67]}"(qf), "{v88}"(IDX), "{v91}"(vstep), [wid] "s"(wid)
        : "v80", "v81", "v82", "v83", "scc", "memory");
  }
  auto normalise = [&]() {
    const float inv = __builtin_amdgcn_rcpf(o[8]);
    return nosent4(u32x4{pkbf(o[0] * inv, o[1] * inv), pkbf(o[2] * inv, o[3] * inv), pkbf(o[4] * inv, o[5] * inv), pkbf(o[6] * inv, o[7] * inv)});
  };
  u32x4 opk = normalise();
  asm volatile("" : "+v"(opk));
  {
    float c0, c1, c2, c3;
    asm("v_add_f32 %0, |%1|, |%2|" : "=v"(c0) : "v"(o[0]), "v"(o[1])); asm("v_add_f32 %0, |%1|, |%2|" : "=v"(c1) : "v"(o[2]), "v"(o[3]));
    asm("v_add_f32 %0, |%1|, |%2|" : "=v"(c2) : "v"(o[4]), "v"(o[5])); asm("v_add_f32 %0, |%1|, |%2|" : "=v"(c3) : "v"(o[6]), "v"(o[7]));
    const float chk = (c0 + c1) + (c2 + c3);
    const bool bad = !(o[8] < 1.8446744e19f) || !(chk < 3.0e38f);
    if (__builtin_expect(__any(bad), 0)) {
      float m = -INFINITY;
      o = f32x16{};
      const int kqo = head * 131072 + t * 32 + h * 16, vq = head * 131072 + 1024 + h * 256 + (lane & 15) * 16;
      const u32x4 ones = u32x4{ONE2, ONE2, ONE2, ONE2};
#pragma unroll 1
      for (int i = 0; i < 64; ++i) {
        f32x16 s = mfma32(LD128(kqo, WS_KV + i * 2048), qf, f32x16{});
        u32x4 v0 = LD128(vq, WS_KV + i * 2048), v1 = LD128(vq + 512, WS_KV + i * 2048);
        if (onel) { v0 = ones; v1 = ones; }
        const float mn = __builtin_fmaxf(m, rowmax16(s));
        const float al = __builtin_amdgcn_exp2f(m - mn);
        m = mn;
#pragma unroll
        for (int r = 0; r < 16; ++r) { o[r] *= al; s[r] = __builtin_amdgcn_exp2f(s[r] - mn); }
        o = smfmac32(v0, lo8(s), o, IDX);
        o = smfmac32(v1, hi8(s), o, IDX);
      }
      opk = normalise();
    }
  }
  __builtin_amdgcn_raw_buffer_store_b128(opk, r_ws, ostoff, 0, SC1);
  BAR();

  const int g = lane >> 4, q = (lane >> 2) & 3, p4 = lane & 3;
  f32x4 a0 = f32x4{bflo(xres[0]), bfhi(xres[0]), bflo(xres[1]), bfhi(xres[1])}, a1 = f32x4{bflo(xres[2]), bfhi(xres[2]), bflo(xres[3]), bfhi(xres[3])};
  unsigned ra0, ra1;
  {
    const int lr = wid * 8 + (lane >> 3);
    const int sl = ((lane & 7) ^ ((lr & 7) | ((lr & 1) << 3))) * 16;
    const unsigned rbase = lds0 + (mh ? L_WO : L_PA) + (lr & 31) * 256;
    ra0 = rbase + sl; ra1 = rbase + (sl ^ 128);
  }
  LAS unsigned* cand = (LAS unsigned*)(lds + L_SEL);
  {
    u32x4 of[8], wf0[8], wf1[8];
    const int flip = ((q >> 1) & 1) << 6;
    const LAS char* wb = lds + L_WO + kq * 16384 + (8 * h + q) * 128 + 32 * (g & 1) + 8 * p4;
#pragma unroll
    for (int e = 0; e < 8; ++e) { wf0[e] = ldstr2(wb + flip + e * 2048, 512); wf1[e] = ldstr2(wb + (64 ^ flip) + e * 2048, 512); }
    const int ooff = t * 32 + h * 16, osoff = WS_OF + pm * 65536 + kq * 16384 + mh * 1024;
    unsigned pa[8];
    {
      const unsigned rb = lds0 + (mh ? L_WO + kq * 16384 : L_PA + kq * 8192) + t * 256;
      const int swz = (t & 7) | ((t & 1) << 3);
#pragma unroll
      for (int gq = 0; gq < 8; ++gq) { pa[gq] = rb + (((h * 8 + gq) ^ swz) * 16); asm volatile("" : "+v"(pa[gq])); }
    }
    asm volatile("" : "+v"(a0), "+v"(a1), "+v"(ra0), "+v"(ra1));
    __builtin_amdgcn_s_waitcnt(0xC07F);
    FENCE();
    if (!mh) *(volatile LAS unsigned*)(lds + L_WF + kq * 4) = 1u;
    else {
#pragma unroll 1
      for (unsigned spins = 0; spins < (1u << 24); ++spins) {
        if (__builtin_amdgcn_readfirstlane(*(volatile LAS unsigned*)(lds + L_WF + kq * 4))) break;
        __builtin_amdgcn_s_sleep(1);
      }
    }
    FENCE();
#pragma unroll
    for (int e = 0; e < 8; ++e) of[e] = LD128(ooff, osoff + e * 2048);
    f32x16 acc0 = f32x16{}, acc1 = f32x16{};
#pragma unroll 1
    for (unsigned spins = 0; spins < SPIN_CAP; ++spins) {
      bool miss[8], any = false;
#pragma unroll
      for (int e = 0; e < 8; ++e) { miss[e] = __any(umax4(of[e]) == SENT); any |= miss[e]; }
      if (!any) break;
      FENCE();
#pragma unroll
      for (int e = 0; e < 8; ++e) if (miss[e]) of[e] = LD128(ooff, osoff + e * 2048);
    }
#pragma unroll
    for (int e = 0; e < 8; ++e) acc0 = mfma32(wf0[e], of[e], acc0);
    __builtin_amdgcn_sched_barrier(0);
#pragma unroll
    for (int e = 0; e < 8; ++e) {
      acc1 = mfma32(wf1[e], of[e], acc1);
      if (e >= 3 && e < 7) *(LAS f32x4*)(uintptr_t)pa[e - 3] = f32x4{acc0[4 * e - 12], acc0[4 * e - 11], acc0[4 * e - 10], acc0[4 * e - 9]};
      asm volatile("" : "+v"(acc1));
      __builtin_amdgcn_sched_barrier(0);
    }
#pragma unroll
    for (int gq = 0; gq < 4; ++gq)
      *(LAS f32x4*)(uintptr_t)pa[4 + gq] = f32x4{acc1[4 * gq], acc1[4 * gq + 1], acc1[4 * gq + 2], acc1[4 * gq + 3]};
  }
  LDS_BAR();
  float hv[8];
  {
    const LAS char* r0 = (const LAS char*)(uintptr_t)ra0;
    const LAS char* r1 = (const LAS char*)(uintptr_t)ra1;
    f32x4 t0[4], t1[4];
    if (mh) {
#pragma unroll
      for (int e = 0; e < 4; ++e) { t0[e] = *(const LAS f32x4*)(r0 + e * 16384); t1[e] = *(const LAS f32x4*)(r1 + e * 16384); }
    } else {
#pragma unroll
      for (int e = 0; e < 4; ++e) { t0[e] = *(const LAS f32x4*)(r0 + e * 8192); t1[e] = *(const LAS f32x4*)(r1 + e * 8192); }
    }
    __builtin_amdgcn_sched_barrier(0);
    a0 += (t0[0] + t0[1]) + (t0[2] + t0[3]);
    a1 += (t1[0] + t1[1]) + (t1[2] + t1[3]);
    const u32x4 hb = u32x4{pkbf(a0[0], a0[1]), pkbf(a0[2], a0[3]), pkbf(a1[0], a1[1]), pkbf(a1[2], a1[3])};
    float s1 = 0.f, s2 = 0.f, sd = 0.f;
#pragma unroll
    for (int e = 0; e < 4; ++e) { s1 = dot2bf(hb[e], ONE2, s1); s2 = dot2bf(hb[e], hb[e], s2); sd = dot2bf(hb[e], wrv[e], sd); }
    s1 = sum8(s1); s2 = sum8(s2); sd = sum8(sd);
    if ((lane & 7) == 0) *(LAS u32x4*)(lds + L_ST + wid * 128 + lane * 2) = u32x4{__float_as_uint(s1), __float_as_uint(s2), __float_as_uint(sd), 0u};
    LDS_BAR();
    if (tid < 80) cand[tid] = 0;
#pragma unroll
    for (int e = 0; e < 4; ++e) { hv[2 * e] = bflo(hb[e]); hv[2 * e + 1] = bfhi(hb[e]); }
  }
  const int trd = (8 * g + q) * 32 + 8 * p4;
  u32x4 fw[8];
  if (isgu || isdn) {
#pragma unroll
    for (int p = 0; p < 8; ++p) {
      const int po = isgu ? L_F + ((p >> 2) * 16 + (wid & 3) * 4 + (p & 3)) * 1024 : p < 5 ? L_F + (p * 8 + wid) * 1024 : L_F40 + min(wid, 2) * 2048;
      fw[p] = ldstr2(lds + po + trd, 128);
    }
  }
  u32x4 k4;
  LDS_BAR();
  {
    const u32x2 st = *(const LAS u32x2*)(lds + L_ST + 1024 + wid * 64 + (lane >> 3) * 8);
    const float rstd = __uint_as_float(st[0]), mr = __uint_as_float(st[1]);
    u32x4 hp, op;
#pragma unroll
    for (int e = 0; e < 4; ++e) {
      hp[e] = pkbf(hv[2 * e] * rstd - mr, hv[2 * e + 1] * rstd - mr);
      op[e] = pkbf(hv[2 * e] + bflo(hp[e]), hv[2 * e + 1] + bfhi(hp[e]));
    }
    ST128(nosent4(hp), row * 1024 + col * 2, WS_HN);
    __builtin_amdgcn_raw_buffer_store_b128(nosent4(op), r_out, row * 1024 + col * 2, 0, SC1);
  }
  if (!isgu && !isdn) return;
#pragma unroll 1
  for (unsigned spins = 0; spins < SPIN_CAP; ++spins) {
    FENCE();
    k4 = LD128(tid * 16, WS_KEY);
    if (__all(umax4(k4) != SENT)) break;
  }
  {
    unsigned m = umax4(k4);
    m = max(m, dppu<0xB1>(m)); m = max(m, dppu<0x4E>(m)); m = max(m, dppu<0x141>(m)); m = max(m, dppu<0x140>(m));
    m = max(m, dppu<0x142>(m)); m = max(m, dppu<0x143>(m));
    if (lane == 63) cand[64 + wid] = m;
  }
  LDS_BAR();
  {
    const u32x4 g0 = *(LAS u32x4*)(cand + 64), g1 = *(LAS u32x4*)(cand + 68);
    const unsigned T = min(min(min(g0[0], g0[1]), min(g0[2], g0[3])), min(min(g1[0], g1[1]), min(g1[2], g1[3])));
    const unsigned c0 = k4[0] >= T, c1 = k4[1] >= T, c2 = k4[2] >= T, c3 = k4[3] >= T;
    const unsigned cnt = (c0 + c1) + (c2 + c3);
    if (cnt) {
      unsigned pos;
      asm volatile("ds_add_rtn_u32 %0, %1, %2\n\ts_waitcnt lgkmcnt(0)" : "=v"(pos) : "v"((unsigned)(lds0 + L_SEL + 72 * 4)), "v"(cnt) : "memory");
      cand[c0 && pos < 64 ? pos : 73] = k4[0]; pos += c0;
      cand[c1 && pos < 64 ? pos : 73] = k4[1]; pos += c1;
      cand[c2 && pos < 64 ? pos : 73] = k4[2]; pos += c2;
      cand[c3 && pos < 64 ? pos : 73] = k4[3];
    }
  }
  LDS_BAR();
  LAS unsigned* sel = cand + 80 + wid * 8;
  {
    unsigned nv = cand[72];
    unsigned c = cand[t];
    u32x4 o4[4];
#pragma unroll
    for (int e = 0; e < 4; ++e) o4[e] = *(LAS u32x4*)(cand + h * 16 + e * 4);
    asm volatile("" : "+v"(nv), "+v"(c), "+v"(o4[0]), "+v"(o4[1]), "+v"(o4[2]), "+v"(o4[3]));
    const unsigned n = __builtin_amdgcn_readfirstlane(nv);
    if (n <= 64 && wid >= (isgu ? 4 : 1)) {
      if (isgu) return;
    } else if (__builtin_expect(n <= 32, 1)) {
      unsigned ba = 0, bb = 0;
#pragma unroll
      for (int e = 0; e < 4; ++e) {
        ba = __builtin_amdgcn_alignbit(ba, c - o4[e][0], 31); bb = __builtin_amdgcn_alignbit(bb, c - o4[e][1], 31);
        ba = __builtin_amdgcn_alignbit(ba, c - o4[e][2], 31); bb = __builtin_amdgcn_alignbit(bb, c - o4[e][3], 31);
      }
      unsigned rk = __builtin_popcount(ba) + __builtin_popcount(bb);
      auto r2 = __builtin_amdgcn_permlane32_swap(rk, rk, false, false);
      rk = r2[0] + r2[1];
      if (rk < 8) sel[rk] = c;
    } else if (n <= 64) {
      const unsigned c2 = cand[32 + t];
      unsigned ba = 0, bb = 0;
#pragma unroll
      for (int e = 0; e < 8; ++e) {
        const u32x4 v = *(LAS u32x4*)(cand + h * 32 + e * 4);
#pragma unroll
        for (int j = 0; j < 4; ++j) { ba = __builtin_amdgcn_alignbit(ba, c - v[j], 31); bb = __builtin_amdgcn_alignbit(bb, c2 - v[j], 31); }
      }
      unsigned ra = __builtin_popcount(ba), rb2 = __builtin_popcount(bb);
      auto r1 = __builtin_amdgcn_permlane32_swap(ra, ra, false, false);
      auto r2 = __builtin_amdgcn_permlane32_swap(rb2, rb2, false, false);
      ra = r1[0] + r1[1]; rb2 = r2[0] + r2[1];
      if (ra < 8) sel[ra] = c;
      if (rb2 < 8) sel[rb2] = c2;
    } else {
      LAS unsigned* all = cand + 256;
      *(LAS u32x4*)(all + tid * 4) = k4;
      LDS_BAR();
      unsigned rk[4] = {0, 0, 0, 0};
#pragma unroll 1
      for (int j = 0; j < 2048; ++j) {
        const unsigned v = all[j];
#pragma unroll
        for (int e = 0; e < 4; ++e) rk[e] += v > k4[e];
      }
#pragma unroll
      for (int e = 0; e < 4; ++e) if (rk[e] < 8) cand[160 + rk[e]] = k4[e];
      LDS_BAR();
      sel = cand + 160;
      if (isgu && wid >= 4) return;
    }
  }
  const int tok = lane & 15;
  const unsigned key = sel[tok & 7];
  const int trow = 2047 - (int)(key & 2047u);
  LAS f32x4* red = (LAS f32x4*)(lds + L_WO);
  if (isgu) {
    u32x4 xb[4];
#pragma unroll 1
    for (unsigned spins = 0; spins < SPIN_CAP; ++spins) {
      FENCE();
#pragma unroll
      for (int u = 0; u < 4; ++u) xb[u] = LD128(trow * 1024 + u * 64 + g * 16, WS_HN + wid * 256);
      if (__all(max(max(umax4(xb[0]), umax4(xb[1])), max(umax4(xb[2]), umax4(xb[3]))) != SENT)) break;
    }
    f32x4 ag = f32x4{}, au = f32x4{};
#pragma unroll
    for (int u = 0; u < 4; ++u) { ag = mfma16(fw[u], xb[u], ag); au = mfma16(fw[4 + u], xb[u], au); }
    const f32x4 mine = tok < 8 ? ag : au;
    if (wid != 0) red[wid * 64 + lane] = mine;
    LDS_BAR();
    if (wid == 0) {
      const f32x4 s = (mine + red[64 + lane]) + (red[128 + lane] + red[192 + lane]);
      float av[4];
#pragma unroll
      for (int r = 0; r < 4; ++r) {
        const float gu = s[r] * dppf<0x128>(s[r]);
        av[r] = gu * __builtin_amdgcn_rcpf(1.f + __builtin_amdgcn_exp2f(s[r] * -1.4426950408889634f));
      }
      if (tok < 8) ST64((u32x2{nosent(pkbf(av[0], av[1])), nosent(pkbf(av[2], av[3]))}), tok * 32 + g * 8, WS_ACT + b * 256);
    }
    return;
  }
  const int nb = b - DN0;
  u32x2 o0 = u32x2{0, 0};
  float sg = 0.f;
  if (wid == 0) {
#pragma unroll 1
    for (unsigned spins = 0; spins < SPIN_CAP; ++spins) {
      FENCE();
      o0 = __builtin_amdgcn_raw_buffer_load_b64(r_out, trow * 1024 + g * 8, nb * 32, SC1);
      if (__all(max(o0[0], o0[1]) != SENT)) break;
    }
    const unsigned ov = ((key & 0x7FFFF800u) << 1) | 0x800u;
    const float r = rbf(__uint_as_float((ov & 0x80000000u) ? (ov & 0x7FFFFFFFu) : ~ov));
    sg = rbf(__builtin_amdgcn_rcpf(1.f + __expf(-r)));
  }
  const __amdgpu_buffer_rsrc_t r_act = mkrsrc(ws + WS_ACT, 22016);
  u32x4 ab[6];
  const int aoff = wid * 512 + (g >> 1) * 256 + (tok & 7) * 32 + (g & 1) * 16;
#pragma unroll
  for (int p = 0; p < 6; ++p) ab[p] = __builtin_amdgcn_raw_buffer_load_b128(r_act, aoff, p * 4096, SC1);
#pragma unroll 1
  for (unsigned spins = 0; spins < SPIN_CAP; ++spins) {
    bool miss[6], any = false;
#pragma unroll
    for (int p = 0; p < 6; ++p) { miss[p] = __any(umax4(ab[p]) == SENT); any |= miss[p]; }
    if (!any) break;
    FENCE();
#pragma unroll
    for (int p = 0; p < 6; ++p) if (miss[p]) ab[p] = __builtin_amdgcn_raw_buffer_load_b128(r_act, aoff, p * 4096, SC1);
  }
  f32x4 ay = f32x4{};
#pragma unroll
  for (int p = 0; p < 6; ++p) ay = mfma16(fw[p], ab[p], ay);
  red[wid * 64 + lane] = ay;
  LDS_BAR();
  if (wid == 0 && tok < 8) {
    ay = ((ay + red[64 + lane]) + (red[128 + lane] + red[192 + lane])) + ((red[256 + lane] + red[320 + lane]) + (red[384 + lane] + red[448 + lane]));
    __builtin_amdgcn_raw_buffer_store_b64(u32x2{pkbf(fmaf(ay[0], sg, bflo(o0[0])), fmaf(ay[1], sg, bfhi(o0[0]))), pkbf(fmaf(ay[2], sg, bflo(o0[1])), fmaf(ay[3], sg, bfhi(o0[1])))}, r_out,
                                          trow * 1024 + g * 8, nb * 32, SC1);
  }
}

extern "C" void kernel_launch(void* const* d_in, const int* in_sizes, int n_in, void* d_out, int out_size, void* d_ws, size_t ws_size,
                              hipStream_t stream) {
  (void)hipFuncSetAttribute((const void*)fused_block, hipFuncAttributeMaxDynamicSharedMemorySize, LDS_BYTES);
  fused_block<<<256, 576, LDS_BYTES, stream>>>((const u16*)d_in[0], (const u16*)d_in[1], (const u16*)d_in[2], (const u16*)d_in[4],
                                               (const u16*)d_in[5], (const u16*)d_in[6], (const u16*)d_in[7], (char*)d_ws, (u16*)d_out);
}
```
